```python
import torch

def _softmax(x, axis=-1):
    return torch.softmax(x, dim=axis)
def _gelu(x):
    return torch.nn.functional.gelu(x, approximate="tanh")
def _silu(x):
    return torch.nn.functional.silu(x)
def _sigmoid(x):
    return torch.sigmoid(x)
def _layernorm(x, axis=-1, eps=1e-5):
    m = x.mean(dim=axis, keepdim=True)
    v = x.var(dim=axis, keepdim=True, unbiased=False)
    return (x - m) / torch.sqrt(v + eps)
def _scatter_add(tgt, idx, src, axis=0):
    return tgt.clone().index_add_(axis, idx, src)
def _segment_sum(src, seg, n):
    out = torch.zeros((n,) + src.shape[1:], dtype=src.dtype)
    return out.index_add_(0, seg, src)
def _pad(x, pw):
    flat = tuple(v for pair in reversed(pw) for v in pair)
    return torch.nn.functional.pad(x, flat)

kernel_name = 'gated_mix__sliding_local_dilated__softmax_full_mha__prompt'

def setup_inputs():

    _w = (lambda a: (lambda t: t.to(torch.bfloat16) if t.is_floating_point() else t)(torch.as_tensor(a)))
    def _A_setup():
        B, S, D = 1, 2048, 640
        return {
            "x": _w((lambda *s: torch.randn(*s, dtype=torch.bfloat16) * 0.05)(B, S, D)),
            "wqkv": _w((lambda *s: torch.randn(*s, dtype=torch.bfloat16) * 1.6)(D, 3 * D)),
            "wo": _w((lambda *s: torch.randn(*s, dtype=torch.bfloat16) * 0.05)(D, D)),
        }
    _A_in = _A_setup()

    def _B_setup():
        B, S, D = 1, 2048, 640
        return {
            "x": _w((lambda *s: torch.randn(*s, dtype=torch.bfloat16) * 0.05)(B, S, D)),
            "wqkv": _w((lambda *s: torch.randn(*s, dtype=torch.bfloat16) * 1.6)(D, 3 * D)),
            "wo": _w((lambda *s: torch.randn(*s, dtype=torch.bfloat16) * 0.1)(D, D)),
        }
    _B_in = _B_setup()

    return {
        "A_x": _A_in["x"],
        "A_wqkv": _A_in["wqkv"],
        "A_wo": _A_in["wo"],
        "B_x": _B_in["x"],
        "B_wqkv": _B_in["wqkv"],
        "B_wo": _B_in["wo"],
        "gate_w": _w((lambda *s: torch.randn(*s, dtype=torch.bfloat16) * 0.05)(640) * 16.0),
    }

def reference(A_x, A_wqkv, A_wo, B_x, B_wqkv, B_wo, gate_w):
    def _A_ref(x, wqkv, wo):
        B, S, D = x.shape
        H = 32; Dh = D // H
        W, dil = 8, 2
        qkv = (lambda x, *d: x.permute(*d))((x @ wqkv).reshape(B, S, 3, H, Dh), 2, 0, 3, 1, 4)
        q, k, v = qkv[0], qkv[1], qkv[2]
        scale = Dh ** -0.5
        pos = torch.arange(S)
        off = torch.arange(W) * dil
        idx = pos[:, None] - off[None, :]
        mask = idx >= 0
        idx_c = torch.where(mask, idx, 0).reshape(-1)
        kg = k[:, :, idx_c, :].reshape(B, H, S, W, Dh)
        vg = v[:, :, idx_c, :].reshape(B, H, S, W, Dh)
        logits = torch.einsum("bhsd,bhswd->bhsw", q, kg) * scale
        logits = torch.where(mask[None, None, :, :], logits, -1e9)
        attn = _softmax(logits, axis=-1)
        o = torch.einsum("bhsw,bhswd->bhsd", attn, vg)
        return (lambda x, *d: x.permute(*d))(o, 0, 2, 1, 3).reshape(B, S, D) @ wo

    def _B_ref(x, wqkv, wo):
        B, S, D = x.shape
        H = 32; Dh = D // H
        qkv = (lambda x, *d: x.permute(*d))((x @ wqkv).reshape(B, S, 3, H, Dh), 2, 0, 3, 1, 4)
        q, k, v = qkv[0], qkv[1], qkv[2]
        scale = Dh ** -0.5
        scores = torch.einsum("bhqd,bhkd->bhqk", q, k) * scale
        p = _softmax(scores, axis=-1)
        o = torch.einsum("bhqk,bhkd->bhqd", p, v)
        return (lambda x, *d: x.permute(*d))(o, 0, 2, 1, 3).reshape(B, S, D) @ wo

    x = A_x
    g = _sigmoid((x * gate_w).sum(-1, keepdims=True))
    a = _A_ref(x, A_wqkv, A_wo)
    b = _B_ref(x, B_wqkv, B_wo)
    return g * a + (1 - g) * b
```

```cpp
#include <hip/hip_runtime.h>
#include <hip/hip_bf16.h>
typedef unsigned short u16;
__device__ __forceinline__ float bf2f(u16 x){ return __uint_as_float((unsigned)x<<16); }
__device__ __forceinline__ u16 f2bf(float f){ unsigned u=__float_as_uint(f); u+=0x7fff+((u>>16)&1); return u>>16; }
__global__ void k_qkv(const u16* x, const u16* wa, const u16* wb, float* qkv) {
  int n = blockIdx.x*256+threadIdx.x; int t0 = blockIdx.y*8;
  const u16* w = n<1920? wa+n : wb+(n-1920);
  float acc[8]={};
  for (int k=0;k<640;++k){ float wv=bf2f(w[(size_t)k*1920]); for(int i=0;i<8;++i) acc[i]+=bf2f(x[(t0+i)*640+k])*wv; }
  for(int i=0;i<8;++i) qkv[((size_t)(n/1920)*2048+t0+i)*1920+n%1920]=bf2f(f2bf(acc[i]));
}
__global__ void k_att(const float* qkv, float* o) {
  int t = blockIdx.x*64+threadIdx.x, h=blockIdx.y, br=blockIdx.z;
  const float* base=qkv+(size_t)br*2048*1920; float q[20], acc[20];
  for(int d=0;d<20;++d){ q[d]=base[t*1920+h*20+d]*0.22360679775f; acc[d]=0; }
  float m=-1e30f,l=0; int n = br==0?8:2048;
  for(int i=0;i<n;++i){ int key = br==0? t-2*i : i; if(key<0) break; float s=0; for(int d=0;d<20;++d) s+=q[d]*base[key*1920+640+h*20+d];
    float mn=fmaxf(m,s), a=__expf(m-mn), p=__expf(s-mn); l=l*a+p; for(int d=0;d<20;++d) acc[d]=acc[d]*a+p*base[key*1920+1280+h*20+d]; m=mn; }
  for(int d=0;d<20;++d) o[((size_t)br*2048+t)*640+h*20+d]=bf2f(f2bf(acc[d]/l));
}
__global__ void k_out(const float* o, const u16* x, const u16* gw, const u16* woa, const u16* wob, u16* out){
  int n=threadIdx.x, t=blockIdx.x; float g=0; for(int k=0;k<640;++k) g+=bf2f(x[t*640+k])*bf2f(gw[k]); g=1.f/(1.f+__expf(-g));
  float a=0,b=0; for(int k=0;k<640;++k){ a+=o[t*640+k]*bf2f(woa[k*640+n]); b+=o[(2048+t)*640+k]*bf2f(wob[k*640+n]); }
  out[t*640+n]=f2bf(g*a+(1-g)*b);
}
extern "C" void kernel_launch(void* const* d_in, const int* in_sizes, int n_in, void* d_out, int out_size, void* d_ws, size_t ws_size, hipStream_t stream) {
  float* qkv=(float*)d_ws; float* o=qkv+2*2048*1920;
  k_qkv<<<dim3(15,256),256,0,stream>>>((u16*)d_in[0],(u16*)d_in[1],(u16*)d_in[4],qkv);
  k_att<<<dim3(32,32,2),64,0,stream>>>(qkv,o);
  k_out<<<2048,640,0,stream>>>(o,(u16*)d_in[0],(u16*)d_in[6],(u16*)d_in[2],(u16*)d_in[5],(u16*)d_out);
}
```

```cpp
#include <hip/hip_runtime.h>
#include <hip/hip_bf16.h>

typedef unsigned short u16;
typedef unsigned u32;
typedef __bf16 bf16x8 __attribute__((ext_vector_type(8)));
typedef __bf16 bf16x16 __attribute__((ext_vector_type(16)));
typedef __bf16 bf16x2 __attribute__((ext_vector_type(2)));
typedef short s16x4 __attribute__((ext_vector_type(4)));
typedef short s16x8 __attribute__((ext_vector_type(8)));
typedef float f32x2 __attribute__((ext_vector_type(2)));
typedef float f32x4 __attribute__((ext_vector_type(4)));
typedef float f32x8 __attribute__((ext_vector_type(8)));
typedef float f32x16 __attribute__((ext_vector_type(16)));
typedef u32 u32x2 __attribute__((ext_vector_type(2)));
typedef u32 u32x4 __attribute__((ext_vector_type(4)));
#define LAS __attribute__((address_space(3)))
#define GAS __attribute__((address_space(1)))
#define DI __device__ __forceinline__
#define BC(T, x) __builtin_bit_cast(T, x)
#define RLX_AGENT __ATOMIC_RELAXED, __HIP_MEMORY_SCOPE_AGENT

#define PROF 0

constexpr int SEQ = 2048, DM = 640, NH = 32;
constexpr float QSCALE = 0.22360679774997896f * 1.4426950408889634f;
constexpr u32 EPOCH_STEP = 0x9E3779B9u;
constexpr int TILE = 2624;
constexpr int SLOT = 8 * TILE;
constexpr int QT = 1280;
constexpr int STG = 51200, STG_W = 32768, WIMG = 9216, L_GW = 162560;
constexpr int L_KVB = 3 * SLOT, L_KVA = 4 * SLOT, L_QA = 5 * SLOT, L_QB = L_QA + 8 * QT, L_G = L_QB + 8 * QT, L_HALO = L_G + 1024, L_CMB = L_HALO + 2 * TILE;
constexpr int LDS_BYTES = 163840;
constexpr size_t W_EPOCH = 0;
constexpr size_t W_FLAG = 8192;
constexpr size_t W_KV = 65536;
constexpr size_t W_HALO = W_KV + (size_t)256 * SLOT;
constexpr size_t W_O = W_HALO + (size_t)512 * TILE;
constexpr size_t W_PROF = W_O + (size_t)512 * 10240;

DI u32 cvtpk(float lo, float hi) { f32x2 v = {lo, hi}; return BC(u32, __builtin_convertvector(v, bf16x2)); }
DI float bflo(u32 p) { return BC(float, p << 16); }
DI float bfhi(u32 p) { return BC(float, p & 0xffff0000u); }
DI f32x4 mfma16(u32x4 a, u32x4 b, f32x4 c) { return __builtin_amdgcn_mfma_f32_16x16x32_bf16(BC(bf16x8, a), BC(bf16x8, b), c, 0, 0, 0); }
DI f32x16 mfma32(u32x4 a, u32x4 b, f32x16 c) { return __builtin_amdgcn_mfma_f32_32x32x16_bf16(BC(bf16x8, a), BC(bf16x8, b), c, 0, 0, 0); }
DI f32x16 mfma32k8(u32x2 a, u32x2 b, f32x16 c) { return __builtin_amdgcn_mfma_f32_32x32x8bf16_1k(BC(s16x4, a), BC(s16x4, b), c, 0, 0, 0); }
DI f32x16 pv(u32x4 v, f32x8 p, f32x16 o) { return __builtin_amdgcn_smfmac_f32_32x32x32_bf16(BC(bf16x8, v), BC(bf16x16, p), o, (int)0xDDDDDDDDu, 0, 0); }
DI f32x8 lo8(f32x16 s) { return __builtin_shufflevector(s, s, 0, 1, 2, 3, 4, 5, 6, 7); }
DI f32x8 hi8(f32x16 s) { return __builtin_shufflevector(s, s, 8, 9, 10, 11, 12, 13, 14, 15); }
DI u32x4 lds128(u32 a) { return *(const LAS u32x4*)(size_t)a; }
DI u32x2 lds64(u32 a) { return *(const LAS u32x2*)(size_t)a; }
DI u32x2 ldstr(u32 a) { return BC(u32x2, __builtin_amdgcn_ds_read_tr16_b64_v4i16((LAS s16x4*)(size_t)a)); }
DI void sts128(u32 a, u32x4 v) { *(LAS u32x4*)(size_t)a = v; }
DI void sts64(u32 a, u32x2 v) { *(LAS u32x2*)(size_t)a = v; }
DI void sts16(u32 a, u32 v) { *(LAS u16*)(size_t)a = (u16)v; }
DI float swap_max(float m) { auto r = __builtin_amdgcn_permlane32_swap(BC(u32, m), BC(u32, m), false, false); return fmaxf(__uint_as_float(r[0]), __uint_as_float(r[1])); }
DI float from_hi_half(float x) { auto r = __builtin_amdgcn_permlane32_swap(BC(u32, x), BC(u32, x), false, false); return __uint_as_float(r[1]); }
DI float max16(f32x16 s) {
    float a = fmaxf(fmaxf(s[0], s[1]), s[2]), b = fmaxf(fmaxf(s[3], s[4]), s[5]);
    a = fmaxf(fmaxf(a, s[6]), s[7]); b = fmaxf(fmaxf(b, s[8]), s[9]); a = fmaxf(fmaxf(a, s[10]), s[11]);
    b = fmaxf(fmaxf(b, s[12]), s[13]); a = fmaxf(fmaxf(a, s[14]), s[15]); return fmaxf(a, b); }
#define BAR_VM(N) asm volatile("s_waitcnt vmcnt(" #N ") lgkmcnt(0)\n\ts_barrier" ::: "memory")
#define BAR_L() asm volatile("s_waitcnt lgkmcnt(0)\n\ts_barrier" ::: "memory")
#define SB() __builtin_amdgcn_sched_barrier(0)
DI void st128(const char* base, u32 off, u32x4 v) { asm volatile("global_store_dwordx4 %0, %1, %2 sc1\n\ts_nop 1" :: "v"(off), "v"(v), "s"(base) : "memory"); }
DI void st64(const char* base, u32 off, u32x2 v) { asm volatile("global_store_dwordx2 %0, %1, %2 sc1" :: "v"(off), "v"(v), "s"(base) : "memory"); }
DI void dma_tile(const char* src, u32 dst, u32 l16) {
    asm volatile("s_mov_b32 m0, %2\n\ts_nop 0\n\tglobal_load_lds_dwordx4 %0, %1 sc1\n\tglobal_load_lds_dwordx4 %0, %1 offset:1024 sc1\n\t"
                 "s_mov_b32 exec_hi, 15\n\tglobal_load_lds_dwordx4 %0, %1 offset:2048 sc1\n\ts_mov_b32 exec_hi, -1" :: "v"(l16), "s"(src), "s"(dst) : "memory"); }
DI void copy_tile_out(const char* dst, u32 src, int lane) {
    const u32 l16 = lane * 16;
    const u32x4 a = lds128(src + l16), b = lds128(src + l16 + 1024), c = lds128(src + l16 + 2048);
    st128(dst, l16, a); st128(dst, l16 + 1024, b); if (lane < 36) st128(dst, l16 + 2048, c); }
#define STAMP(i)

DI void store_o(f32x16 o, float gate, const char* dst, int pos, int hi) {
    const float sc = gate * __builtin_amdgcn_rcpf(from_hi_half(o[8]));
    const u32x4 c = {cvtpk(o[0] * sc, o[1] * sc), cvtpk(o[2] * sc, o[3] * sc), cvtpk(o[4] * sc, o[5] * sc), cvtpk(o[6] * sc, o[7] * sc)};
    const u32x2 d = {cvtpk(o[8] * sc, o[9] * sc), cvtpk(o[10] * sc, o[11] * sc)};
    st128(dst, hi * 4096 + pos * 16, c);
    if (!hi) st64(dst, 8192 + pos * 8, d);
}

struct Frag { u32x4 x[4], w[4], g; };

__global__ __launch_bounds__(512, 2) void k_qkv_att(const char* __restrict__ x, const char* __restrict__ wA, const char* __restrict__ wB,
                                                    const char* __restrict__ gw, char* __restrict__ ws) {
    const int tid = threadIdx.x, lane = tid & 63, w = __builtin_amdgcn_readfirstlane(tid >> 6);
    const int xcd = blockIdx.x & 7, bi = blockIdx.x >> 3;
    const int h = xcd * 4 + (bi & 3), j = bi >> 2;
    const int u = w & 3, half = w >> 2;
    STAMP(0);
    u32 xo[4], wo[3];
    {
        const int p = 32 * w + (lane >> 3), tok = 256 * j + 2 * (p & 127) + (p >> 7);
#pragma unroll
        for (int i = 0; i < 4; ++i) xo[i] = (tok + 16 * i) * 1280 + (((lane & 7) ^ ((4 * i + (lane >> 4)) & 7)) << 4) - i * 1024;
#pragma unroll
        for (int i = 0; i < 3; ++i) {
            const int ci = (i == 2 ? 8 : 2 * u + i) * 64 + lane, rho = ci / 9, cc = ci - rho * 9, part = cc / 3, c3 = cc - part * 3;
            const int k = (rho & 48) + 8 * ((rho >> 3) & 1) + 4 * (rho & 1) + ((rho >> 1) & 3);
            wo[i] = k * 3840 + part * 1280 + 40 * h - 8 * (h & 1) + c3 * 16 - (i == 1 ? 1024 : 0);
        }
    }
    const char* xs = x; const char* wsrc = half ? wA : wB;
    auto stage = [&](u32 slot) {
        asm volatile("s_mov_b32 m0, %5\n\ts_nop 0\n\tglobal_load_lds_dwordx4 %0, %4\n\tglobal_load_lds_dwordx4 %1, %4 offset:1024\n\t"
                     "global_load_lds_dwordx4 %2, %4 offset:2048\n\tglobal_load_lds_dwordx4 %3, %4 offset:3072"
                     :: "v"(xo[0]), "v"(xo[1]), "v"(xo[2]), "v"(xo[3]), "s"(xs), "s"(slot + w * 4096) : "memory");
        asm volatile("s_mov_b32 m0, %3\n\ts_nop 0\n\tglobal_load_lds_dwordx4 %0, %2\n\tglobal_load_lds_dwordx4 %1, %2 offset:1024"
                     :: "v"(wo[0]), "v"(wo[1]), "s"(wsrc), "s"(slot + STG_W + half * WIMG + u * 2048) : "memory");
        if (u == 0) asm volatile("s_mov_b32 m0, %2\n\ts_nop 0\n\tglobal_load_lds_dwordx4 %0, %1"
                     :: "v"(wo[2]), "s"(wsrc), "s"(slot + STG_W + half * WIMG + 8192) : "memory");
        xs += 128; wsrc += 64 * 3840;
    };
    stage(0);
    if (w == 1 || w == 2) {
        if (lane < 40) asm volatile("s_mov_b32 m0, %2\n\ts_nop 0\n\tglobal_load_lds_dwordx4 %0, %1" :: "v"(lane * 16), "s"(gw + (w - 1) * 640), "s"(L_GW + (w - 1) * 640) : "memory");
    }
    const u32 epoch = __hip_atomic_load((const GAS u32*)(ws + W_EPOCH + h * 256), RLX_AGENT) + EPOCH_STEP;
    stage(STG); stage(2 * STG);

    const int l15 = lane & 15, g = lane >> 4;
    u32 xa0 = (64 * u + l15) * 128 + ((g ^ (l15 >> 1)) << 4);
    u32 wa0 = STG_W + half * WIMG + (16 * (g >> 1) + 8 * (g & 1) + 2 * ((lane >> 2) & 3)) * 144 + 8 * (h & 1);
    u32 wa2 = wa0 + (lane & 3) * 48 + 32; wa0 += (lane & 3) * 8;
    u32 ga = L_GW + g * 16;
    const bool isg = l15 == 12;
    f32x4 acc[4][4] = {};
    auto rd = [&](Frag& f, int ks) {
        const u32 xa = xa0 ^ (ks * 64), o = ks * 32 * 144;
#pragma unroll
        for (int t = 0; t < 4; ++t) {
            const u32 a = t == 2 ? wa2 + o : wa0 + o + (t == 3 ? 96 : t * 48);
            const u32x2 lo = ldstr(a), hi = ldstr(a + 144);
            f.w[t] = u32x4{lo[0], lo[1], hi[0], hi[1]};
            f.x[t] = lds128(xa + t * 2048);
        }
        f.g = lds128(ga + ks * 64);
    };
    auto mm = [&](const Frag& f) {
        u32x4 w2;
#pragma unroll
        for (int e = 0; e < 4; ++e) w2[e] = isg ? f.g[e] : f.w[2][e];
#pragma unroll
        for (int i = 0; i < 4; ++i) acc[0][i] = mfma16(f.w[0], f.x[i], acc[0][i]);
#pragma unroll
        for (int i = 0; i < 4; ++i) acc[1][i] = mfma16(f.w[1], f.x[i], acc[1][i]);
#pragma unroll
        for (int i = 0; i < 4; ++i) acc[3][i] = mfma16(f.x[i], f.w[3], acc[3][i]);
#pragma unroll
        for (int i = 0; i < 4; ++i) acc[2][i] = mfma16(w2, f.x[i], acc[2][i]);
    };
    Frag f0, f1;
    BAR_VM(12);
    STAMP(1);
    rd(f0, 0);
    u32 slot = 0;
#pragma unroll 1
    for (int s = 0; s < 9; ++s) {
        rd(f1, 1); SB();
        mm(f0); SB();
        if (s < 8) BAR_VM(6); else BAR_VM(0);
        if (s < 7) stage(slot);
        const int d = slot == 2 * STG ? -2 * STG : STG;
        slot += d; xa0 += d; wa0 += d; wa2 += d; ga += 128;
        rd(f0, 0); SB();
        mm(f1); SB();
    }
    rd(f1, 1); SB();
    mm(f0); mm(f1);
    STAMP(2);
    {
        const u32 qi = half ? L_QA : L_QB, kv = half ? L_KVA : L_KVB;
#pragma unroll
        for (int i = 0; i < 4; ++i) {
            const int t32 = 2 * u + (i >> 1), p32 = 16 * (i & 1) + l15;
            const u32 fo = (g >> 1) * 512 + p32 * 16 + (g & 1) * 8;
            const u32 q01 = cvtpk(acc[0][i][0], acc[0][i][1]), q23 = cvtpk(acc[0][i][2], acc[0][i][3]);
            sts64(qi + t32 * QT + fo, u32x2{cvtpk(bflo(q01) * QSCALE, bfhi(q01) * QSCALE), cvtpk(bflo(q23) * QSCALE, bfhi(q23) * QSCALE)});
            sts64(kv + t32 * TILE + fo, u32x2{cvtpk(acc[1][i][0], acc[1][i][1]), cvtpk(acc[1][i][2], acc[1][i][3])});
            const u32 vb = kv + t32 * TILE + 1280 + (i & 1) * 672;
            sts64(vb + ((g >> 1) * 20 + l15) * 16 + (g & 1) * 8, u32x2{cvtpk(acc[3][i][0], acc[3][i][1]), cvtpk(acc[3][i][2], acc[3][i][3])});
            u32 a = cvtpk(acc[2][i][0], acc[2][i][1]), b = cvtpk(acc[2][i][2], acc[2][i][3]);
            if (g == 2) {
                const u32 va = vb + ((l15 >> 3) * 20 + 16) * 16 + (l15 & 7) * 2;
                sts16(va, a); sts16(va + 16, a >> 16); sts16(va + 32, b); sts16(va + 48, b >> 16);
            } else if (g == 3) {
                if (!half) *(LAS float*)(size_t)(L_G + (64 * u + 16 * i + l15) * 4) = acc[2][i][0];
                if (l15 < 2) sts128(vb + 640 + l15 * 16, l15 ? u32x4{0, 0, 0, 0} : u32x4{0x3f803f80u, 0x3f803f80u, 0x3f803f80u, 0x3f803f80u});
            } else {
                if (g == 0) { a = cvtpk(bflo(a) * QSCALE, bfhi(a) * QSCALE); b = cvtpk(bflo(b) * QSCALE, bfhi(b) * QSCALE); }
                sts64((g == 0 ? qi + t32 * QT : kv + t32 * TILE) + 1024 + p32 * 8, u32x2{a, b});
            }
        }
    }
    BAR_L();
    STAMP(3);
    const char* kvx = ws + W_KV + (size_t)h * 8 * SLOT + w * TILE;
    copy_tile_out(kvx + j * SLOT, L_KVB + w * TILE, lane);
    if (u == 3) copy_tile_out(ws + W_HALO + (size_t)((h * 8 + j) * 2 + half) * TILE, L_KVA + w * TILE, lane);

    const int l31 = lane & 31, hi = lane >> 5;
    const u32 k2o = hi ? 1920 : 1024 + l31 * 8;
    const u32 vo = 1280 + (l31 < 20 ? hi * 320 + l31 * 16 : (l31 == 20 ? 640 : 656));
    u32x4 qa1 = lds128(L_QB + (2 * u) * QT + lane * 16), qb1 = lds128(L_QB + (2 * u + 1) * QT + lane * 16);
    u32x2 qa2 = lds64(L_QB + (2 * u) * QT + 1024 + l31 * 8), qb2 = lds64(L_QB + (2 * u + 1) * QT + 1024 + l31 * 8);
    const float glogB = *(const LAS float*)(size_t)(L_G + (64 * u + 32 * half + l31) * 4);
    const float glogA = *(const LAS float*)(size_t)(L_G + (32 * w + l31) * 4);
    if (hi) { qa2 = u32x2{0, 0}; qb2 = u32x2{0, 0}; }
    {
        const u32x4 k1 = lds128(L_KVB + lane * 16); const u32x2 k2 = lds64(L_KVB + k2o);
        const f32x16 z = {};
        const float ma = swap_max(max16(mfma32k8(k2, qa2, mfma32(k1, qa1, z)))), mb = swap_max(max16(mfma32k8(k2, qb2, mfma32(k1, qb1, z))));
        const u32 mh = cvtpk(-ma, -mb), ml = cvtpk(-ma - bflo(mh), -mb - bfhi(mh));
        if (hi) { qa2[0] = (mh & 0xffffu) | (ml << 16); qb2[0] = (mh >> 16) | (ml & 0xffff0000u); }
    }
    f32x16 oa = {}, ob = {}, s3 = {}; u32x4 v0 = {}, v1 = {};
    const u32 kb = half * 4 * TILE;
    {
        u32 ak1 = L_KVB + kb + lane * 16, ak2 = L_KVB + kb + k2o, av = L_KVB + kb + vo;
        asm volatile(
    "ds_read_b128 v[144:147], v45 offset:0\n\t"
    "ds_read_b64 v[148:149], v46 offset:0\n\t"
    "s_waitcnt lgkmcnt(0)\n\t"
    "v_mfma_f32_32x32x16_bf16 v[80:95], v[144:147], v[32:35], 0\n\t"
    "v_mfma_f32_32x32x8_bf16 v[80:95], v[148:149], v[40:41], v[80:95]\n\t"
    "s_nop 7\n\t"
    "s_nop 3\n\t"
    "v_mfma_f32_32x32x16_bf16 v[96:111], v[144:147], v[36:39], 0\n\t"
    "ds_read_b128 v[144:147], v45 offset:2624\n\t"
    "v_exp_f32_e32 v80, v80\n\t"
    "v_exp_f32_e32 v81, v81\n\t"
    "v_exp_f32_e32 v82, v82\n\t"
    "v_exp_f32_e32 v83, v83\n\t"
    "v_mfma_f32_32x32x8_bf16 v[96:111], v[148:149], v[42:43], v[96:111]\n\t"
    "ds_read_b64 v[148:149], v46 offset:2624\n\t"
    "v_exp_f32_e32 v84, v84\n\t"
    "v_exp_f32_e32 v85, v85\n\t"
    "v_exp_f32_e32 v86, v86\n\t"
    "v_exp_f32_e32 v87, v87\n\t"
    "v_smfmac_f32_32x32x32_bf16 v[64:79], v[152:155], v[128:135], v44\n\t"
    "ds_read_b128 v[152:155], v47 offset:0\n\t"
    "v_exp_f32_e32 v88, v88\n\t"
    "v_exp_f32_e32 v89, v89\n\t"
    "v_exp_f32_e32 v90, v90\n\t"
    "v_exp_f32_e32 v91, v91\n\t"
    "v_smfmac_f32_32x32x32_bf16 v[64:79], v[156:159], v[136:143], v44\n\t"
    "ds_read_b128 v[156:159], v47 offset:672\n\t"
    "v_exp_f32_e32 v92, v92\n\t"
    "v_exp_f32_e32 v93, v93\n\t"
    "v_exp_f32_e32 v94, v94\n\t"
    "v_exp_f32_e32 v95, v95\n\t"
    "s_waitcnt lgkmcnt(2)\n\t"
    "v_mfma_f32_32x32x16_bf16 v[112:127], v[144:147], v[32:35], 0\n\t"
    "v_exp_f32_e32 v96, v96\n\t"
    "v_exp_f32_e32 v97, v97\n\t"
    "v_exp_f32_e32 v98, v98\n\t"
    "v_exp_f32_e32 v99, v99\n\t"
    "v_mfma_f32_32x32x8_bf16 v[112:127], v[148:149], v[40:41], v[112:127]\n\t"
    "v_exp_f32_e32 v100, v100\n\t"
    "v_exp_f32_e32 v101, v101\n\t"
    "v_exp_f32_e32 v102, v102\n\t"
    "v_exp_f32_e32 v103, v103\n\t"
    "s_waitcnt lgkmcnt(0)\n\t"
    "v_smfmac_f32_32x32x32_bf16 v[48:63], v[152:155], v[80:87], v44\n\t"
    "v_exp_f32_e32 v104, v104\n\t"
    "v_exp_f32_e32 v105, v105\n\t"
    "v_exp_f32_e32 v106, v106\n\t"
    "v_exp_f32_e32 v107, v107\n\t"
    "v_smfmac_f32_32x32x32_bf16 v[48:63], v[156:159], v[88:95], v44\n\t"
    "v_exp_f32_e32 v108, v108\n\t"
    "v_exp_f32_e32 v109, v109\n\t"
    "v_exp_f32_e32 v110, v110\n\t"
    "v_exp_f32_e32 v111, v111\n\t"
    "v_mfma_f32_32x32x16_bf16 v[128:143], v[144:147], v[36:39], 0\n\t"
    "ds_read_b128 v[144:147], v45 offset:5248\n\t"
    "v_exp_f32_e32 v112, v112\n\t"
    "v_exp_f32_e32 v113, v113\n\t"
    "v_exp_f32_e32 v114, v114\n\t"
    "v_exp_f32_e32 v115, v115\n\t"
    "v_mfma_f32_32x32x8_bf16 v[128:143], v[148:149], v[42:43], v[128:143]\n\t"
    "ds_read_b64 v[148:149], v46 offset:5248\n\t"
    "v_exp_f32_e32 v116, v116\n\t"
    "v_exp_f32_e32 v117, v117\n\t"
    "v_exp_f32_e32 v118, v118\n\t"
    "v_exp_f32_e32 v119, v119\n\t"
    "v_smfmac_f32_32x32x32_bf16 v[64:79], v[152:155], v[96:103], v44\n\t"
    "ds_read_b128 v[152:155], v47 offset:2624\n\t"
    "v_exp_f32_e32 v120, v120\n\t"
    "v_exp_f32_e32 v121, v121\n\t"
    "v_exp_f32_e32 v122, v122\n\t"
    "v_exp_f32_e32 v123, v123\n\t"
    "v_smfmac_f32_32x32x32_bf16 v[64:79], v[156:159], v[104:111], v44\n\t"
    "ds_read_b128 v[156:159], v47 offset:3296\n\t"
    "v_exp_f32_e32 v124, v124\n\t"
    "v_exp_f32_e32 v125, v125\n\t"
    "v_exp_f32_e32 v126, v126\n\t"
    "v_exp_f32_e32 v127, v127\n\t"
    "s_waitcnt lgkmcnt(2)\n\t"
    "v_mfma_f32_32x32x16_bf16 v[80:95], v[144:147], v[32:35], 0\n\t"
    "v_exp_f32_e32 v128, v128\n\t"
    "v_exp_f32_e32 v129, v129\n\t"
    "v_exp_f32_e32 v130, v130\n\t"
    "v_exp_f32_e32 v131, v131\n\t"
    "v_mfma_f32_32x32x8_bf16 v[80:95], v[148:149], v[40:41], v[80:95]\n\t"
    "v_exp_f32_e32 v132, v132\n\t"
    "v_exp_f32_e32 v133, v133\n\t"
    "v_exp_f32_e32 v134, v134\n\t"
    "v_exp_f32_e32 v135, v135\n\t"
    "s_waitcnt lgkmcnt(0)\n\t"
    "v_smfmac_f32_32x32x32_bf16 v[48:63], v[152:155], v[112:119], v44\n\t"
    "v_exp_f32_e32 v136, v136\n\t"
    "v_exp_f32_e32 v137, v137\n\t"
    "v_exp_f32_e32 v138, v138\n\t"
    "v_exp_f32_e32 v139, v139\n\t"
    "v_smfmac_f32_32x32x32_bf16 v[48:63], v[156:159], v[120:127], v44\n\t"
    "v_exp_f32_e32 v140, v140\n\t"
    "v_exp_f32_e32 v141, v141\n\t"
    "v_exp_f32_e32 v142, v142\n\t"
    "v_exp_f32_e32 v143, v143\n\t"
    "s_waitcnt vmcnt(0)\n\t"
    "s_mov_b64 exec, 1\n\t"
    "global_store_dword %[fz], %[fval], %[fptr] sc1\n\t"
    "s_mov_b64 exec, -1\n\t"
    "v_mfma_f32_32x32x16_bf16 v[96:111], v[144:147], v[36:39], 0\n\t"
    "ds_read_b128 v[144:147], v45 offset:7872\n\t"
    "v_exp_f32_e32 v80, v80\n\t"
    "v_exp_f32_e32 v81, v81\n\t"
    "v_exp_f32_e32 v82, v82\n\t"
    "v_exp_f32_e32 v83, v83\n\t"
    "v_mfma_f32_32x32x8_bf16 v[96:111], v[148:149], v[42:43], v[96:111]\n\t"
    "ds_read_b64 v[148:149], v46 offset:7872\n\t"
    "v_exp_f32_e32 v84, v84\n\t"
    "v_exp_f32_e32 v85, v85\n\t"
    "v_exp_f32_e32 v86, v86\n\t"
    "v_exp_f32_e32 v87, v87\n\t"
    "v_smfmac_f32_32x32x32_bf16 v[64:79], v[152:155], v[128:135], v44\n\t"
    "ds_read_b128 v[152:155], v47 offset:5248\n\t"
    "v_exp_f32_e32 v88, v88\n\t"
    "v_exp_f32_e32 v89, v89\n\t"
    "v_exp_f32_e32 v90, v90\n\t"
    "v_exp_f32_e32 v91, v91\n\t"
    "v_smfmac_f32_32x32x32_bf16 v[64:79], v[156:159], v[136:143], v44\n\t"
    "ds_read_b128 v[156:159], v47 offset:5920\n\t"
    "v_exp_f32_e32 v92, v92\n\t"
    "v_exp_f32_e32 v93, v93\n\t"
    "v_exp_f32_e32 v94, v94\n\t"
    "v_exp_f32_e32 v95, v95\n\t"
    "s_waitcnt lgkmcnt(2)\n\t"
    "v_mfma_f32_32x32x16_bf16 v[112:127], v[144:147], v[32:35], 0\n\t"
    "v_exp_f32_e32 v96, v96\n\t"
    "v_exp_f32_e32 v97, v97\n\t"
    "v_exp_f32_e32 v98, v98\n\t"
    "v_exp_f32_e32 v99, v99\n\t"
    "v_mfma_f32_32x32x8_bf16 v[112:127], v[148:149], v[40:41], v[112:127]\n\t"
    "v_exp_f32_e32 v100, v100\n\t"
    "v_exp_f32_e32 v101, v101\n\t"
    "v_exp_f32_e32 v102, v102\n\t"
    "v_exp_f32_e32 v103, v103\n\t"
    "s_waitcnt lgkmcnt(0)\n\t"
    "v_smfmac_f32_32x32x32_bf16 v[48:63], v[152:155], v[80:87], v44\n\t"
    "v_exp_f32_e32 v104, v104\n\t"
    "v_exp_f32_e32 v105, v105\n\t"
    "v_exp_f32_e32 v106, v106\n\t"
    "v_exp_f32_e32 v107, v107\n\t"
    "v_smfmac_f32_32x32x32_bf16 v[48:63], v[156:159], v[88:95], v44\n\t"
    "v_exp_f32_e32 v108, v108\n\t"
    "v_exp_f32_e32 v109, v109\n\t"
    "v_exp_f32_e32 v110, v110\n\t"
    "v_exp_f32_e32 v111, v111\n\t"
    "v_mfma_f32_32x32x16_bf16 v[128:143], v[144:147], v[36:39], 0\n\t"
    "ds_read_b128 v[144:147], v45 offset:0\n\t"
    "v_exp_f32_e32 v112, v112\n\t"
    "v_exp_f32_e32 v113, v113\n\t"
    "v_exp_f32_e32 v114, v114\n\t"
    "v_exp_f32_e32 v115, v115\n\t"
    "v_mfma_f32_32x32x8_bf16 v[128:143], v[148:149], v[42:43], v[128:143]\n\t"
    "ds_read_b64 v[148:149], v46 offset:0\n\t"
    "v_exp_f32_e32 v116, v116\n\t"
    "v_exp_f32_e32 v117, v117\n\t"
    "v_exp_f32_e32 v118, v118\n\t"
    "v_exp_f32_e32 v119, v119\n\t"
    "v_smfmac_f32_32x32x32_bf16 v[64:79], v[152:155], v[96:103], v44\n\t"
    "ds_read_b128 v[152:155], v47 offset:7872\n\t"
    "v_exp_f32_e32 v120, v120\n\t"
    "v_exp_f32_e32 v121, v121\n\t"
    "v_exp_f32_e32 v122, v122\n\t"
    "v_exp_f32_e32 v123, v123\n\t"
    "v_smfmac_f32_32x32x32_bf16 v[64:79], v[156:159], v[104:111], v44\n\t"
    "ds_read_b128 v[156:159], v47 offset:8544\n\t"
    "v_exp_f32_e32 v124, v124\n\t"
    "v_exp_f32_e32 v125, v125\n\t"
    "v_exp_f32_e32 v126, v126\n\t"
    "v_exp_f32_e32 v127, v127\n\t"
    "s_waitcnt lgkmcnt(2)\n\t"
    "v_mfma_f32_32x32x16_bf16 v[80:95], v[144:147], v[32:35], 0\n\t"
    "v_exp_f32_e32 v128, v128\n\t"
    "v_exp_f32_e32 v129, v129\n\t"
    "v_exp_f32_e32 v130, v130\n\t"
    "v_exp_f32_e32 v131, v131\n\t"
    "v_mfma_f32_32x32x8_bf16 v[80:95], v[148:149], v[40:41], v[80:95]\n\t"
    "v_exp_f32_e32 v132, v132\n\t"
    "v_exp_f32_e32 v133, v133\n\t"
    "v_exp_f32_e32 v134, v134\n\t"
    "v_exp_f32_e32 v135, v135\n\t"
    "s_waitcnt lgkmcnt(0)\n\t"
    "v_smfmac_f32_32x32x32_bf16 v[48:63], v[152:155], v[112:119], v44\n\t"
    "v_exp_f32_e32 v136, v136\n\t"
    "v_exp_f32_e32 v137, v137\n\t"
    "v_exp_f32_e32 v138, v138\n\t"
    "v_exp_f32_e32 v139, v139\n\t"
    "v_smfmac_f32_32x32x32_bf16 v[48:63], v[156:159], v[120:127], v44\n\t"
    "v_exp_f32_e32 v140, v140\n\t"
    "v_exp_f32_e32 v141, v141\n\t"
    "v_exp_f32_e32 v142, v142\n\t"
    "v_exp_f32_e32 v143, v143\n\t"
    "s_nop 7\n\t"
    "s_nop 7\n\t"
        : "+{v[48:63]}"(oa), "+{v[64:79]}"(ob), "+{v[128:143]}"(s3), "+{v[152:155]}"(v0), "+{v[156:159]}"(v1)
        : "{v[32:35]}"(qa1), "{v[36:39]}"(qb1), "{v[40:41]}"(qa2), "{v[42:43]}"(qb2), "{v44}"(0xDDDDDDDDu), "{v45}"(ak1), "{v46}"(ak2), "{v47}"(av),
          [fz] "v"(0), [fval] "v"(epoch), [fptr] "s"(ws + W_FLAG + (h * 64 + j * 8 + w) * 4)
        : "v80","v81","v82","v83","v84","v85","v86","v87","v88","v89","v90","v91","v92","v93","v94","v95","v96","v97","v98","v99","v100","v101","v102","v103","v104","v105","v106","v107","v108","v109","v110","v111","v112","v113","v114","v115","v116","v117","v118","v119","v120","v121","v122","v123","v124","v125","v126","v127","v144","v145","v146","v147","v148","v149", "memory");
    }
    STAMP(4);
    {
        const GAS u32* fl = (const GAS u32*)(ws + W_FLAG + (h * 64 + lane) * 4);
        for (u32 spin = 0; spin < (1u << 20); ++spin) {
            if (__all(__hip_atomic_load(fl, RLX_AGENT) == epoch)) break;
            __builtin_amdgcn_s_sleep(1);
        }
        asm volatile("" ::: "memory");
        if (j == 0 && tid == 0) __hip_atomic_store((GAS u32*)(ws + W_EPOCH + h * 256), epoch, RLX_AGENT);
    }
    STAMP(5);
    if (u == 0 && j > 0) dma_tile(ws + W_HALO + (size_t)((h * 8 + j - 1) * 2 + half) * TILE, L_HALO + half * TILE, lane * 16);
    dma_tile(kvx + ((j + 1) & 7) * SLOT, w * TILE, lane * 16);
    dma_tile(kvx + ((j + 2) & 7) * SLOT, SLOT + w * TILE, lane * 16);
    BAR_VM(3);
    STAMP(6);
    {
        const u32 cur = L_KVA + w * TILE, prv = u ? cur - TILE : (j ? L_HALO + half * TILE : cur);
        const u32x4 q1 = lds128(L_QA + w * QT + lane * 16); u32x2 q2 = lds64(L_QA + w * QT + 1024 + l31 * 8);
        if (hi) q2 = u32x2{0, 0};
        const f32x16 z = {};
        f32x16 sc = mfma32k8(lds64(cur + k2o), q2, mfma32(lds128(cur + lane * 16), q1, z));
        const f32x16 sp = mfma32k8(lds64(prv + k2o), q2, mfma32(lds128(prv + lane * 16), q1, z));
        const int t = l31 - 4 * hi, tp = (u | j) ? t : 64;
        f32x8 pp = {};
#pragma unroll
        for (int r = 0; r < 16; ++r) sc[r] = (u32)(t - ((r & 3) + 8 * (r >> 2))) < 8u ? sc[r] : -INFINITY;
#pragma unroll
        for (int r = 0; r < 4; ++r) pp[4 + r] = tp < r ? sp[12 + r] : -INFINITY;
        const float m = swap_max(fmaxf(max16(sc), fmaxf(fmaxf(pp[4], pp[5]), fmaxf(pp[6], pp[7]))));
#pragma unroll
        for (int r = 0; r < 16; ++r) sc[r] = __builtin_amdgcn_exp2f(sc[r] - m);
#pragma unroll
        for (int r = 4; r < 8; ++r) pp[r] = __builtin_amdgcn_exp2f(pp[r] - m);
        f32x16 o = pv(lds128(cur + vo), lo8(sc), z);
        o = pv(lds128(cur + vo + 672), hi8(sc), o);
        o = pv(lds128(prv + vo + 672), pp, o);
        store_o(o, __builtin_amdgcn_rcpf(1.f + __expf(-glogA)), ws + W_O + (size_t)(j * 32 + h) * 10240, 32 * w + l31, hi);
    }
    STAMP(7);
    {
        u32 ak1 = kb + lane * 16, ak2 = kb + k2o, av = kb + vo;
        u32 cnt = 7, cur = 0, prv = 2 * SLOT, blk = (j + 2) & 7, nxt, dlt;
        asm volatile(
    "ds_read_b128 v[144:147], v45 offset:0\n\t"
    "ds_read_b64 v[148:149], v46 offset:0\n\t"
    "s_waitcnt lgkmcnt(0)\n\t"
    "v_mfma_f32_32x32x16_bf16 v[80:95], v[144:147], v[32:35], 0\n\t"
    "v_mfma_f32_32x32x8_bf16 v[80:95], v[148:149], v[40:41], v[80:95]\n\t"
    "s_nop 7\n\t"
    "s_nop 3\n\t"
    "L_att_%=:\n\t"
    "v_mfma_f32_32x32x16_bf16 v[96:111], v[144:147], v[36:39], 0\n\t"
    "ds_read_b128 v[144:147], v45 offset:2624\n\t"
    "v_exp_f32_e32 v80, v80\n\t"
    "v_exp_f32_e32 v81, v81\n\t"
    "v_exp_f32_e32 v82, v82\n\t"
    "v_exp_f32_e32 v83, v83\n\t"
    "v_mfma_f32_32x32x8_bf16 v[96:111], v[148:149], v[42:43], v[96:111]\n\t"
    "ds_read_b64 v[148:149], v46 offset:2624\n\t"
    "v_exp_f32_e32 v84, v84\n\t"
    "v_exp_f32_e32 v85, v85\n\t"
    "v_exp_f32_e32 v86, v86\n\t"
    "v_exp_f32_e32 v87, v87\n\t"
    "v_smfmac_f32_32x32x32_bf16 v[64:79], v[152:155], v[128:135], v44\n\t"
    "ds_read_b128 v[152:155], v47 offset:0\n\t"
    "v_exp_f32_e32 v88, v88\n\t"
    "v_exp_f32_e32 v89, v89\n\t"
    "v_exp_f32_e32 v90, v90\n\t"
    "v_exp_f32_e32 v91, v91\n\t"
    "v_smfmac_f32_32x32x32_bf16 v[64:79], v[156:159], v[136:143], v44\n\t"
    "ds_read_b128 v[156:159], v47 offset:672\n\t"
    "v_exp_f32_e32 v92, v92\n\t"
    "v_exp_f32_e32 v93, v93\n\t"
    "v_exp_f32_e32 v94, v94\n\t"
    "v_exp_f32_e32 v95, v95\n\t"
    "s_waitcnt lgkmcnt(2)\n\t"
    "v_mfma_f32_32x32x16_bf16 v[112:127], v[144:147], v[32:35], 0\n\t"
    "v_exp_f32_e32 v96, v96\n\t"
    "v_exp_f32_e32 v97, v97\n\t"
    "v_exp_f32_e32 v98, v98\n\t"
    "v_exp_f32_e32 v99, v99\n\t"
    "v_mfma_f32_32x32x8_bf16 v[112:127], v[148:149], v[40:41], v[112:127]\n\t"
    "v_exp_f32_e32 v100, v100\n\t"
    "v_exp_f32_e32 v101, v101\n\t"
    "v_exp_f32_e32 v102, v102\n\t"
    "v_exp_f32_e32 v103, v103\n\t"
    "s_waitcnt lgkmcnt(0)\n\t"
    "v_smfmac_f32_32x32x32_bf16 v[48:63], v[152:155], v[80:87], v44\n\t"
    "v_exp_f32_e32 v104, v104\n\t"
    "v_exp_f32_e32 v105, v105\n\t"
    "v_exp_f32_e32 v106, v106\n\t"
    "v_exp_f32_e32 v107, v107\n\t"
    "v_smfmac_f32_32x32x32_bf16 v[48:63], v[156:159], v[88:95], v44\n\t"
    "v_exp_f32_e32 v108, v108\n\t"
    "v_exp_f32_e32 v109, v109\n\t"
    "v_exp_f32_e32 v110, v110\n\t"
    "v_exp_f32_e32 v111, v111\n\t"
    "v_mfma_f32_32x32x16_bf16 v[128:143], v[144:147], v[36:39], 0\n\t"
    "ds_read_b128 v[144:147], v45 offset:5248\n\t"
    "v_exp_f32_e32 v112, v112\n\t"
    "v_exp_f32_e32 v113, v113\n\t"
    "v_exp_f32_e32 v114, v114\n\t"
    "v_exp_f32_e32 v115, v115\n\t"
    "v_mfma_f32_32x32x8_bf16 v[128:143], v[148:149], v[42:43], v[128:143]\n\t"
    "ds_read_b64 v[148:149], v46 offset:5248\n\t"
    "v_exp_f32_e32 v116, v116\n\t"
    "v_exp_f32_e32 v117, v117\n\t"
    "v_exp_f32_e32 v118, v118\n\t"
    "v_exp_f32_e32 v119, v119\n\t"
    "v_smfmac_f32_32x32x32_bf16 v[64:79], v[152:155], v[96:103], v44\n\t"
    "ds_read_b128 v[152:155], v47 offset:2624\n\t"
    "v_exp_f32_e32 v120, v120\n\t"
    "v_exp_f32_e32 v121, v121\n\t"
    "v_exp_f32_e32 v122, v122\n\t"
    "v_exp_f32_e32 v123, v123\n\t"
    "v_smfmac_f32_32x32x32_bf16 v[64:79], v[156:159], v[104:111], v44\n\t"
    "ds_read_b128 v[156:159], v47 offset:3296\n\t"
    "v_exp_f32_e32 v124, v124\n\t"
    "v_exp_f32_e32 v125, v125\n\t"
    "v_exp_f32_e32 v126, v126\n\t"
    "v_exp_f32_e32 v127, v127\n\t"
    "s_waitcnt lgkmcnt(2)\n\t"
    "v_mfma_f32_32x32x16_bf16 v[80:95], v[144:147], v[32:35], 0\n\t"
    "v_exp_f32_e32 v128, v128\n\t"
    "v_exp_f32_e32 v129, v129\n\t"
    "v_exp_f32_e32 v130, v130\n\t"
    "v_exp_f32_e32 v131, v131\n\t"
    "v_mfma_f32_32x32x8_bf16 v[80:95], v[148:149], v[40:41], v[80:95]\n\t"
    "v_exp_f32_e32 v132, v132\n\t"
    "v_exp_f32_e32 v133, v133\n\t"
    "v_exp_f32_e32 v134, v134\n\t"
    "v_exp_f32_e32 v135, v135\n\t"
    "s_waitcnt lgkmcnt(0)\n\t"
    "v_smfmac_f32_32x32x32_bf16 v[48:63], v[152:155], v[112:119], v44\n\t"
    "v_exp_f32_e32 v136, v136\n\t"
    "v_exp_f32_e32 v137, v137\n\t"
    "v_exp_f32_e32 v138, v138\n\t"
    "v_exp_f32_e32 v139, v139\n\t"
    "v_smfmac_f32_32x32x32_bf16 v[48:63], v[156:159], v[120:127], v44\n\t"
    "v_exp_f32_e32 v140, v140\n\t"
    "v_exp_f32_e32 v141, v141\n\t"
    "v_exp_f32_e32 v142, v142\n\t"
    "v_exp_f32_e32 v143, v143\n\t"
    "v_mfma_f32_32x32x16_bf16 v[96:111], v[144:147], v[36:39], 0\n\t"
    "ds_read_b128 v[144:147], v45 offset:7872\n\t"
    "v_exp_f32_e32 v80, v80\n\t"
    "v_exp_f32_e32 v81, v81\n\t"
    "v_exp_f32_e32 v82, v82\n\t"
    "v_exp_f32_e32 v83, v83\n\t"
    "v_mfma_f32_32x32x8_bf16 v[96:111], v[148:149], v[42:43], v[96:111]\n\t"
    "ds_read_b64 v[148:149], v46 offset:7872\n\t"
    "v_exp_f32_e32 v84, v84\n\t"
    "v_exp_f32_e32 v85, v85\n\t"
    "v_exp_f32_e32 v86, v86\n\t"
    "v_exp_f32_e32 v87, v87\n\t"
    "v_smfmac_f32_32x32x32_bf16 v[64:79], v[152:155], v[128:135], v44\n\t"
    "ds_read_b128 v[152:155], v47 offset:5248\n\t"
    "v_exp_f32_e32 v88, v88\n\t"
    "v_exp_f32_e32 v89, v89\n\t"
    "v_exp_f32_e32 v90, v90\n\t"
    "v_exp_f32_e32 v91, v91\n\t"
    "v_smfmac_f32_32x32x32_bf16 v[64:79], v[156:159], v[136:143], v44\n\t"
    "ds_read_b128 v[156:159], v47 offset:5920\n\t"
    "v_exp_f32_e32 v92, v92\n\t"
    "v_exp_f32_e32 v93, v93\n\t"
    "v_exp_f32_e32 v94, v94\n\t"
    "v_exp_f32_e32 v95, v95\n\t"
    "s_waitcnt lgkmcnt(2)\n\t"
    "v_mfma_f32_32x32x16_bf16 v[112:127], v[144:147], v[32:35], 0\n\t"
    "v_exp_f32_e32 v96, v96\n\t"
    "v_exp_f32_e32 v97, v97\n\t"
    "v_exp_f32_e32 v98, v98\n\t"
    "v_exp_f32_e32 v99, v99\n\t"
    "v_mfma_f32_32x32x8_bf16 v[112:127], v[148:149], v[40:41], v[112:127]\n\t"
    "v_exp_f32_e32 v100, v100\n\t"
    "v_exp_f32_e32 v101, v101\n\t"
    "v_exp_f32_e32 v102, v102\n\t"
    "v_exp_f32_e32 v103, v103\n\t"
    "s_waitcnt lgkmcnt(0)\n\t"
    "v_smfmac_f32_32x32x32_bf16 v[48:63], v[152:155], v[80:87], v44\n\t"
    "v_exp_f32_e32 v104, v104\n\t"
    "v_exp_f32_e32 v105, v105\n\t"
    "v_exp_f32_e32 v106, v106\n\t"
    "v_exp_f32_e32 v107, v107\n\t"
    "v_smfmac_f32_32x32x32_bf16 v[48:63], v[156:159], v[88:95], v44\n\t"
    "v_exp_f32_e32 v108, v108\n\t"
    "v_exp_f32_e32 v109, v109\n\t"
    "v_exp_f32_e32 v110, v110\n\t"
    "v_exp_f32_e32 v111, v111\n\t"
    "s_add_u32 %[nxt], %[cur], 20992\n\t"
    "s_cmp_eq_u32 %[nxt], 62976\n\t"
    "s_cselect_b32 %[nxt], 0, %[nxt]\n\t"
    "s_sub_u32 %[dlt], %[nxt], %[cur]\n\t"
    "s_waitcnt vmcnt(0)\n\t"
    "s_barrier\n\t"
    "v_add_u32_e32 v45, %[dlt], v45\n\t"
    "v_add_u32_e32 v46, %[dlt], v46\n\t"
    "v_mfma_f32_32x32x16_bf16 v[128:143], v[144:147], v[36:39], 0\n\t"
    "ds_read_b128 v[144:147], v45 offset:0\n\t"
    "v_exp_f32_e32 v112, v112\n\t"
    "v_exp_f32_e32 v113, v113\n\t"
    "v_exp_f32_e32 v114, v114\n\t"
    "v_exp_f32_e32 v115, v115\n\t"
    "v_mfma_f32_32x32x8_bf16 v[128:143], v[148:149], v[42:43], v[128:143]\n\t"
    "ds_read_b64 v[148:149], v46 offset:0\n\t"
    "v_exp_f32_e32 v116, v116\n\t"
    "v_exp_f32_e32 v117, v117\n\t"
    "v_exp_f32_e32 v118, v118\n\t"
    "v_exp_f32_e32 v119, v119\n\t"
    "v_smfmac_f32_32x32x32_bf16 v[64:79], v[152:155], v[96:103], v44\n\t"
    "ds_read_b128 v[152:155], v47 offset:7872\n\t"
    "v_exp_f32_e32 v120, v120\n\t"
    "v_exp_f32_e32 v121, v121\n\t"
    "v_exp_f32_e32 v122, v122\n\t"
    "v_exp_f32_e32 v123, v123\n\t"
    "v_smfmac_f32_32x32x32_bf16 v[64:79], v[156:159], v[104:111], v44\n\t"
    "ds_read_b128 v[156:159], v47 offset:8544\n\t"
    "v_exp_f32_e32 v124, v124\n\t"
    "v_exp_f32_e32 v125, v125\n\t"
    "v_exp_f32_e32 v126, v126\n\t"
    "v_exp_f32_e32 v127, v127\n\t"
    "s_waitcnt lgkmcnt(2)\n\t"
    "v_mfma_f32_32x32x16_bf16 v[80:95], v[144:147], v[32:35], 0\n\t"
    "v_add_u32_e32 v47, %[dlt], v47\n\t"
    "s_cmp_lt_u32 %[cnt], 3\n\t"
    "s_cbranch_scc1 L_nodma_%=\n\t"
    "s_add_u32 %[blk], %[blk], 1\n\t"
    "s_and_b32 %[blk], %[blk], 7\n\t"
    "s_mul_i32 %[dlt], %[blk], 20992\n\t"
    "s_add_u32 s40, %[srclo], %[dlt]\n\t"
    "s_addc_u32 s41, %[srchi], 0\n\t"
    "s_add_u32 m0, %[prv], %[wofs]\n\t"
    "s_nop 0\n\t"
    "global_load_lds_dwordx4 v150, s[40:41] sc1\n\t"
    "global_load_lds_dwordx4 v150, s[40:41] offset:1024 sc1\n\t"
    "s_mov_b32 exec_hi, 15\n\t"
    "global_load_lds_dwordx4 v150, s[40:41] offset:2048 sc1\n\t"
    "s_mov_b32 exec_hi, -1\n\t"
    "L_nodma_%=:\n\t"
    "s_mov_b32 %[prv], %[cur]\n\t"
    "s_mov_b32 %[cur], %[nxt]\n\t"
    "v_exp_f32_e32 v128, v128\n\t"
    "v_exp_f32_e32 v129, v129\n\t"
    "v_exp_f32_e32 v130, v130\n\t"
    "v_exp_f32_e32 v131, v131\n\t"
    "v_mfma_f32_32x32x8_bf16 v[80:95], v[148:149], v[40:41], v[80:95]\n\t"
    "v_exp_f32_e32 v132, v132\n\t"
    "v_exp_f32_e32 v133, v133\n\t"
    "v_exp_f32_e32 v134, v134\n\t"
    "v_exp_f32_e32 v135, v135\n\t"
    "s_waitcnt lgkmcnt(0)\n\t"
    "v_smfmac_f32_32x32x32_bf16 v[48:63], v[152:155], v[112:119], v44\n\t"
    "v_exp_f32_e32 v136, v136\n\t"
    "v_exp_f32_e32 v137, v137\n\t"
    "v_exp_f32_e32 v138, v138\n\t"
    "v_exp_f32_e32 v139, v139\n\t"
    "v_smfmac_f32_32x32x32_bf16 v[48:63], v[156:159], v[120:127], v44\n\t"
    "v_exp_f32_e32 v140, v140\n\t"
    "v_exp_f32_e32 v141, v141\n\t"
    "v_exp_f32_e32 v142, v142\n\t"
    "v_exp_f32_e32 v143, v143\n\t"
    "s_sub_u32 %[cnt], %[cnt], 1\n\t"
    "s_cmp_lg_u32 %[cnt], 0\n\t"
    "s_cbranch_scc1 L_att_%=\n\t"
    "s_nop 1\n\t"
    "v_smfmac_f32_32x32x32_bf16 v[64:79], v[152:155], v[128:135], v44\n\t"
    "v_smfmac_f32_32x32x32_bf16 v[64:79], v[156:159], v[136:143], v44\n\t"
    "s_nop 7\n\t"
    "s_nop 7\n\t"
        : "+{v[48:63]}"(oa), "+{v[64:79]}"(ob), "+{v[128:143]}"(s3), "+{v[152:155]}"(v0), "+{v[156:159]}"(v1), "+{v45}"(ak1), "+{v46}"(ak2), "+{v47}"(av),
          [cnt] "+s"(cnt), [cur] "+s"(cur), [prv] "+s"(prv), [blk] "+s"(blk), [nxt] "=&s"(nxt), [dlt] "=&s"(dlt)
        : "{v[32:35]}"(qa1), "{v[36:39]}"(qb1), "{v[40:41]}"(qa2), "{v[42:43]}"(qb2), "{v44}"(0xDDDDDDDDu), "{v150}"(lane * 16),
          [srclo] "s"((u32)(size_t)kvx), [srchi] "s"((u32)((size_t)kvx >> 32)), [wofs] "s"(w * TILE)
        : "v80","v81","v82","v83","v84","v85","v86","v87","v88","v89","v90","v91","v92","v93","v94","v95","v96","v97","v98","v99","v100","v101","v102","v103","v104","v105","v106","v107","v108","v109","v110","v111","v112","v113","v114","v115","v116","v117","v118","v119","v120","v121","v122","v123","v124","v125","v126","v127","v144","v145","v146","v147","v148","v149", "s40", "s41", "scc", "memory");
    }
    STAMP(8);
    {
        const f32x16 give = half ? oa : ob; f32x16 o = half ? ob : oa;
        const u32 mine = L_CMB + w * 3072 + lane * 16, other = L_CMB + (w ^ 4) * 3072 + lane * 16;
#pragma unroll
        for (int c = 0; c < 3; ++c) sts128(mine + c * 1024, BC(u32x4, (f32x4{give[4 * c], give[4 * c + 1], give[4 * c + 2], give[4 * c + 3]})));
        BAR_L();
        float chk = 0.f;
#pragma unroll
        for (int c = 0; c < 3; ++c) { const f32x4 t = BC(f32x4, lds128(other + c * 1024));
#pragma unroll
            for (int e = 0; e < 4; ++e) { o[4 * c + e] += t[e]; chk += fabsf(o[4 * c + e]); } }
        const float l = from_hi_half(o[8]);
        if (__any(!(chk < 1e30f && l > 1e-20f))) {
            const u32x4 q1 = half ? qb1 : qa1; u32x2 q2 = half ? qb2 : qa2; if (hi) q2 = u32x2{0, 0};
            const GAS char* kv = (const GAS char*)(ws + W_KV + (size_t)h * 8 * SLOT);
            const f32x16 z = {}; float m = -INFINITY;
#pragma unroll 1
            for (int t = 0; t < 64; ++t)
                m = fmaxf(m, max16(mfma32k8(*(const GAS u32x2*)(kv + t * TILE + k2o), q2, mfma32(*(const GAS u32x4*)(kv + t * TILE + lane * 16), q1, z))));
            m = swap_max(m); o = z;
#pragma unroll 1
            for (int t = 0; t < 64; ++t) {
                f32x16 s = mfma32k8(*(const GAS u32x2*)(kv + t * TILE + k2o), q2, mfma32(*(const GAS u32x4*)(kv + t * TILE + lane * 16), q1, z));
#pragma unroll
                for (int r = 0; r < 16; ++r) s[r] = __builtin_amdgcn_exp2f(s[r] - m);
                o = pv(*(const GAS u32x4*)(kv + t * TILE + vo), lo8(s), o);
                o = pv(*(const GAS u32x4*)(kv + t * TILE + vo + 672), hi8(s), o);
            }
        }
        store_o(o, __builtin_amdgcn_rcpf(1.f + __expf(glogB)), ws + W_O + (size_t)(256 + j * 32 + h) * 10240, 64 * u + 32 * half + l31, hi);
    }
    STAMP(9);
}

__global__ __launch_bounds__(512, 2) void k_out(const char* __restrict__ woA, const char* __restrict__ woB, const char* __restrict__ ws, char* __restrict__ out) {
    const int tid = threadIdx.x, lane = tid & 63, w = __builtin_amdgcn_readfirstlane(tid >> 6);
    const int j = blockIdx.x & 7, bi = blockIdx.x >> 3, rt = bi & 3, ct = bi >> 2;
    const int u = w & 3, kh = w >> 2, l15 = lane & 15, g = lane >> 4;
    STAMP(10);
    u32 vo[7];
#pragma unroll
    for (int i = 0; i < 7; ++i) { const int ci = i * 64 + lane, row = ci / 10, c = ci - row * 10; vo[i] = (40 * w + row) * 1280 + c * 16 + ct * 160 + 4096 - (i & 3) * 1024; }
    auto stage = [&](const char* src, u32 dst) {
        src -= 4096;
        asm volatile("s_mov_b32 m0, %5\n\ts_nop 0\n\tglobal_load_lds_dwordx4 %0, %4\n\tglobal_load_lds_dwordx4 %1, %4 offset:1024\n\t"
                     "global_load_lds_dwordx4 %2, %4 offset:2048\n\tglobal_load_lds_dwordx4 %3, %4 offset:3072"
                     :: "v"(vo[0]), "v"(vo[1]), "v"(vo[2]), "v"(vo[3]), "s"(src), "s"(dst + w * 6400) : "memory");
        asm volatile("s_mov_b32 m0, %4\n\ts_nop 0\n\tglobal_load_lds_dwordx4 %0, %3\n\tglobal_load_lds_dwordx4 %1, %3 offset:1024\n\t"
                     "s_mov_b64 exec, 0xffff\n\tglobal_load_lds_dwordx4 %2, %3 offset:2048\n\ts_mov_b64 exec, -1"
                     :: "v"(vo[4]), "v"(vo[5]), "v"(vo[6]), "s"(src), "s"(dst + w * 6400 + 4096) : "memory");
    };
    u32x4 of[4][5];
    const int pos = 64 * rt + 16 * u + l15;
    auto ldo = [&](int r) {
        const GAS char* b = (const GAS char*)(ws + W_O + (size_t)((r >> 1) * 256 + j * 32 + (r & 1) * 16 + kh * 8) * 10240);
#pragma unroll
        for (int s = 0; s < 4; ++s) of[r][s] = *(const GAS u32x4*)(b + (2 * s + (g >> 1)) * 10240 + (g & 1) * 4096 + pos * 16);
        const u32x2 a = *(const GAS u32x2*)(b + g * 10240 + 8192 + pos * 8), c = *(const GAS u32x2*)(b + (g + 4) * 10240 + 8192 + pos * 8);
        of[r][4] = u32x4{a[0], a[1], c[0], c[1]};
    };
    ldo(0); ldo(1); ldo(2); ldo(3);
    stage(woA, 0); stage(woA + 320 * 1280, STG); stage(woB, 2 * STG);
    const u32 q = (lane >> 2) & 3, p = lane & 3;
    const u32 wm = kh * 25600 + ((g >> 1) * 20 + (g & 1) * 4 + q) * 160 + p * 8, w4 = kh * 25600 + (g * 20 + 16 + q) * 160 + p * 8;
    f32x4 acc[5] = {};
    auto round = [&](int r, u32 base) {
#pragma unroll
        for (int s = 0; s < 5; ++s)
#pragma unroll
            for (int c = 0; c < 5; ++c) {
                const u32 a = base + c * 32 + (s < 4 ? wm + s * 6400 : w4);
                const u32x2 lo = ldstr(a), hi = ldstr(a + (s < 4 ? 8 * 160 : 80 * 160));
                acc[c] = mfma16(u32x4{lo[0], lo[1], hi[0], hi[1]}, of[r][s], acc[c]);
            }
    };
    BAR_VM(14);
    STAMP(11);
    round(0, 0);
    BAR_VM(7);
    stage(woB + 320 * 1280, 0);
    round(1, STG);
    BAR_VM(7);
    round(2, 2 * STG);
    BAR_VM(0);
    round(3, 0);
    STAMP(12);
    const u32 xb = STG + u * 5120 + lane * 16;
    if (kh) { sts128(xb, BC(u32x4, acc[0])); sts128(xb + 1024, BC(u32x4, acc[1])); sts128(xb + 2048, BC(u32x4, acc[2])); }
    else { sts128(xb + 3072, BC(u32x4, acc[3])); sts128(xb + 4096, BC(u32x4, acc[4])); }
    BAR_L();
    GAS char* op = (GAS char*)out + (size_t)(256 * j + 2 * (pos & 127) + (pos >> 7)) * 1280 + ct * 160 + g * 8;
#pragma unroll
    for (int c = 0; c < 5; ++c) if ((c >= 3) == (kh != 0)) {
        const f32x4 t = acc[c] + BC(f32x4, lds128(xb + c * 1024));
        *(GAS u32x2*)(op + c * 32) = u32x2{cvtpk(t[0], t[1]), cvtpk(t[2], t[3])};
    }
    STAMP(13);
}

extern "C" void kernel_launch(void* const* d_in, const int* in_sizes, int n_in, void* d_out, int out_size, void* d_ws, size_t ws_size, hipStream_t stream) {
    (void)hipFuncSetAttribute((const void*)k_qkv_att, hipFuncAttributeMaxDynamicSharedMemorySize, LDS_BYTES);
    (void)hipFuncSetAttribute((const void*)k_out, hipFuncAttributeMaxDynamicSharedMemorySize, LDS_BYTES);
    k_qkv_att<<<256, 512, LDS_BYTES, stream>>>((const char*)d_in[0], (const char*)d_in[1], (const char*)d_in[4], (const char*)d_in[6], (char*)d_ws);
    k_out<<<256, 512, LDS_BYTES, stream>>>((const char*)d_in[2], (const char*)d_in[5], (const char*)d_ws, (char*)d_out);
}
```

```cpp
#include <hip/hip_runtime.h>
#include <hip/hip_bf16.h>

typedef unsigned u32;
typedef __bf16 bf16x8 __attribute__((ext_vector_type(8)));
typedef float f32x16 __attribute__((ext_vector_type(16)));
typedef u32 u32x4 __attribute__((ext_vector_type(4)));
#define LAS __attribute__((address_space(3)))
#define GAS __attribute__((address_space(1)))
#define DI __device__ __forceinline__
#define BC(T, x) __builtin_bit_cast(T, x)
#define RLX_AGENT __ATOMIC_RELAXED, __HIP_MEMORY_SCOPE_AGENT

constexpr u32 ONES = 0x3f803f80u;
constexpr int QT = 1280, GT = 2560;
constexpr int LT = 2592, ENT = 2 * LT;
constexpr int N0 = 3;
constexpr int STG = 32768, STG_W = 16384, L_QX = 65536, L_PM = 49152, LDS_BYTES = 163840;
constexpr u32 W_EPOCH = 0, W_FB = 4096, W_FA = W_FB + 32768, W_FOA = W_FA + 32768, W_G = W_FOA + 32768;
constexpr u32 W_Q = 262144;
constexpr u32 W_KV = W_Q + 2 * 32 * 64 * QT;
constexpr u32 W_O = W_KV + 2 * 32 * 64 * GT;
constexpr u32 W_END = W_O + 512 * 10240;

DI f32x16 mfma32(u32x4 a, u32x4 b, f32x16 c) { return __builtin_amdgcn_mfma_f32_32x32x16_bf16(BC(bf16x8, a), BC(bf16x8, b), c, 0, 0, 0); }
DI u32x4 lds128(u32 a) { return *(const LAS u32x4*)(size_t)a; }
#define BAR_VM(N) asm volatile("s_waitcnt vmcnt(" #N ") lgkmcnt(0)\n\ts_barrier" ::: "memory")
#define BAR_L() asm volatile("s_waitcnt lgkmcnt(0)\n\ts_barrier" ::: "memory")
#define SB() __builtin_amdgcn_sched_barrier(0)
DI void st32(const char* base, u32 off, u32 v) { asm volatile("global_store_dword %0, %1, %2 sc1" :: "v"(off), "v"(v), "s"(base) : "memory"); }
DI u32x4 srd(const void* p, u32 bytes) { return u32x4{(u32)(size_t)p, (u32)((size_t)p >> 32) & 0xffffu, bytes, 0x20000u}; }
template <int SRC, int DST> DI void dma4(const char* src, u32 dst, const u32 (&o)[4]) {
    asm volatile("s_add_u32 m0, %6, %8\n\ts_add_u32 s98, %4, %7\n\ts_addc_u32 s99, %5, 0\n\tglobal_load_lds_dwordx4 %0, s[98:99]\n\tglobal_load_lds_dwordx4 %1, s[98:99] offset:1024\n\t"
                 "global_load_lds_dwordx4 %2, s[98:99] offset:2048\n\tglobal_load_lds_dwordx4 %3, s[98:99] offset:3072"
                 :: "v"(o[0]), "v"(o[1]), "v"(o[2]), "v"(o[3]), "s"((u32)(size_t)src), "s"((u32)((size_t)src >> 32)), "s"(dst), "i"(SRC), "i"(DST) : "s98", "s99", "scc", "memory"); }

__global__ __launch_bounds__(512, 2) void k_fused(const char* __restrict__ x, const char* __restrict__ wA, const char* __restrict__ wB, const char* __restrict__ gw,
                                                  const char* __restrict__ woA, const char* __restrict__ woB, char* __restrict__ ws, char* __restrict__ out) {
    const int tid = threadIdx.x, lane = tid & 63, w = __builtin_amdgcn_readfirstlane(tid >> 6);
    const int xcd = blockIdx.x & 7, bi = blockIdx.x >> 3;
    const int u = w & 3, grp = w >> 2, l31 = lane & 31, hi = lane >> 5;
    const int tb = (bi & 7) * 2 + (xcd & 1), ct = (bi >> 3) * 4 + (xcd >> 1);
    const bool vt = ct >= 10;
    u32 xo[4], wo[4];
    auto mkwo = [&]() { int ln = lane; asm volatile("" : "+v"(ln)); __builtin_assume(ln >= 0 && ln < 64);
#pragma unroll
        for (int i = 0; i < 4; ++i) wo[i] = (ln >> 4) * 3840 + (((ln & 15) ^ (((i >> 1) * 4 + (ln >> 4)) << 1)) << 4) + i * (4 * 3840 - 1024); };
    auto mkxo = [&]() { int ln = lane; asm volatile("" : "+v"(ln)); __builtin_assume(ln >= 0 && ln < 64);
        const int m = ln >> 3, sh = vt ? 0 : 1, p0 = ((m & 3) << sh) + ((m & 4) << 1);
#pragma unroll
        for (int i = 0; i < 4; ++i) xo[i] = p0 * 2560 + (((ln & 7) ^ (ln >> 4) ^ ((i & 1) << 2)) << 4) + (i & 1) * 16 * 2560 + (i >> 1) * (vt ? 4 * 2560 : 2560) - i * 1024; };
    const char* xs = x + (128 * tb + (u >> 1) + 64 * (u & 1)) * 1280;
    const char *wsB = wB + (16 * u * 3840 + ct * 256), *wsA = wA + (16 * u * 3840 + ct * 256);
#define ldx(s, slot) dma4<(s) * 128, (slot) * STG>(xs, u * 4096, xo)
#define ldw(m, s, slot) dma4<(s) * 64 * 3840, (slot) * STG + STG_W>(ws##m, u * 4096, wo)
    auto touch = [&](const char* base, u32 off, u32 junk) { asm volatile("s_mov_b32 m0, %2\n\ts_nop 0\n\tglobal_load_lds_dword %0, %1" :: "v"(off), "s"(base), "s"(junk) : "memory"); };
    asm volatile("" :: "s"(x), "s"(wA), "s"(wB), "s"(gw), "s"(woA), "s"(woB), "s"(ws), "s"(out));
    auto burst = [&](int A, int B) {
        const u32 junk = ct < 15 ? 4 * STG : 0;
        touch(x + (tb * 128 + ct * 8 + u * 2) * 1280 + A * 128, hi * 1280 + min(l31, B - A) * 128, junk);
        touch(wB + (A * 64 + tb * 4 + u) * 3840 + min(ct, 14) * 256, min(lane >> 1, B - A) * (64 * 3840) + (lane & 1) * 128, junk); };
    if (grp == 0) {
        burst(1, N0);
        u32 cpf = (bi * 8 + (lane & 7) + (lane & 8) * 32) * 128; unsigned long long pc;
        if (w == 0) asm volatile("s_getpc_b64 %0\n.Lpc0:\n\tv_min_u32_e32 %1, .Lpc_end-.Lpc0, %1\n\tglobal_load_lds_dword %1, %0" : "=&s"(pc), "+v"(cpf) :: "memory");
    } else {
        if (ct < 15) { mkwo(); ldw(B, 0, 0); }
        mkxo();
        if (ct < 15) ldx(0, 0);
    }
    u32 epoch;
    const int h = xcd * 4 + (bi & 3), j = bi >> 2, clo = (20 * h) >> 7, chi = (20 * h + 19) >> 7;
    const int rt = bi & 3, ts = ct < 5 ? QT : GT;
    const u32 dsto = (ct < 5 ? W_Q : W_KV) + (grp * 2048 + tb * 4 + (u & 1) * 2) * ts;
    u32x4 sv[9];
    {
        const u32 T = 8 * j + w, gate = (ct + 1) >> 4; const bool first = !(T & 1) && T < 4;
        const u32 Tp = first ? T : ((T & 1) ? T - 1 : T - 3), kvh = W_KV + h * 64 * GT, oh = W_O + (j * 32 + h) * 10240, ob = W_O + (xcd * 32 + grp * 8) * 10240;
#define SV(i, reg, a, b, c, d) sv[i] = u32x4{u32(a), u32(b), u32(c), u32(d)}; asm volatile("" : "+{" reg "}"(sv[i]))
        SV(0, "s[56:59]", kvh + (16 + w) * GT, (u ? u - 1 : 8) * ENT + grp * LT, 9 * ENT + grp * LT, grp * LT);
        SV(1, "s[60:63]", W_FA + (ct * 16 + tb) * 128, W_FOA + (j * 32 + h) * 128, W_FOA + xcd * 4096, W_Q + ((32 + h) * 64 + T) * QT);
        SV(2, "s[64:67]", kvh + (2048 + T) * GT, kvh + (2048 + Tp) * GT, W_G + T * 128, W_G + 8192 + (8 * j + 2 * u + grp) * 128);
        SV(3, "s[68:71]", oh + w * 512, oh + 7168 + w * 256, oh + 256 * 10240 + (2 * u + grp) * 512, oh + 256 * 10240 + 7168 + (2 * u + grp) * 256);
        SV(4, "s[72:75]", ob + u * 256, ob + u * 128, 40 * w * 1280 + j * 160, w * 6400);
        SV(5, "s[76:79]", first ? L_PM + 1024 : L_PM, kvh, w, grp * 25600);
        SV(6, "s[80:83]", (blockIdx.x * 8 + w) * 512, (bi * 8 + w) * 25 * 128, (256 * xcd + 128 * (rt >> 1) + (rt & 1) + 32 * u) * 1280 + j * 160, dsto);
        SV(7, "s[84:87]", (gate ^ 1) | (u << 2), L_QX + u * 3072, W_Q + (h * 64 + 8 * j + 2 * u) * QT, kvh + u * GT);
        SV(8, "s[88:91]", 5 * ENT + w * 3072, 5 * ENT + (w ^ 4) * 3072, u * ENT, blockIdx.x | w);
    }
    if (grp == 1) { if (ct < 15) { ldw(B, 1, 1); ldx(1, 1); } burst(N0 + 1, 9); }
    u32x4 cs[6], mk, mp;
    auto consts0 = [&]() {
        int ln = lane; asm volatile("" : "+v"(ln)); __builtin_assume(ln >= 0 && ln < 64);
        const int l31 = ln & 31, hi = ln >> 5, g = ln >> 4;
        cs[4] = u32x4{u32(hi * 512 + l31 * 8), u32(1024 + l31 * 8), u32(hi ? 2560 : 1024 + l31 * 8), 0u};
        cs[5] = u32x4{W_FB + ((min(g, 2) * 5 + ((ln & 4) ? chi : clo)) * 16 + (g ? ln & 3 : 2 * j + (u >> 1))) * 128, W_FB + (((u >> 1) * 5 + 5 + ((u & 1) ? chi : clo)) * 16 + (ln & 15)) * 128, u32(l31 * 4), u32(l31 * 128)};
        asm volatile("" : "+{v[16:19]}"(cs[4]), "+{v[20:23]}"(cs[5]));
    };
    auto consts1 = [&]() {
        int ln = lane; asm volatile("" : "+v"(ln)); __builtin_assume(ln >= 0 && ln < 64);
#pragma unroll
        for (int i = 0; i < 7; ++i) cs[i >> 2][i & 3] = __umul24((__umul24(ln, 6554) + i * 64 * 6554) >> 16, 1120) + ln * 16 + (i >> 2) * 4096;
        cs[1][3] = W_FA + (((ln & 7) >= 6 ? 15 : ((ln & 7) >> 1) * 5 + ((ln & 1) ? chi : clo)) * 16 + max(2 * j - 1 + min(ln >> 3, 2), 0)) * 128;
        asm volatile("" : "+{v[0:3]}"(cs[0]), "+{v[4:7]}"(cs[1]));
    };
    auto consts2 = [&]() {
        int ln = lane; asm volatile("" : "+v"(ln)); __builtin_assume(ln >= 0 && ln < 64);
        const int l31 = ln & 31, hi = ln >> 5, g = ln >> 4, q = (ln >> 2) & 3, pos = 64 * rt + (ln & 15);
        cs[2] = u32x4{0u, 0u, u32(1280 + (l31 < 20 ? l31 : 0) * 32 + ((hi ^ (l31 >> 4)) << 4)), u32(hi * 4096 + l31 * 16)};
        cs[3] = u32x4{u32((g >> 1) * 10240 + (g & 1) * 4096 + pos * 16), u32(g * 10240 + 8192 + pos * 8),
                      u32(((g >> 1) * 20 + (g & 1) * 4 + q) * 160 + (ln & 3) * 8), u32((g * 20 + 16 + q) * 160 + (ln & 3) * 8)};
#pragma unroll
        for (int e = 0; e < 4; ++e) {
            mk[e] = (u32)(l31 - 4 * hi - 8 * u - e) < 8u ? 0u : 0xff800000u; mp[e] = u == 0 && l31 - 4 * hi < e ? 0u : u < 2 ? 0xff800000u : ONES; }
        asm volatile("" : "+{v[8:11]}"(cs[2]), "+{v[12:15]}"(cs[3]), "+{v[200:203]}"(mk), "+{v[204:207]}"(mp));
    };
    u32x4 pk[8], so0;
#define PK_REGS(c) c "{v[160:163]}"(pk[0]), c "{v[164:167]}"(pk[1]), c "{v[168:171]}"(pk[2]), c "{v[172:175]}"(pk[3]), c "{v[176:179]}"(pk[4]), c "{v[180:183]}"(pk[5]), c "{v[184:187]}"(pk[6]), c "{v[188:191]}"(pk[7])
    auto mkso = [&]() {
        int ln = lane; asm volatile("" : "+v"(ln)); __builtin_assume(ln >= 0 && ln < 64);
        const int c5 = ct >= 10 ? ct - 10 : (ct >= 5 ? ct - 5 : ct), l15 = ln & 15, g = ln >> 4;
        if (vt) {
#pragma unroll
            for (int t = 0; t < 4; ++t) { const int col = 128 * c5 + 64 * (u >> 1) + 16 * t + l15, hv = (col * 3277) >> 16, d = col - hv * 20;
                so0[t] = hv * (64 * GT) + 1280 + (g >> 1) * 640 + d * 32 + (((g & 1) ^ (d >> 4)) << 4); }
        } else {
#pragma unroll
            for (int t = 0; t < 4; ++t) { const int qd = 32 * c5 + 16 * (u >> 1) + 4 * t + g, hq = (qd * 205) >> 10; so0[t] = hq * (64 * ts - 1280) + qd * 256 + l15 * 16; }
        }
    };
    u32x4 sa0, sa1;
    const char* dst = ws + dsto;
    if (ct < 15) {
        u32 xa0, xa1, wa0, wa1, wa2, wa3;
        auto mkga = [&]() { int ln = lane; asm volatile("" : "+v"(ln)); __builtin_assume(ln >= 0 && ln < 64);
            const int l15 = ln & 15, g = ln >> 4, q = (ln >> 2) & 3;
            xa0 = (64 * (u & 1) + l15) * 128 + ((g ^ (l15 >> 1)) << 4); xa1 = xa0 ^ 64;
            wa0 = STG_W + (8 * g + q) * 256 + (((4 * (u >> 1)) ^ ((g & 1) * 4 + q)) << 5) + (ln & 3) * 8; wa1 = wa0 ^ 32; wa2 = wa0 ^ 64; wa3 = wa0 ^ 96; };
#define GEMM_OUT PK_REGS("=&"), GEMM_ADDR
#define GEMM_ADDR "+{v56}"(xa0), "+{v57}"(xa1), "+{v58}"(wa0), "+{v59}"(wa1), "+{v60}"(wa2), "+{v61}"(wa3)
        if (grp == 1) {
            BAR_VM(10); ldw(B, 2, 2); ldx(2, 2); touch(wA + (tb * 4 + u) * 3840 + min(ct, 14) * 256, min(lane >> 1, 9) * (64 * 3840) + (lane & 1) * 128, 4 * STG + u * 4096);
            BAR_VM(11); consts1(); consts2(); ldw(B, 3, 3); ldx(3, 3);
            BAR_VM(9); ldw(B, 4, 4); ldx(4, 4);
            BAR_VM(8); ldw(B, 5, 0); ldx(5, 0); consts0();
            BAR_VM(8); ldw(B, 6, 1); ldx(6, 1);
            BAR_VM(8); ldw(B, 7, 2); ldx(7, 2); mkso(); sa0 = so0; sa1 = so0 + ts; asm volatile("" : "+{v[192:195]}"(sa0), "+{v[196:199]}"(sa1));
            BAR_VM(8); ldw(B, 8, 3); ldx(8, 3); mkga(); asm volatile("" : GEMM_ADDR);
            BAR_VM(8); ldw(B, 9, 4); ldx(9, 4);
            BAR_VM(8); ldw(A, 5, 0); ldw(A, 6, 1);
            BAR_VM(8); ldw(A, 7, 2); ldw(A, 8, 3);
            BAR_VM(12); ldw(A, 9, 4);
            asm volatile(
        "s_cmp_ge_u32 %[ct], 10\n\t"
        "s_cbranch_scc1 .Lgv_%=\n\t"
        "ds_read_b64_tr_b16 v[176:177], v58 offset:0\n\t"
        "ds_read_b64_tr_b16 v[178:179], v58 offset:1024\n\t"
        "ds_read_b128 v[160:163], v56 offset:0\n\t"
        "ds_read_b128 v[164:167], v56 offset:2048\n\t"
        "ds_read_b128 v[168:171], v56 offset:4096\n\t"
        "ds_read_b128 v[172:175], v56 offset:6144\n\t"
        "ds_read_b64_tr_b16 v[180:181], v59 offset:0\n\t"
        "ds_read_b64_tr_b16 v[182:183], v59 offset:1024\n\t"
        "ds_read_b64_tr_b16 v[184:185], v60 offset:0\n\t"
        "ds_read_b64_tr_b16 v[186:187], v60 offset:1024\n\t"
        "ds_read_b64_tr_b16 v[188:189], v61 offset:0\n\t"
        "ds_read_b64_tr_b16 v[190:191], v61 offset:1024\n\t"
        "s_waitcnt lgkmcnt(6)\n\t"
        "v_mfma_f32_16x16x32_bf16 v[64:67], v[176:179], v[160:163], 0\n\t"
        "ds_read_b64_tr_b16 v[144:145], v58 offset:8192\n\t"
        "v_mfma_f32_16x16x32_bf16 v[68:71], v[176:179], v[164:167], 0\n\t"
        "ds_read_b64_tr_b16 v[146:147], v58 offset:9216\n\t"
        "v_mfma_f32_16x16x32_bf16 v[72:75], v[176:179], v[168:171], 0\n\t"
        "ds_read_b128 v[128:131], v57 offset:0\n\t"
        "v_mfma_f32_16x16x32_bf16 v[76:79], v[176:179], v[172:175], 0\n\t"
        "ds_read_b128 v[132:135], v57 offset:2048\n\t"
        "s_waitcnt lgkmcnt(4)\n\t"
        "v_mfma_f32_16x16x32_bf16 v[80:83], v[180:183], v[160:163], 0\n\t"
        "ds_read_b128 v[136:139], v57 offset:4096\n\t"
        "v_mfma_f32_16x16x32_bf16 v[84:87], v[180:183], v[164:167], 0\n\t"
        "ds_read_b128 v[140:143], v57 offset:6144\n\t"
        "v_mfma_f32_16x16x32_bf16 v[88:91], v[180:183], v[168:171], 0\n\t"
        "ds_read_b64_tr_b16 v[148:149], v59 offset:8192\n\t"
        "v_mfma_f32_16x16x32_bf16 v[92:95], v[180:183], v[172:175], 0\n\t"
        "ds_read_b64_tr_b16 v[150:151], v59 offset:9216\n\t"
        "s_waitcnt vmcnt(8)\n\t"
        "s_barrier\n\t"
        "v_mfma_f32_16x16x32_bf16 v[96:99], v[184:187], v[160:163], 0\n\t"
        "ds_read_b64_tr_b16 v[152:153], v60 offset:8192\n\t"
        "v_mfma_f32_16x16x32_bf16 v[100:103], v[184:187], v[164:167], 0\n\t"
        "ds_read_b64_tr_b16 v[154:155], v60 offset:9216\n\t"
        "v_mfma_f32_16x16x32_bf16 v[104:107], v[184:187], v[168:171], 0\n\t"
        "ds_read_b64_tr_b16 v[156:157], v61 offset:8192\n\t"
        "v_mfma_f32_16x16x32_bf16 v[108:111], v[184:187], v[172:175], 0\n\t"
        "ds_read_b64_tr_b16 v[158:159], v61 offset:9216\n\t"
        "v_mfma_f32_16x16x32_bf16 v[112:115], v[188:191], v[160:163], 0\n\t"
        "v_mfma_f32_16x16x32_bf16 v[116:119], v[188:191], v[164:167], 0\n\t"
        "v_mfma_f32_16x16x32_bf16 v[120:123], v[188:191], v[168:171], 0\n\t"
        "v_mfma_f32_16x16x32_bf16 v[124:127], v[188:191], v[172:175], 0\n\t"
        "s_waitcnt lgkmcnt(6)\n\t"
        "v_mfma_f32_16x16x32_bf16 v[64:67], v[144:147], v[128:131], v[64:67]\n\t"
        "ds_read_b64_tr_b16 v[176:177], v58 offset:32768\n\t"
        "v_mfma_f32_16x16x32_bf16 v[68:71], v[144:147], v[132:135], v[68:71]\n\t"
        "ds_read_b64_tr_b16 v[178:179], v58 offset:33792\n\t"
        "v_mfma_f32_16x16x32_bf16 v[72:75], v[144:147], v[136:139], v[72:75]\n\t"
        "ds_read_b128 v[160:163], v56 offset:32768\n\t"
        "v_mfma_f32_16x16x32_bf16 v[76:79], v[144:147], v[140:143], v[76:79]\n\t"
        "ds_read_b128 v[164:167], v56 offset:34816\n\t"
        "s_waitcnt lgkmcnt(4)\n\t"
        "v_mfma_f32_16x16x32_bf16 v[80:83], v[148:151], v[128:131], v[80:83]\n\t"
        "ds_read_b128 v[168:171], v56 offset:36864\n\t"
        "v_mfma_f32_16x16x32_bf16 v[84:87], v[148:151], v[132:135], v[84:87]\n\t"
        "ds_read_b128 v[172:175], v56 offset:38912\n\t"
        "v_mfma_f32_16x16x32_bf16 v[88:91], v[148:151], v[136:139], v[88:91]\n\t"
        "ds_read_b64_tr_b16 v[180:181], v59 offset:32768\n\t"
        "v_mfma_f32_16x16x32_bf16 v[92:95], v[148:151], v[140:143], v[92:95]\n\t"
        "ds_read_b64_tr_b16 v[182:183], v59 offset:33792\n\t"
        "v_mfma_f32_16x16x32_bf16 v[96:99], v[152:155], v[128:131], v[96:99]\n\t"
        "ds_read_b64_tr_b16 v[184:185], v60 offset:32768\n\t"
        "v_mfma_f32_16x16x32_bf16 v[100:103], v[152:155], v[132:135], v[100:103]\n\t"
        "ds_read_b64_tr_b16 v[186:187], v60 offset:33792\n\t"
        "v_mfma_f32_16x16x32_bf16 v[104:107], v[152:155], v[136:139], v[104:107]\n\t"
        "ds_read_b64_tr_b16 v[188:189], v61 offset:32768\n\t"
        "v_mfma_f32_16x16x32_bf16 v[108:111], v[152:155], v[140:143], v[108:111]\n\t"
        "ds_read_b64_tr_b16 v[190:191], v61 offset:33792\n\t"
        "v_mfma_f32_16x16x32_bf16 v[112:115], v[156:159], v[128:131], v[112:115]\n\t"
        "v_mfma_f32_16x16x32_bf16 v[116:119], v[156:159], v[132:135], v[116:119]\n\t"
        "v_mfma_f32_16x16x32_bf16 v[120:123], v[156:159], v[136:139], v[120:123]\n\t"
        "v_mfma_f32_16x16x32_bf16 v[124:127], v[156:159], v[140:143], v[124:127]\n\t"
        "s_waitcnt lgkmcnt(6)\n\t"
        "v_mfma_f32_16x16x32_bf16 v[64:67], v[176:179], v[160:163], v[64:67]\n\t"
        "ds_read_b64_tr_b16 v[144:145], v58 offset:40960\n\t"
        "v_mfma_f32_16x16x32_bf16 v[68:71], v[176:179], v[164:167], v[68:71]\n\t"
        "ds_read_b64_tr_b16 v[146:147], v58 offset:41984\n\t"
        "v_mfma_f32_16x16x32_bf16 v[72:75], v[176:179], v[168:171], v[72:75]\n\t"
        "ds_read_b128 v[128:131], v57 offset:32768\n\t"
        "v_mfma_f32_16x16x32_bf16 v[76:79], v[176:179], v[172:175], v[76:79]\n\t"
        "ds_read_b128 v[132:135], v57 offset:34816\n\t"
        "s_waitcnt lgkmcnt(4)\n\t"
        "v_mfma_f32_16x16x32_bf16 v[80:83], v[180:183], v[160:163], v[80:83]\n\t"
        "ds_read_b128 v[136:139], v57 offset:36864\n\t"
        "v_mfma_f32_16x16x32_bf16 v[84:87], v[180:183], v[164:167], v[84:87]\n\t"
        "ds_read_b128 v[140:143], v57 offset:38912\n\t"
        "v_mfma_f32_16x16x32_bf16 v[88:91], v[180:183], v[168:171], v[88:91]\n\t"
        "ds_read_b64_tr_b16 v[148:149], v59 offset:40960\n\t"
        "v_mfma_f32_16x16x32_bf16 v[92:95], v[180:183], v[172:175], v[92:95]\n\t"
        "ds_read_b64_tr_b16 v[150:151], v59 offset:41984\n\t"
        "v_mfma_f32_16x16x32_bf16 v[96:99], v[184:187], v[160:163], v[96:99]\n\t"
        "ds_read_b64_tr_b16 v[152:153], v60 offset:40960\n\t"
        "v_mfma_f32_16x16x32_bf16 v[100:103], v[184:187], v[164:167], v[100:103]\n\t"
        "ds_read_b64_tr_b16 v[154:155], v60 offset:41984\n\t"
        "v_mfma_f32_16x16x32_bf16 v[104:107], v[184:187], v[168:171], v[104:107]\n\t"
        "ds_read_b64_tr_b16 v[156:157], v61 offset:40960\n\t"
        "v_mfma_f32_16x16x32_bf16 v[108:111], v[184:187], v[172:175], v[108:111]\n\t"
        "ds_read_b64_tr_b16 v[158:159], v61 offset:41984\n\t"
        "v_mfma_f32_16x16x32_bf16 v[112:115], v[188:191], v[160:163], v[112:115]\n\t"
        "v_add_u32_e32 v56, 0x10000, v56\n\t"
        "v_add_u32_e32 v57, 0x10000, v57\n\t"
        "v_add_u32_e32 v58, 0x10000, v58\n\t"
        "v_mfma_f32_16x16x32_bf16 v[116:119], v[188:191], v[164:167], v[116:119]\n\t"
        "v_add_u32_e32 v59, 0x10000, v59\n\t"
        "v_add_u32_e32 v60, 0x10000, v60\n\t"
        "v_add_u32_e32 v61, 0x10000, v61\n\t"
        "v_mfma_f32_16x16x32_bf16 v[120:123], v[188:191], v[168:171], v[120:123]\n\t"
        "v_mfma_f32_16x16x32_bf16 v[124:127], v[188:191], v[172:175], v[124:127]\n\t"
        "s_waitcnt lgkmcnt(6)\n\t"
        "v_mfma_f32_16x16x32_bf16 v[64:67], v[144:147], v[128:131], v[64:67]\n\t"
        "ds_read_b64_tr_b16 v[176:177], v58 offset:0\n\t"
        "v_mfma_f32_16x16x32_bf16 v[68:71], v[144:147], v[132:135], v[68:71]\n\t"
        "ds_read_b64_tr_b16 v[178:179], v58 offset:1024\n\t"
        "v_mfma_f32_16x16x32_bf16 v[72:75], v[144:147], v[136:139], v[72:75]\n\t"
        "ds_read_b128 v[160:163], v56 offset:0\n\t"
        "v_mfma_f32_16x16x32_bf16 v[76:79], v[144:147], v[140:143], v[76:79]\n\t"
        "ds_read_b128 v[164:167], v56 offset:2048\n\t"
        "s_waitcnt lgkmcnt(4)\n\t"
        "v_mfma_f32_16x16x32_bf16 v[80:83], v[148:151], v[128:131], v[80:83]\n\t"
        "ds_read_b128 v[168:171], v56 offset:4096\n\t"
        "v_mfma_f32_16x16x32_bf16 v[84:87], v[148:151], v[132:135], v[84:87]\n\t"
        "ds_read_b128 v[172:175], v56 offset:6144\n\t"
        "v_mfma_f32_16x16x32_bf16 v[88:91], v[148:151], v[136:139], v[88:91]\n\t"
        "ds_read_b64_tr_b16 v[180:181], v59 offset:0\n\t"
        "v_mfma_f32_16x16x32_bf16 v[92:95], v[148:151], v[140:143], v[92:95]\n\t"
        "ds_read_b64_tr_b16 v[182:183], v59 offset:1024\n\t"
        "v_mfma_f32_16x16x32_bf16 v[96:99], v[152:155], v[128:131], v[96:99]\n\t"
        "ds_read_b64_tr_b16 v[184:185], v60 offset:0\n\t"
        "v_mfma_f32_16x16x32_bf16 v[100:103], v[152:155], v[132:135], v[100:103]\n\t"
        "ds_read_b64_tr_b16 v[186:187], v60 offset:1024\n\t"
        "v_mfma_f32_16x16x32_bf16 v[104:107], v[152:155], v[136:139], v[104:107]\n\t"
        "ds_read_b64_tr_b16 v[188:189], v61 offset:0\n\t"
        "v_mfma_f32_16x16x32_bf16 v[108:111], v[152:155], v[140:143], v[108:111]\n\t"
        "ds_read_b64_tr_b16 v[190:191], v61 offset:1024\n\t"
        "v_mfma_f32_16x16x32_bf16 v[112:115], v[156:159], v[128:131], v[112:115]\n\t"
        "v_mfma_f32_16x16x32_bf16 v[116:119], v[156:159], v[132:135], v[116:119]\n\t"
        "v_mfma_f32_16x16x32_bf16 v[120:123], v[156:159], v[136:139], v[120:123]\n\t"
        "v_mfma_f32_16x16x32_bf16 v[124:127], v[156:159], v[140:143], v[124:127]\n\t"
        "s_waitcnt lgkmcnt(6)\n\t"
        "v_mfma_f32_16x16x32_bf16 v[64:67], v[176:179], v[160:163], v[64:67]\n\t"
        "ds_read_b64_tr_b16 v[144:145], v58 offset:8192\n\t"
        "v_mfma_f32_16x16x32_bf16 v[68:71], v[176:179], v[164:167], v[68:71]\n\t"
        "ds_read_b64_tr_b16 v[146:147], v58 offset:9216\n\t"
        "v_mfma_f32_16x16x32_bf16 v[72:75], v[176:179], v[168:171], v[72:75]\n\t"
        "ds_read_b128 v[128:131], v57 offset:0\n\t"
        "v_mfma_f32_16x16x32_bf16 v[76:79], v[176:179], v[172:175], v[76:79]\n\t"
        "ds_read_b128 v[132:135], v57 offset:2048\n\t"
        "s_waitcnt lgkmcnt(4)\n\t"
        "v_mfma_f32_16x16x32_bf16 v[80:83], v[180:183], v[160:163], v[80:83]\n\t"
        "ds_read_b128 v[136:139], v57 offset:4096\n\t"
        "v_mfma_f32_16x16x32_bf16 v[84:87], v[180:183], v[164:167], v[84:87]\n\t"
        "ds_read_b128 v[140:143], v57 offset:6144\n\t"
        "v_mfma_f32_16x16x32_bf16 v[88:91], v[180:183], v[168:171], v[88:91]\n\t"
        "ds_read_b64_tr_b16 v[148:149], v59 offset:8192\n\t"
        "v_mfma_f32_16x16x32_bf16 v[92:95], v[180:183], v[172:175], v[92:95]\n\t"
        "ds_read_b64_tr_b16 v[150:151], v59 offset:9216\n\t"
        "v_mfma_f32_16x16x32_bf16 v[96:99], v[184:187], v[160:163], v[96:99]\n\t"
        "ds_read_b64_tr_b16 v[152:153], v60 offset:8192\n\t"
        "v_mfma_f32_16x16x32_bf16 v[100:103], v[184:187], v[164:167], v[100:103]\n\t"
        "ds_read_b64_tr_b16 v[154:155], v60 offset:9216\n\t"
        "v_mfma_f32_16x16x32_bf16 v[104:107], v[184:187], v[168:171], v[104:107]\n\t"
        "ds_read_b64_tr_b16 v[156:157], v61 offset:8192\n\t"
        "v_mfma_f32_16x16x32_bf16 v[108:111], v[184:187], v[172:175], v[108:111]\n\t"
        "ds_read_b64_tr_b16 v[158:159], v61 offset:9216\n\t"
        "v_mfma_f32_16x16x32_bf16 v[112:115], v[188:191], v[160:163], v[112:115]\n\t"
        "v_mfma_f32_16x16x32_bf16 v[116:119], v[188:191], v[164:167], v[116:119]\n\t"
        "v_mfma_f32_16x16x32_bf16 v[120:123], v[188:191], v[168:171], v[120:123]\n\t"
        "v_mfma_f32_16x16x32_bf16 v[124:127], v[188:191], v[172:175], v[124:127]\n\t"
        "s_waitcnt lgkmcnt(6)\n\t"
        "v_mfma_f32_16x16x32_bf16 v[64:67], v[144:147], v[128:131], v[64:67]\n\t"
        "v_mfma_f32_16x16x32_bf16 v[68:71], v[144:147], v[132:135], v[68:71]\n\t"
        "s_waitcnt vmcnt(0) lgkmcnt(0)\n\t"
        "s_barrier\n\t"
        "v_mfma_f32_16x16x32_bf16 v[72:75], v[144:147], v[136:139], v[72:75]\n\t"
        "ds_read_b64_tr_b16 v[176:177], v58 offset:32768\n\t"
        "v_mfma_f32_16x16x32_bf16 v[76:79], v[144:147], v[140:143], v[76:79]\n\t"
        "ds_read_b64_tr_b16 v[178:179], v58 offset:33792\n\t"
        "v_mfma_f32_16x16x32_bf16 v[80:83], v[148:151], v[128:131], v[80:83]\n\t"
        "ds_read_b128 v[160:163], v56 offset:32768\n\t"
        "v_mfma_f32_16x16x32_bf16 v[84:87], v[148:151], v[132:135], v[84:87]\n\t"
        "ds_read_b128 v[164:167], v56 offset:34816\n\t"
        "v_mfma_f32_16x16x32_bf16 v[88:91], v[148:151], v[136:139], v[88:91]\n\t"
        "ds_read_b128 v[168:171], v56 offset:36864\n\t"
        "v_mfma_f32_16x16x32_bf16 v[92:95], v[148:151], v[140:143], v[92:95]\n\t"
        "ds_read_b128 v[172:175], v56 offset:38912\n\t"
        "v_mfma_f32_16x16x32_bf16 v[96:99], v[152:155], v[128:131], v[96:99]\n\t"
        "ds_read_b64_tr_b16 v[180:181], v59 offset:32768\n\t"
        "v_mfma_f32_16x16x32_bf16 v[100:103], v[152:155], v[132:135], v[100:103]\n\t"
        "ds_read_b64_tr_b16 v[182:183], v59 offset:33792\n\t"
        "v_mfma_f32_16x16x32_bf16 v[104:107], v[152:155], v[136:139], v[104:107]\n\t"
        "ds_read_b64_tr_b16 v[184:185], v60 offset:32768\n\t"
        "v_mfma_f32_16x16x32_bf16 v[108:111], v[152:155], v[140:143], v[108:111]\n\t"
        "ds_read_b64_tr_b16 v[186:187], v60 offset:33792\n\t"
        "v_mfma_f32_16x16x32_bf16 v[112:115], v[156:159], v[128:131], v[112:115]\n\t"
        "ds_read_b64_tr_b16 v[188:189], v61 offset:32768\n\t"
        "v_mfma_f32_16x16x32_bf16 v[116:119], v[156:159], v[132:135], v[116:119]\n\t"
        "ds_read_b64_tr_b16 v[190:191], v61 offset:33792\n\t"
        "v_mfma_f32_16x16x32_bf16 v[120:123], v[156:159], v[136:139], v[120:123]\n\t"
        "v_mfma_f32_16x16x32_bf16 v[124:127], v[156:159], v[140:143], v[124:127]\n\t"
        "s_waitcnt lgkmcnt(6)\n\t"
        "v_mfma_f32_16x16x32_bf16 v[64:67], v[176:179], v[160:163], v[64:67]\n\t"
        "ds_read_b64_tr_b16 v[144:145], v58 offset:40960\n\t"
        "v_mfma_f32_16x16x32_bf16 v[68:71], v[176:179], v[164:167], v[68:71]\n\t"
        "ds_read_b64_tr_b16 v[146:147], v58 offset:41984\n\t"
        "v_mfma_f32_16x16x32_bf16 v[72:75], v[176:179], v[168:171], v[72:75]\n\t"
        "ds_read_b128 v[128:131], v57 offset:32768\n\t"
        "v_mfma_f32_16x16x32_bf16 v[76:79], v[176:179], v[172:175], v[76:79]\n\t"
        "ds_read_b128 v[132:135], v57 offset:34816\n\t"
        "s_waitcnt lgkmcnt(4)\n\t"
        "v_mfma_f32_16x16x32_bf16 v[80:83], v[180:183], v[160:163], v[80:83]\n\t"
        "ds_read_b128 v[136:139], v57 offset:36864\n\t"
        "v_mfma_f32_16x16x32_bf16 v[84:87], v[180:183], v[164:167], v[84:87]\n\t"
        "ds_read_b128 v[140:143], v57 offset:38912\n\t"
        "v_mfma_f32_16x16x32_bf16 v[88:91], v[180:183], v[168:171], v[88:91]\n\t"
        "ds_read_b64_tr_b16 v[148:149], v59 offset:40960\n\t"
        "v_mfma_f32_16x16x32_bf16 v[92:95], v[180:183], v[172:175], v[92:95]\n\t"
        "ds_read_b64_tr_b16 v[150:151], v59 offset:41984\n\t"
        "v_mfma_f32_16x16x32_bf16 v[96:99], v[184:187], v[160:163], v[96:99]\n\t"
        "ds_read_b64_tr_b16 v[152:153], v60 offset:40960\n\t"
        "v_mfma_f32_16x16x32_bf16 v[100:103], v[184:187], v[164:167], v[100:103]\n\t"
        "ds_read_b64_tr_b16 v[154:155], v60 offset:41984\n\t"
        "v_mfma_f32_16x16x32_bf16 v[104:107], v[184:187], v[168:171], v[104:107]\n\t"
        "ds_read_b64_tr_b16 v[156:157], v61 offset:40960\n\t"
        "v_mfma_f32_16x16x32_bf16 v[108:111], v[184:187], v[172:175], v[108:111]\n\t"
        "ds_read_b64_tr_b16 v[158:159], v61 offset:41984\n\t"
        "v_mfma_f32_16x16x32_bf16 v[112:115], v[188:191], v[160:163], v[112:115]\n\t"
        "v_add_u32_e32 v56, 0x10000, v56\n\t"
        "v_add_u32_e32 v57, 0x10000, v57\n\t"
        "v_add_u32_e32 v58, 0x10000, v58\n\t"
        "v_mfma_f32_16x16x32_bf16 v[116:119], v[188:191], v[164:167], v[116:119]\n\t"
        "v_add_u32_e32 v59, 0x10000, v59\n\t"
        "v_add_u32_e32 v60, 0x10000, v60\n\t"
        "v_add_u32_e32 v61, 0x10000, v61\n\t"
        "v_mfma_f32_16x16x32_bf16 v[120:123], v[188:191], v[168:171], v[120:123]\n\t"
        "v_mfma_f32_16x16x32_bf16 v[124:127], v[188:191], v[172:175], v[124:127]\n\t"
        "s_waitcnt lgkmcnt(6)\n\t"
        "v_mfma_f32_16x16x32_bf16 v[64:67], v[144:147], v[128:131], v[64:67]\n\t"
        "ds_read_b64_tr_b16 v[176:177], v58 offset:0\n\t"
        "v_mfma_f32_16x16x32_bf16 v[68:71], v[144:147], v[132:135], v[68:71]\n\t"
        "ds_read_b64_tr_b16 v[178:179], v58 offset:1024\n\t"
        "v_mfma_f32_16x16x32_bf16 v[72:75], v[144:147], v[136:139], v[72:75]\n\t"
        "ds_read_b128 v[160:163], v56 offset:0\n\t"
        "v_mfma_f32_16x16x32_bf16 v[76:79], v[144:147], v[140:143], v[76:79]\n\t"
        "ds_read_b128 v[164:167], v56 offset:2048\n\t"
        "s_waitcnt lgkmcnt(4)\n\t"
        "v_mfma_f32_16x16x32_bf16 v[80:83], v[148:151], v[128:131], v[80:83]\n\t"
        "ds_read_b128 v[168:171], v56 offset:4096\n\t"
        "v_mfma_f32_16x16x32_bf16 v[84:87], v[148:151], v[132:135], v[84:87]\n\t"
        "ds_read_b128 v[172:175], v56 offset:6144\n\t"
        "v_mfma_f32_16x16x32_bf16 v[88:91], v[148:151], v[136:139], v[88:91]\n\t"
        "ds_read_b64_tr_b16 v[180:181], v59 offset:0\n\t"
        "v_mfma_f32_16x16x32_bf16 v[92:95], v[148:151], v[140:143], v[92:95]\n\t"
        "ds_read_b64_tr_b16 v[182:183], v59 offset:1024\n\t"
        "v_mfma_f32_16x16x32_bf16 v[96:99], v[152:155], v[128:131], v[96:99]\n\t"
        "ds_read_b64_tr_b16 v[184:185], v60 offset:0\n\t"
        "v_mfma_f32_16x16x32_bf16 v[100:103], v[152:155], v[132:135], v[100:103]\n\t"
        "ds_read_b64_tr_b16 v[186:187], v60 offset:1024\n\t"
        "v_mfma_f32_16x16x32_bf16 v[104:107], v[152:155], v[136:139], v[104:107]\n\t"
        "ds_read_b64_tr_b16 v[188:189], v61 offset:0\n\t"
        "v_mfma_f32_16x16x32_bf16 v[108:111], v[152:155], v[140:143], v[108:111]\n\t"
        "ds_read_b64_tr_b16 v[190:191], v61 offset:1024\n\t"
        "v_mfma_f32_16x16x32_bf16 v[112:115], v[156:159], v[128:131], v[112:115]\n\t"
        "v_mfma_f32_16x16x32_bf16 v[116:119], v[156:159], v[132:135], v[116:119]\n\t"
        "v_mfma_f32_16x16x32_bf16 v[120:123], v[156:159], v[136:139], v[120:123]\n\t"
        "v_mfma_f32_16x16x32_bf16 v[124:127], v[156:159], v[140:143], v[124:127]\n\t"
        "s_waitcnt lgkmcnt(6)\n\t"
        "v_mfma_f32_16x16x32_bf16 v[64:67], v[176:179], v[160:163], v[64:67]\n\t"
        "ds_read_b64_tr_b16 v[144:145], v58 offset:8192\n\t"
        "v_mfma_f32_16x16x32_bf16 v[68:71], v[176:179], v[164:167], v[68:71]\n\t"
        "ds_read_b64_tr_b16 v[146:147], v58 offset:9216\n\t"
        "v_mfma_f32_16x16x32_bf16 v[72:75], v[176:179], v[168:171], v[72:75]\n\t"
        "ds_read_b128 v[128:131], v57 offset:0\n\t"
        "v_mfma_f32_16x16x32_bf16 v[76:79], v[176:179], v[172:175], v[76:79]\n\t"
        "ds_read_b128 v[132:135], v57 offset:2048\n\t"
        "s_waitcnt lgkmcnt(4)\n\t"
        "v_mfma_f32_16x16x32_bf16 v[80:83], v[180:183], v[160:163], v[80:83]\n\t"
        "ds_read_b128 v[136:139], v57 offset:4096\n\t"
        "v_mfma_f32_16x16x32_bf16 v[84:87], v[180:183], v[164:167], v[84:87]\n\t"
        "ds_read_b128 v[140:143], v57 offset:6144\n\t"
        "v_mfma_f32_16x16x32_bf16 v[88:91], v[180:183], v[168:171], v[88:91]\n\t"
        "ds_read_b64_tr_b16 v[148:149], v59 offset:8192\n\t"
        "v_mfma_f32_16x16x32_bf16 v[92:95], v[180:183], v[172:175], v[92:95]\n\t"
        "ds_read_b64_tr_b16 v[150:151], v59 offset:9216\n\t"
        "v_mfma_f32_16x16x32_bf16 v[96:99], v[184:187], v[160:163], v[96:99]\n\t"
        "ds_read_b64_tr_b16 v[152:153], v60 offset:8192\n\t"
        "v_mfma_f32_16x16x32_bf16 v[100:103], v[184:187], v[164:167], v[100:103]\n\t"
        "ds_read_b64_tr_b16 v[154:155], v60 offset:9216\n\t"
        "v_mfma_f32_16x16x32_bf16 v[104:107], v[184:187], v[168:171], v[104:107]\n\t"
        "ds_read_b64_tr_b16 v[156:157], v61 offset:8192\n\t"
        "v_mfma_f32_16x16x32_bf16 v[108:111], v[184:187], v[172:175], v[108:111]\n\t"
        "ds_read_b64_tr_b16 v[158:159], v61 offset:9216\n\t"
        "v_mfma_f32_16x16x32_bf16 v[112:115], v[188:191], v[160:163], v[112:115]\n\t"
        "v_add_u32_e32 v56, 0xfffe0000, v56\n\t"
        "v_add_u32_e32 v57, 0xfffe0000, v57\n\t"
        "v_add_u32_e32 v58, 0xfffe0000, v58\n\t"
        "v_mfma_f32_16x16x32_bf16 v[116:119], v[188:191], v[164:167], v[116:119]\n\t"
        "v_add_u32_e32 v59, 0xfffe0000, v59\n\t"
        "v_add_u32_e32 v60, 0xfffe0000, v60\n\t"
        "v_add_u32_e32 v61, 0xfffe0000, v61\n\t"
        "v_mfma_f32_16x16x32_bf16 v[120:123], v[188:191], v[168:171], v[120:123]\n\t"
        "v_mfma_f32_16x16x32_bf16 v[124:127], v[188:191], v[172:175], v[124:127]\n\t"
        "s_waitcnt lgkmcnt(6)\n\t"
        "v_mfma_f32_16x16x32_bf16 v[64:67], v[144:147], v[128:131], v[64:67]\n\t"
        "v_mfma_f32_16x16x32_bf16 v[68:71], v[144:147], v[132:135], v[68:71]\n\t"
        "s_waitcnt lgkmcnt(0)\n\t"
        "s_barrier\n\t"
        "v_mfma_f32_16x16x32_bf16 v[72:75], v[144:147], v[136:139], v[72:75]\n\t"
        "ds_read_b64_tr_b16 v[176:177], v58 offset:0\n\t"
        "v_mfma_f32_16x16x32_bf16 v[76:79], v[144:147], v[140:143], v[76:79]\n\t"
        "ds_read_b64_tr_b16 v[178:179], v58 offset:1024\n\t"
        "v_mfma_f32_16x16x32_bf16 v[80:83], v[148:151], v[128:131], v[80:83]\n\t"
        "ds_read_b128 v[160:163], v56 offset:0\n\t"
        "v_mfma_f32_16x16x32_bf16 v[84:87], v[148:151], v[132:135], v[84:87]\n\t"
        "ds_read_b128 v[164:167], v56 offset:2048\n\t"
        "v_mfma_f32_16x16x32_bf16 v[88:91], v[148:151], v[136:139], v[88:91]\n\t"
        "ds_read_b128 v[168:171], v56 offset:4096\n\t"
        "v_mfma_f32_16x16x32_bf16 v[92:95], v[148:151], v[140:143], v[92:95]\n\t"
        "ds_read_b128 v[172:175], v56 offset:6144\n\t"
        "v_mfma_f32_16x16x32_bf16 v[96:99], v[152:155], v[128:131], v[96:99]\n\t"
        "ds_read_b64_tr_b16 v[180:181], v59 offset:0\n\t"
        "v_mfma_f32_16x16x32_bf16 v[100:103], v[152:155], v[132:135], v[100:103]\n\t"
        "ds_read_b64_tr_b16 v[182:183], v59 offset:1024\n\t"
        "v_mfma_f32_16x16x32_bf16 v[104:107], v[152:155], v[136:139], v[104:107]\n\t"
        "ds_read_b64_tr_b16 v[184:185], v60 offset:0\n\t"
        "v_mfma_f32_16x16x32_bf16 v[108:111], v[152:155], v[140:143], v[108:111]\n\t"
        "ds_read_b64_tr_b16 v[186:187], v60 offset:1024\n\t"
        "v_mfma_f32_16x16x32_bf16 v[112:115], v[156:159], v[128:131], v[112:115]\n\t"
        "ds_read_b64_tr_b16 v[188:189], v61 offset:0\n\t"
        "v_mfma_f32_16x16x32_bf16 v[116:119], v[156:159], v[132:135], v[116:119]\n\t"
        "ds_read_b64_tr_b16 v[190:191], v61 offset:1024\n\t"
        "v_mfma_f32_16x16x32_bf16 v[120:123], v[156:159], v[136:139], v[120:123]\n\t"
        "v_mfma_f32_16x16x32_bf16 v[124:127], v[156:159], v[140:143], v[124:127]\n\t"
        "s_waitcnt lgkmcnt(6)\n\t"
        "v_mfma_f32_16x16x32_bf16 v[64:67], v[176:179], v[160:163], v[64:67]\n\t"
        "ds_read_b64_tr_b16 v[144:145], v58 offset:8192\n\t"
        "v_mfma_f32_16x16x32_bf16 v[68:71], v[176:179], v[164:167], v[68:71]\n\t"
        "ds_read_b64_tr_b16 v[146:147], v58 offset:9216\n\t"
        "v_mfma_f32_16x16x32_bf16 v[72:75], v[176:179], v[168:171], v[72:75]\n\t"
        "ds_read_b128 v[128:131], v57 offset:0\n\t"
        "v_mfma_f32_16x16x32_bf16 v[76:79], v[176:179], v[172:175], v[76:79]\n\t"
        "ds_read_b128 v[132:135], v57 offset:2048\n\t"
        "s_waitcnt lgkmcnt(4)\n\t"
        "v_mfma_f32_16x16x32_bf16 v[80:83], v[180:183], v[160:163], v[80:83]\n\t"
        "ds_read_b128 v[136:139], v57 offset:4096\n\t"
        "v_mfma_f32_16x16x32_bf16 v[84:87], v[180:183], v[164:167], v[84:87]\n\t"
        "ds_read_b128 v[140:143], v57 offset:6144\n\t"
        "v_mfma_f32_16x16x32_bf16 v[88:91], v[180:183], v[168:171], v[88:91]\n\t"
        "ds_read_b64_tr_b16 v[148:149], v59 offset:8192\n\t"
        "v_mfma_f32_16x16x32_bf16 v[92:95], v[180:183], v[172:175], v[92:95]\n\t"
        "ds_read_b64_tr_b16 v[150:151], v59 offset:9216\n\t"
        "v_mfma_f32_16x16x32_bf16 v[96:99], v[184:187], v[160:163], v[96:99]\n\t"
        "ds_read_b64_tr_b16 v[152:153], v60 offset:8192\n\t"
        "v_mfma_f32_16x16x32_bf16 v[100:103], v[184:187], v[164:167], v[100:103]\n\t"
        "ds_read_b64_tr_b16 v[154:155], v60 offset:9216\n\t"
        "v_mfma_f32_16x16x32_bf16 v[104:107], v[184:187], v[168:171], v[104:107]\n\t"
        "ds_read_b64_tr_b16 v[156:157], v61 offset:8192\n\t"
        "v_mfma_f32_16x16x32_bf16 v[108:111], v[184:187], v[172:175], v[108:111]\n\t"
        "ds_read_b64_tr_b16 v[158:159], v61 offset:9216\n\t"
        "v_mfma_f32_16x16x32_bf16 v[112:115], v[188:191], v[160:163], v[112:115]\n\t"
        "v_mfma_f32_16x16x32_bf16 v[116:119], v[188:191], v[164:167], v[116:119]\n\t"
        "v_mfma_f32_16x16x32_bf16 v[120:123], v[188:191], v[168:171], v[120:123]\n\t"
        "v_mfma_f32_16x16x32_bf16 v[124:127], v[188:191], v[172:175], v[124:127]\n\t"
        "s_waitcnt lgkmcnt(6)\n\t"
        "v_mfma_f32_16x16x32_bf16 v[64:67], v[144:147], v[128:131], v[64:67]\n\t"
        "v_mfma_f32_16x16x32_bf16 v[68:71], v[144:147], v[132:135], v[68:71]\n\t"
        "s_waitcnt lgkmcnt(0)\n\t"
        "s_barrier\n\t"
        "v_mfma_f32_16x16x32_bf16 v[72:75], v[144:147], v[136:139], v[72:75]\n\t"
        "ds_read_b64_tr_b16 v[176:177], v58 offset:32768\n\t"
        "v_mfma_f32_16x16x32_bf16 v[76:79], v[144:147], v[140:143], v[76:79]\n\t"
        "ds_read_b64_tr_b16 v[178:179], v58 offset:33792\n\t"
        "v_mfma_f32_16x16x32_bf16 v[80:83], v[148:151], v[128:131], v[80:83]\n\t"
        "ds_read_b128 v[160:163], v56 offset:32768\n\t"
        "v_mfma_f32_16x16x32_bf16 v[84:87], v[148:151], v[132:135], v[84:87]\n\t"
        "ds_read_b128 v[164:167], v56 offset:34816\n\t"
        "v_mfma_f32_16x16x32_bf16 v[88:91], v[148:151], v[136:139], v[88:91]\n\t"
        "ds_read_b128 v[168:171], v56 offset:36864\n\t"
        "v_mfma_f32_16x16x32_bf16 v[92:95], v[148:151], v[140:143], v[92:95]\n\t"
        "ds_read_b128 v[172:175], v56 offset:38912\n\t"
        "v_mfma_f32_16x16x32_bf16 v[96:99], v[152:155], v[128:131], v[96:99]\n\t"
        "ds_read_b64_tr_b16 v[180:181], v59 offset:32768\n\t"
        "v_mfma_f32_16x16x32_bf16 v[100:103], v[152:155], v[132:135], v[100:103]\n\t"
        "ds_read_b64_tr_b16 v[182:183], v59 offset:33792\n\t"
        "v_mfma_f32_16x16x32_bf16 v[104:107], v[152:155], v[136:139], v[104:107]\n\t"
        "ds_read_b64_tr_b16 v[184:185], v60 offset:32768\n\t"
        "v_mfma_f32_16x16x32_bf16 v[108:111], v[152:155], v[140:143], v[108:111]\n\t"
        "ds_read_b64_tr_b16 v[186:187], v60 offset:33792\n\t"
        "v_mfma_f32_16x16x32_bf16 v[112:115], v[156:159], v[128:131], v[112:115]\n\t"
        "ds_read_b64_tr_b16 v[188:189], v61 offset:32768\n\t"
        "v_mfma_f32_16x16x32_bf16 v[116:119], v[156:159], v[132:135], v[116:119]\n\t"
        "ds_read_b64_tr_b16 v[190:191], v61 offset:33792\n\t"
        "v_mfma_f32_16x16x32_bf16 v[120:123], v[156:159], v[136:139], v[120:123]\n\t"
        "v_mfma_f32_16x16x32_bf16 v[124:127], v[156:159], v[140:143], v[124:127]\n\t"
        "s_waitcnt lgkmcnt(6)\n\t"
        "v_mfma_f32_16x16x32_bf16 v[64:67], v[176:179], v[160:163], v[64:67]\n\t"
        "ds_read_b64_tr_b16 v[144:145], v58 offset:40960\n\t"
        "v_mfma_f32_16x16x32_bf16 v[68:71], v[176:179], v[164:167], v[68:71]\n\t"
        "ds_read_b64_tr_b16 v[146:147], v58 offset:41984\n\t"
        "v_mfma_f32_16x16x32_bf16 v[72:75], v[176:179], v[168:171], v[72:75]\n\t"
        "ds_read_b128 v[128:131], v57 offset:32768\n\t"
        "v_mfma_f32_16x16x32_bf16 v[76:79], v[176:179], v[172:175], v[76:79]\n\t"
        "ds_read_b128 v[132:135], v57 offset:34816\n\t"
        "s_waitcnt lgkmcnt(4)\n\t"
        "v_mfma_f32_16x16x32_bf16 v[80:83], v[180:183], v[160:163], v[80:83]\n\t"
        "ds_read_b128 v[136:139], v57 offset:36864\n\t"
        "v_mfma_f32_16x16x32_bf16 v[84:87], v[180:183], v[164:167], v[84:87]\n\t"
        "ds_read_b128 v[140:143], v57 offset:38912\n\t"
        "v_mfma_f32_16x16x32_bf16 v[88:91], v[180:183], v[168:171], v[88:91]\n\t"
        "ds_read_b64_tr_b16 v[148:149], v59 offset:40960\n\t"
        "v_mfma_f32_16x16x32_bf16 v[92:95], v[180:183], v[172:175], v[92:95]\n\t"
        "ds_read_b64_tr_b16 v[150:151], v59 offset:41984\n\t"
        "v_mfma_f32_16x16x32_bf16 v[96:99], v[184:187], v[160:163], v[96:99]\n\t"
        "ds_read_b64_tr_b16 v[152:153], v60 offset:40960\n\t"
        "v_mfma_f32_16x16x32_bf16 v[100:103], v[184:187], v[164:167], v[100:103]\n\t"
        "ds_read_b64_tr_b16 v[154:155], v60 offset:41984\n\t"
        "v_mfma_f32_16x16x32_bf16 v[104:107], v[184:187], v[168:171], v[104:107]\n\t"
        "ds_read_b64_tr_b16 v[156:157], v61 offset:40960\n\t"
        "v_mfma_f32_16x16x32_bf16 v[108:111], v[184:187], v[172:175], v[108:111]\n\t"
        "ds_read_b64_tr_b16 v[158:159], v61 offset:41984\n\t"
        "v_mfma_f32_16x16x32_bf16 v[112:115], v[188:191], v[160:163], v[112:115]\n\t"
        "v_add_u32_e32 v56, 0x10000, v56\n\t"
        "v_add_u32_e32 v57, 0x10000, v57\n\t"
        "v_add_u32_e32 v58, 0x10000, v58\n\t"
        "v_mfma_f32_16x16x32_bf16 v[116:119], v[188:191], v[164:167], v[116:119]\n\t"
        "v_add_u32_e32 v59, 0x10000, v59\n\t"
        "v_add_u32_e32 v60, 0x10000, v60\n\t"
        "v_add_u32_e32 v61, 0x10000, v61\n\t"
        "v_mfma_f32_16x16x32_bf16 v[120:123], v[188:191], v[168:171], v[120:123]\n\t"
        "v_mfma_f32_16x16x32_bf16 v[124:127], v[188:191], v[172:175], v[124:127]\n\t"
        "s_waitcnt lgkmcnt(6)\n\t"
        "v_mfma_f32_16x16x32_bf16 v[64:67], v[144:147], v[128:131], v[64:67]\n\t"
        "v_mfma_f32_16x16x32_bf16 v[68:71], v[144:147], v[132:135], v[68:71]\n\t"
        "s_waitcnt lgkmcnt(0)\n\t"
        "s_barrier\n\t"
        "v_mfma_f32_16x16x32_bf16 v[72:75], v[144:147], v[136:139], v[72:75]\n\t"
        "ds_read_b64_tr_b16 v[176:177], v58 offset:0\n\t"
        "v_mfma_f32_16x16x32_bf16 v[76:79], v[144:147], v[140:143], v[76:79]\n\t"
        "ds_read_b64_tr_b16 v[178:179], v58 offset:1024\n\t"
        "v_mfma_f32_16x16x32_bf16 v[80:83], v[148:151], v[128:131], v[80:83]\n\t"
        "ds_read_b128 v[160:163], v56 offset:0\n\t"
        "v_mfma_f32_16x16x32_bf16 v[84:87], v[148:151], v[132:135], v[84:87]\n\t"
        "ds_read_b128 v[164:167], v56 offset:2048\n\t"
        "v_mfma_f32_16x16x32_bf16 v[88:91], v[148:151], v[136:139], v[88:91]\n\t"
        "ds_read_b128 v[168:171], v56 offset:4096\n\t"
        "v_mfma_f32_16x16x32_bf16 v[92:95], v[148:151], v[140:143], v[92:95]\n\t"
        "ds_read_b128 v[172:175], v56 offset:6144\n\t"
        "v_mfma_f32_16x16x32_bf16 v[96:99], v[152:155], v[128:131], v[96:99]\n\t"
        "ds_read_b64_tr_b16 v[180:181], v59 offset:0\n\t"
        "v_mfma_f32_16x16x32_bf16 v[100:103], v[152:155], v[132:135], v[100:103]\n\t"
        "ds_read_b64_tr_b16 v[182:183], v59 offset:1024\n\t"
        "v_mfma_f32_16x16x32_bf16 v[104:107], v[152:155], v[136:139], v[104:107]\n\t"
        "ds_read_b64_tr_b16 v[184:185], v60 offset:0\n\t"
        "v_mfma_f32_16x16x32_bf16 v[108:111], v[152:155], v[140:143], v[108:111]\n\t"
        "ds_read_b64_tr_b16 v[186:187], v60 offset:1024\n\t"
        "v_mfma_f32_16x16x32_bf16 v[112:115], v[156:159], v[128:131], v[112:115]\n\t"
        "ds_read_b64_tr_b16 v[188:189], v61 offset:0\n\t"
        "v_mfma_f32_16x16x32_bf16 v[116:119], v[156:159], v[132:135], v[116:119]\n\t"
        "ds_read_b64_tr_b16 v[190:191], v61 offset:1024\n\t"
        "v_mfma_f32_16x16x32_bf16 v[120:123], v[156:159], v[136:139], v[120:123]\n\t"
        "v_mfma_f32_16x16x32_bf16 v[124:127], v[156:159], v[140:143], v[124:127]\n\t"
        "s_waitcnt lgkmcnt(6)\n\t"
        "v_mfma_f32_16x16x32_bf16 v[64:67], v[176:179], v[160:163], v[64:67]\n\t"
        "ds_read_b64_tr_b16 v[144:145], v58 offset:8192\n\t"
        "v_mfma_f32_16x16x32_bf16 v[68:71], v[176:179], v[164:167], v[68:71]\n\t"
        "ds_read_b64_tr_b16 v[146:147], v58 offset:9216\n\t"
        "v_mfma_f32_16x16x32_bf16 v[72:75], v[176:179], v[168:171], v[72:75]\n\t"
        "ds_read_b128 v[128:131], v57 offset:0\n\t"
        "v_mfma_f32_16x16x32_bf16 v[76:79], v[176:179], v[172:175], v[76:79]\n\t"
        "ds_read_b128 v[132:135], v57 offset:2048\n\t"
        "s_waitcnt lgkmcnt(4)\n\t"
        "v_mfma_f32_16x16x32_bf16 v[80:83], v[180:183], v[160:163], v[80:83]\n\t"
        "ds_read_b128 v[136:139], v57 offset:4096\n\t"
        "v_mfma_f32_16x16x32_bf16 v[84:87], v[180:183], v[164:167], v[84:87]\n\t"
        "ds_read_b128 v[140:143], v57 offset:6144\n\t"
        "v_mfma_f32_16x16x32_bf16 v[88:91], v[180:183], v[168:171], v[88:91]\n\t"
        "ds_read_b64_tr_b16 v[148:149], v59 offset:8192\n\t"
        "v_mfma_f32_16x16x32_bf16 v[92:95], v[180:183], v[172:175], v[92:95]\n\t"
        "ds_read_b64_tr_b16 v[150:151], v59 offset:9216\n\t"
        "v_mfma_f32_16x16x32_bf16 v[96:99], v[184:187], v[160:163], v[96:99]\n\t"
        "ds_read_b64_tr_b16 v[152:153], v60 offset:8192\n\t"
        "v_mfma_f32_16x16x32_bf16 v[100:103], v[184:187], v[164:167], v[100:103]\n\t"
        "ds_read_b64_tr_b16 v[154:155], v60 offset:9216\n\t"
        "v_mfma_f32_16x16x32_bf16 v[104:107], v[184:187], v[168:171], v[104:107]\n\t"
        "ds_read_b64_tr_b16 v[156:157], v61 offset:8192\n\t"
        "v_mfma_f32_16x16x32_bf16 v[108:111], v[184:187], v[172:175], v[108:111]\n\t"
        "ds_read_b64_tr_b16 v[158:159], v61 offset:9216\n\t"
        "v_mfma_f32_16x16x32_bf16 v[112:115], v[188:191], v[160:163], v[112:115]\n\t"
        "v_mfma_f32_16x16x32_bf16 v[116:119], v[188:191], v[164:167], v[116:119]\n\t"
        "v_mfma_f32_16x16x32_bf16 v[120:123], v[188:191], v[168:171], v[120:123]\n\t"
        "v_mfma_f32_16x16x32_bf16 v[124:127], v[188:191], v[172:175], v[124:127]\n\t"
        "s_waitcnt lgkmcnt(6)\n\t"
        "v_mfma_f32_16x16x32_bf16 v[64:67], v[144:147], v[128:131], v[64:67]\n\t"
        "v_mfma_f32_16x16x32_bf16 v[68:71], v[144:147], v[132:135], v[68:71]\n\t"
        "s_waitcnt lgkmcnt(0)\n\t"
        "s_barrier\n\t"
        "v_mfma_f32_16x16x32_bf16 v[72:75], v[144:147], v[136:139], v[72:75]\n\t"
        "ds_read_b64_tr_b16 v[176:177], v58 offset:32768\n\t"
        "v_mfma_f32_16x16x32_bf16 v[76:79], v[144:147], v[140:143], v[76:79]\n\t"
        "ds_read_b64_tr_b16 v[178:179], v58 offset:33792\n\t"
        "v_mfma_f32_16x16x32_bf16 v[80:83], v[148:151], v[128:131], v[80:83]\n\t"
        "ds_read_b128 v[160:163], v56 offset:32768\n\t"
        "v_mfma_f32_16x16x32_bf16 v[84:87], v[148:151], v[132:135], v[84:87]\n\t"
        "ds_read_b128 v[164:167], v56 offset:34816\n\t"
        "v_mfma_f32_16x16x32_bf16 v[88:91], v[148:151], v[136:139], v[88:91]\n\t"
        "ds_read_b128 v[168:171], v56 offset:36864\n\t"
        "v_mfma_f32_16x16x32_bf16 v[92:95], v[148:151], v[140:143], v[92:95]\n\t"
        "ds_read_b128 v[172:175], v56 offset:38912\n\t"
        "v_mfma_f32_16x16x32_bf16 v[96:99], v[152:155], v[128:131], v[96:99]\n\t"
        "ds_read_b64_tr_b16 v[180:181], v59 offset:32768\n\t"
        "v_mfma_f32_16x16x32_bf16 v[100:103], v[152:155], v[132:135], v[100:103]\n\t"
        "ds_read_b64_tr_b16 v[182:183], v59 offset:33792\n\t"
        "v_mfma_f32_16x16x32_bf16 v[104:107], v[152:155], v[136:139], v[104:107]\n\t"
        "ds_read_b64_tr_b16 v[184:185], v60 offset:32768\n\t"
        "v_mfma_f32_16x16x32_bf16 v[108:111], v[152:155], v[140:143], v[108:111]\n\t"
        "ds_read_b64_tr_b16 v[186:187], v60 offset:33792\n\t"
        "v_mfma_f32_16x16x32_bf16 v[112:115], v[156:159], v[128:131], v[112:115]\n\t"
        "ds_read_b64_tr_b16 v[188:189], v61 offset:32768\n\t"
        "v_mfma_f32_16x16x32_bf16 v[116:119], v[156:159], v[132:135], v[116:119]\n\t"
        "ds_read_b64_tr_b16 v[190:191], v61 offset:33792\n\t"
        "v_mfma_f32_16x16x32_bf16 v[120:123], v[156:159], v[136:139], v[120:123]\n\t"
        "v_mfma_f32_16x16x32_bf16 v[124:127], v[156:159], v[140:143], v[124:127]\n\t"
        "s_waitcnt lgkmcnt(6)\n\t"
        "v_mfma_f32_16x16x32_bf16 v[64:67], v[176:179], v[160:163], v[64:67]\n\t"
        "ds_read_b64_tr_b16 v[144:145], v58 offset:40960\n\t"
        "v_mfma_f32_16x16x32_bf16 v[68:71], v[176:179], v[164:167], v[68:71]\n\t"
        "ds_read_b64_tr_b16 v[146:147], v58 offset:41984\n\t"
        "v_mfma_f32_16x16x32_bf16 v[72:75], v[176:179], v[168:171], v[72:75]\n\t"
        "ds_read_b128 v[128:131], v57 offset:32768\n\t"
        "v_mfma_f32_16x16x32_bf16 v[76:79], v[176:179], v[172:175], v[76:79]\n\t"
        "ds_read_b128 v[132:135], v57 offset:34816\n\t"
        "s_waitcnt lgkmcnt(4)\n\t"
        "v_mfma_f32_16x16x32_bf16 v[80:83], v[180:183], v[160:163], v[80:83]\n\t"
        "ds_read_b128 v[136:139], v57 offset:36864\n\t"
        "v_mfma_f32_16x16x32_bf16 v[84:87], v[180:183], v[164:167], v[84:87]\n\t"
        "ds_read_b128 v[140:143], v57 offset:38912\n\t"
        "v_mfma_f32_16x16x32_bf16 v[88:91], v[180:183], v[168:171], v[88:91]\n\t"
        "ds_read_b64_tr_b16 v[148:149], v59 offset:40960\n\t"
        "v_mfma_f32_16x16x32_bf16 v[92:95], v[180:183], v[172:175], v[92:95]\n\t"
        "ds_read_b64_tr_b16 v[150:151], v59 offset:41984\n\t"
        "v_mfma_f32_16x16x32_bf16 v[96:99], v[184:187], v[160:163], v[96:99]\n\t"
        "ds_read_b64_tr_b16 v[152:153], v60 offset:40960\n\t"
        "v_mfma_f32_16x16x32_bf16 v[100:103], v[184:187], v[164:167], v[100:103]\n\t"
        "ds_read_b64_tr_b16 v[154:155], v60 offset:41984\n\t"
        "v_mfma_f32_16x16x32_bf16 v[104:107], v[184:187], v[168:171], v[104:107]\n\t"
        "ds_read_b64_tr_b16 v[156:157], v61 offset:40960\n\t"
        "v_mfma_f32_16x16x32_bf16 v[108:111], v[184:187], v[172:175], v[108:111]\n\t"
        "ds_read_b64_tr_b16 v[158:159], v61 offset:41984\n\t"
        "v_mfma_f32_16x16x32_bf16 v[112:115], v[188:191], v[160:163], v[112:115]\n\t"
        "v_add_u32_e32 v56, 0x10000, v56\n\t"
        "v_add_u32_e32 v57, 0x10000, v57\n\t"
        "v_add_u32_e32 v58, 0x10000, v58\n\t"
        "v_mfma_f32_16x16x32_bf16 v[116:119], v[188:191], v[164:167], v[116:119]\n\t"
        "v_add_u32_e32 v59, 0x10000, v59\n\t"
        "v_add_u32_e32 v60, 0x10000, v60\n\t"
        "v_add_u32_e32 v61, 0x10000, v61\n\t"
        "v_mfma_f32_16x16x32_bf16 v[120:123], v[188:191], v[168:171], v[120:123]\n\t"
        "v_mfma_f32_16x16x32_bf16 v[124:127], v[188:191], v[172:175], v[124:127]\n\t"
        "s_waitcnt lgkmcnt(6)\n\t"
        "v_mfma_f32_16x16x32_bf16 v[64:67], v[144:147], v[128:131], v[64:67]\n\t"
        "ds_read_b64_tr_b16 v[176:177], v58 offset:0\n\t"
        "v_mfma_f32_16x16x32_bf16 v[68:71], v[144:147], v[132:135], v[68:71]\n\t"
        "ds_read_b64_tr_b16 v[178:179], v58 offset:1024\n\t"
        "v_mfma_f32_16x16x32_bf16 v[72:75], v[144:147], v[136:139], v[72:75]\n\t"
        "ds_read_b128 v[160:163], v56 offset:0\n\t"
        "v_mfma_f32_16x16x32_bf16 v[76:79], v[144:147], v[140:143], v[76:79]\n\t"
        "ds_read_b128 v[164:167], v56 offset:2048\n\t"
        "s_waitcnt lgkmcnt(4)\n\t"
        "v_mfma_f32_16x16x32_bf16 v[80:83], v[148:151], v[128:131], v[80:83]\n\t"
        "ds_read_b128 v[168:171], v56 offset:4096\n\t"
        "v_mfma_f32_16x16x32_bf16 v[84:87], v[148:151], v[132:135], v[84:87]\n\t"
        "ds_read_b128 v[172:175], v56 offset:6144\n\t"
        "v_mfma_f32_16x16x32_bf16 v[88:91], v[148:151], v[136:139], v[88:91]\n\t"
        "ds_read_b64_tr_b16 v[180:181], v59 offset:0\n\t"
        "v_mfma_f32_16x16x32_bf16 v[92:95], v[148:151], v[140:143], v[92:95]\n\t"
        "ds_read_b64_tr_b16 v[182:183], v59 offset:1024\n\t"
        "v_mfma_f32_16x16x32_bf16 v[96:99], v[152:155], v[128:131], v[96:99]\n\t"
        "ds_read_b64_tr_b16 v[184:185], v60 offset:0\n\t"
        "v_mfma_f32_16x16x32_bf16 v[100:103], v[152:155], v[132:135], v[100:103]\n\t"
        "ds_read_b64_tr_b16 v[186:187], v60 offset:1024\n\t"
        "v_mfma_f32_16x16x32_bf16 v[104:107], v[152:155], v[136:139], v[104:107]\n\t"
        "ds_read_b64_tr_b16 v[188:189], v61 offset:0\n\t"
        "v_mfma_f32_16x16x32_bf16 v[108:111], v[152:155], v[140:143], v[108:111]\n\t"
        "ds_read_b64_tr_b16 v[190:191], v61 offset:1024\n\t"
        "v_mfma_f32_16x16x32_bf16 v[112:115], v[156:159], v[128:131], v[112:115]\n\t"
        "v_mfma_f32_16x16x32_bf16 v[116:119], v[156:159], v[132:135], v[116:119]\n\t"
        "v_mfma_f32_16x16x32_bf16 v[120:123], v[156:159], v[136:139], v[120:123]\n\t"
        "v_mfma_f32_16x16x32_bf16 v[124:127], v[156:159], v[140:143], v[124:127]\n\t"
        "s_waitcnt lgkmcnt(6)\n\t"
        "v_mfma_f32_16x16x32_bf16 v[64:67], v[176:179], v[160:163], v[64:67]\n\t"
        "ds_read_b64_tr_b16 v[144:145], v58 offset:8192\n\t"
        "v_mfma_f32_16x16x32_bf16 v[68:71], v[176:179], v[164:167], v[68:71]\n\t"
        "ds_read_b64_tr_b16 v[146:147], v58 offset:9216\n\t"
        "v_mfma_f32_16x16x32_bf16 v[72:75], v[176:179], v[168:171], v[72:75]\n\t"
        "ds_read_b128 v[128:131], v57 offset:0\n\t"
        "v_mfma_f32_16x16x32_bf16 v[76:79], v[176:179], v[172:175], v[76:79]\n\t"
        "ds_read_b128 v[132:135], v57 offset:2048\n\t"
        "s_waitcnt lgkmcnt(4)\n\t"
        "v_mfma_f32_16x16x32_bf16 v[80:83], v[180:183], v[160:163], v[80:83]\n\t"
        "ds_read_b128 v[136:139], v57 offset:4096\n\t"
        "v_mfma_f32_16x16x32_bf16 v[84:87], v[180:183], v[164:167], v[84:87]\n\t"
        "ds_read_b128 v[140:143], v57 offset:6144\n\t"
        "v_mfma_f32_16x16x32_bf16 v[88:91], v[180:183], v[168:171], v[88:91]\n\t"
        "ds_read_b64_tr_b16 v[148:149], v59 offset:8192\n\t"
        "v_mfma_f32_16x16x32_bf16 v[92:95], v[180:183], v[172:175], v[92:95]\n\t"
        "ds_read_b64_tr_b16 v[150:151], v59 offset:9216\n\t"
        "v_mfma_f32_16x16x32_bf16 v[96:99], v[184:187], v[160:163], v[96:99]\n\t"
        "ds_read_b64_tr_b16 v[152:153], v60 offset:8192\n\t"
        "v_mfma_f32_16x16x32_bf16 v[100:103], v[184:187], v[164:167], v[100:103]\n\t"
        "ds_read_b64_tr_b16 v[154:155], v60 offset:9216\n\t"
        "v_mfma_f32_16x16x32_bf16 v[104:107], v[184:187], v[168:171], v[104:107]\n\t"
        "ds_read_b64_tr_b16 v[156:157], v61 offset:8192\n\t"
        "v_mfma_f32_16x16x32_bf16 v[108:111], v[184:187], v[172:175], v[108:111]\n\t"
        "ds_read_b64_tr_b16 v[158:159], v61 offset:9216\n\t"
        "v_mfma_f32_16x16x32_bf16 v[112:115], v[188:191], v[160:163], v[112:115]\n\t"
        "v_mfma_f32_16x16x32_bf16 v[116:119], v[188:191], v[164:167], v[116:119]\n\t"
        "v_mfma_f32_16x16x32_bf16 v[120:123], v[188:191], v[168:171], v[120:123]\n\t"
        "v_mfma_f32_16x16x32_bf16 v[124:127], v[188:191], v[172:175], v[124:127]\n\t"
        "s_waitcnt lgkmcnt(6)\n\t"
        "v_mfma_f32_16x16x32_bf16 v[64:67], v[144:147], v[128:131], v[64:67]\n\t"
        "v_mfma_f32_16x16x32_bf16 v[68:71], v[144:147], v[132:135], v[68:71]\n\t"
        "v_mfma_f32_16x16x32_bf16 v[72:75], v[144:147], v[136:139], v[72:75]\n\t"
        "v_mfma_f32_16x16x32_bf16 v[76:79], v[144:147], v[140:143], v[76:79]\n\t"
        "s_waitcnt lgkmcnt(0)\n\t"
        "v_mfma_f32_16x16x32_bf16 v[80:83], v[148:151], v[128:131], v[80:83]\n\t"
        "v_mfma_f32_16x16x32_bf16 v[84:87], v[148:151], v[132:135], v[84:87]\n\t"
        "v_mfma_f32_16x16x32_bf16 v[88:91], v[148:151], v[136:139], v[88:91]\n\t"
        "v_mfma_f32_16x16x32_bf16 v[92:95], v[148:151], v[140:143], v[92:95]\n\t"
        "v_mfma_f32_16x16x32_bf16 v[96:99], v[152:155], v[128:131], v[96:99]\n\t"
        "v_cvt_pk_bf16_f32 v160, v64, v65\n\t"
        "v_cvt_pk_bf16_f32 v161, v66, v67\n\t"
        "v_mfma_f32_16x16x32_bf16 v[100:103], v[152:155], v[132:135], v[100:103]\n\t"
        "v_cvt_pk_bf16_f32 v162, v68, v69\n\t"
        "v_cvt_pk_bf16_f32 v163, v70, v71\n\t"
        "v_mfma_f32_16x16x32_bf16 v[104:107], v[152:155], v[136:139], v[104:107]\n\t"
        "v_cvt_pk_bf16_f32 v164, v72, v73\n\t"
        "v_cvt_pk_bf16_f32 v165, v74, v75\n\t"
        "v_mfma_f32_16x16x32_bf16 v[108:111], v[152:155], v[140:143], v[108:111]\n\t"
        "v_cvt_pk_bf16_f32 v166, v76, v77\n\t"
        "v_cvt_pk_bf16_f32 v167, v78, v79\n\t"
        "v_mfma_f32_16x16x32_bf16 v[112:115], v[156:159], v[128:131], v[112:115]\n\t"
        "v_cvt_pk_bf16_f32 v168, v80, v81\n\t"
        "v_cvt_pk_bf16_f32 v169, v82, v83\n\t"
        "v_mfma_f32_16x16x32_bf16 v[116:119], v[156:159], v[132:135], v[116:119]\n\t"
        "v_cvt_pk_bf16_f32 v170, v84, v85\n\t"
        "v_cvt_pk_bf16_f32 v171, v86, v87\n\t"
        "v_mfma_f32_16x16x32_bf16 v[120:123], v[156:159], v[136:139], v[120:123]\n\t"
        "v_cvt_pk_bf16_f32 v172, v88, v89\n\t"
        "v_cvt_pk_bf16_f32 v173, v90, v91\n\t"
        "v_mfma_f32_16x16x32_bf16 v[124:127], v[156:159], v[140:143], v[124:127]\n\t"
        "v_cvt_pk_bf16_f32 v174, v92, v93\n\t"
        "v_cvt_pk_bf16_f32 v175, v94, v95\n\t"
        "s_branch .Lge_%=\n\t"
        ".Lgv_%=:\n\t"
        "ds_read_b64_tr_b16 v[176:177], v58 offset:0\n\t"
        "ds_read_b64_tr_b16 v[178:179], v58 offset:1024\n\t"
        "ds_read_b128 v[160:163], v56 offset:0\n\t"
        "ds_read_b128 v[164:167], v56 offset:2048\n\t"
        "ds_read_b128 v[168:171], v56 offset:4096\n\t"
        "ds_read_b128 v[172:175], v56 offset:6144\n\t"
        "ds_read_b64_tr_b16 v[180:181], v59 offset:0\n\t"
        "ds_read_b64_tr_b16 v[182:183], v59 offset:1024\n\t"
        "ds_read_b64_tr_b16 v[184:185], v60 offset:0\n\t"
        "ds_read_b64_tr_b16 v[186:187], v60 offset:1024\n\t"
        "ds_read_b64_tr_b16 v[188:189], v61 offset:0\n\t"
        "ds_read_b64_tr_b16 v[190:191], v61 offset:1024\n\t"
        "s_waitcnt lgkmcnt(6)\n\t"
        "v_mfma_f32_16x16x32_bf16 v[64:67], v[160:163], v[176:179], 0\n\t"
        "ds_read_b64_tr_b16 v[144:145], v58 offset:8192\n\t"
        "v_mfma_f32_16x16x32_bf16 v[68:71], v[164:167], v[176:179], 0\n\t"
        "ds_read_b64_tr_b16 v[146:147], v58 offset:9216\n\t"
        "v_mfma_f32_16x16x32_bf16 v[72:75], v[168:171], v[176:179], 0\n\t"
        "ds_read_b128 v[128:131], v57 offset:0\n\t"
        "v_mfma_f32_16x16x32_bf16 v[76:79], v[172:175], v[176:179], 0\n\t"
        "ds_read_b128 v[132:135], v57 offset:2048\n\t"
        "s_waitcnt lgkmcnt(4)\n\t"
        "v_mfma_f32_16x16x32_bf16 v[80:83], v[160:163], v[180:183], 0\n\t"
        "ds_read_b128 v[136:139], v57 offset:4096\n\t"
        "v_mfma_f32_16x16x32_bf16 v[84:87], v[164:167], v[180:183], 0\n\t"
        "ds_read_b128 v[140:143], v57 offset:6144\n\t"
        "v_mfma_f32_16x16x32_bf16 v[88:91], v[168:171], v[180:183], 0\n\t"
        "ds_read_b64_tr_b16 v[148:149], v59 offset:8192\n\t"
        "v_mfma_f32_16x16x32_bf16 v[92:95], v[172:175], v[180:183], 0\n\t"
        "ds_read_b64_tr_b16 v[150:151], v59 offset:9216\n\t"
        "s_waitcnt vmcnt(8)\n\t"
        "s_barrier\n\t"
        "v_mfma_f32_16x16x32_bf16 v[96:99], v[160:163], v[184:187], 0\n\t"
        "ds_read_b64_tr_b16 v[152:153], v60 offset:8192\n\t"
        "v_mfma_f32_16x16x32_bf16 v[100:103], v[164:167], v[184:187], 0\n\t"
        "ds_read_b64_tr_b16 v[154:155], v60 offset:9216\n\t"
        "v_mfma_f32_16x16x32_bf16 v[104:107], v[168:171], v[184:187], 0\n\t"
        "ds_read_b64_tr_b16 v[156:157], v61 offset:8192\n\t"
        "v_mfma_f32_16x16x32_bf16 v[108:111], v[172:175], v[184:187], 0\n\t"
        "ds_read_b64_tr_b16 v[158:159], v61 offset:9216\n\t"
        "v_mfma_f32_16x16x32_bf16 v[112:115], v[160:163], v[188:191], 0\n\t"
        "v_mfma_f32_16x16x32_bf16 v[116:119], v[164:167], v[188:191], 0\n\t"
        "v_mfma_f32_16x16x32_bf16 v[120:123], v[168:171], v[188:191], 0\n\t"
        "v_mfma_f32_16x16x32_bf16 v[124:127], v[172:175], v[188:191], 0\n\t"
        "s_waitcnt lgkmcnt(6)\n\t"
        "v_mfma_f32_16x16x32_bf16 v[64:67], v[128:131], v[144:147], v[64:67]\n\t"
        "ds_read_b64_tr_b16 v[176:177], v58 offset:32768\n\t"
        "v_mfma_f32_16x16x32_bf16 v[68:71], v[132:135], v[144:147], v[68:71]\n\t"
        "ds_read_b64_tr_b16 v[178:179], v58 offset:33792\n\t"
        "v_mfma_f32_16x16x32_bf16 v[72:75], v[136:139], v[144:147], v[72:75]\n\t"
        "ds_read_b128 v[160:163], v56 offset:32768\n\t"
        "v_mfma_f32_16x16x32_bf16 v[76:79], v[140:143], v[144:147], v[76:79]\n\t"
        "ds_read_b128 v[164:167], v56 offset:34816\n\t"
        "s_waitcnt lgkmcnt(4)\n\t"
        "v_mfma_f32_16x16x32_bf16 v[80:83], v[128:131], v[148:151], v[80:83]\n\t"
        "ds_read_b128 v[168:171], v56 offset:36864\n\t"
        "v_mfma_f32_16x16x32_bf16 v[84:87], v[132:135], v[148:151], v[84:87]\n\t"
        "ds_read_b128 v[172:175], v56 offset:38912\n\t"
        "v_mfma_f32_16x16x32_bf16 v[88:91], v[136:139], v[148:151], v[88:91]\n\t"
        "ds_read_b64_tr_b16 v[180:181], v59 offset:32768\n\t"
        "v_mfma_f32_16x16x32_bf16 v[92:95], v[140:143], v[148:151], v[92:95]\n\t"
        "ds_read_b64_tr_b16 v[182:183], v59 offset:33792\n\t"
        "v_mfma_f32_16x16x32_bf16 v[96:99], v[128:131], v[152:155], v[96:99]\n\t"
        "ds_read_b64_tr_b16 v[184:185], v60 offset:32768\n\t"
        "v_mfma_f32_16x16x32_bf16 v[100:103], v[132:135], v[152:155], v[100:103]\n\t"
        "ds_read_b64_tr_b16 v[186:187], v60 offset:33792\n\t"
        "v_mfma_f32_16x16x32_bf16 v[104:107], v[136:139], v[152:155], v[104:107]\n\t"
        "ds_read_b64_tr_b16 v[188:189], v61 offset:32768\n\t"
        "v_mfma_f32_16x16x32_bf16 v[108:111], v[140:143], v[152:155], v[108:111]\n\t"
        "ds_read_b64_tr_b16 v[190:191], v61 offset:33792\n\t"
        "v_mfma_f32_16x16x32_bf16 v[112:115], v[128:131], v[156:159], v[112:115]\n\t"
        "v_mfma_f32_16x16x32_bf16 v[116:119], v[132:135], v[156:159], v[116:119]\n\t"
        "v_mfma_f32_16x16x32_bf16 v[120:123], v[136:139], v[156:159], v[120:123]\n\t"
        "v_mfma_f32_16x16x32_bf16 v[124:127], v[140:143], v[156:159], v[124:127]\n\t"
        "s_waitcnt lgkmcnt(6)\n\t"
        "v_mfma_f32_16x16x32_bf16 v[64:67], v[160:163], v[176:179], v[64:67]\n\t"
        "ds_read_b64_tr_b16 v[144:145], v58 offset:40960\n\t"
        "v_mfma_f32_16x16x32_bf16 v[68:71], v[164:167], v[176:179], v[68:71]\n\t"
        "ds_read_b64_tr_b16 v[146:147], v58 offset:41984\n\t"
        "v_mfma_f32_16x16x32_bf16 v[72:75], v[168:171], v[176:179], v[72:75]\n\t"
        "ds_read_b128 v[128:131], v57 offset:32768\n\t"
        "v_mfma_f32_16x16x32_bf16 v[76:79], v[172:175], v[176:179], v[76:79]\n\t"
        "ds_read_b128 v[132:135], v57 offset:34816\n\t"
        "s_waitcnt lgkmcnt(4)\n\t"
        "v_mfma_f32_16x16x32_bf16 v[80:83], v[160:163], v[180:183], v[80:83]\n\t"
        "ds_read_b128 v[136:139], v57 offset:36864\n\t"
        "v_mfma_f32_16x16x32_bf16 v[84:87], v[164:167], v[180:183], v[84:87]\n\t"
        "ds_read_b128 v[140:143], v57 offset:38912\n\t"
        "v_mfma_f32_16x16x32_bf16 v[88:91], v[168:171], v[180:183], v[88:91]\n\t"
        "ds_read_b64_tr_b16 v[148:149], v59 offset:40960\n\t"
        "v_mfma_f32_16x16x32_bf16 v[92:95], v[172:175], v[180:183], v[92:95]\n\t"
        "ds_read_b64_tr_b16 v[150:151], v59 offset:41984\n\t"
        "v_mfma_f32_16x16x32_bf16 v[96:99], v[160:163], v[184:187], v[96:99]\n\t"
        "ds_read_b64_tr_b16 v[152:153], v60 offset:40960\n\t"
        "v_mfma_f32_16x16x32_bf16 v[100:103], v[164:167], v[184:187], v[100:103]\n\t"
        "ds_read_b64_tr_b16 v[154:155], v60 offset:41984\n\t"
        "v_mfma_f32_16x16x32_bf16 v[104:107], v[168:171], v[184:187], v[104:107]\n\t"
        "ds_read_b64_tr_b16 v[156:157], v61 offset:40960\n\t"
        "v_mfma_f32_16x16x32_bf16 v[108:111], v[172:175], v[184:187], v[108:111]\n\t"
        "ds_read_b64_tr_b16 v[158:159], v61 offset:41984\n\t"
        "v_mfma_f32_16x16x32_bf16 v[112:115], v[160:163], v[188:191], v[112:115]\n\t"
        "v_add_u32_e32 v56, 0x10000, v56\n\t"
        "v_add_u32_e32 v57, 0x10000, v57\n\t"
        "v_add_u32_e32 v58, 0x10000, v58\n\t"
        "v_mfma_f32_16x16x32_bf16 v[116:119], v[164:167], v[188:191], v[116:119]\n\t"
        "v_add_u32_e32 v59, 0x10000, v59\n\t"
        "v_add_u32_e32 v60, 0x10000, v60\n\t"
        "v_add_u32_e32 v61, 0x10000, v61\n\t"
        "v_mfma_f32_16x16x32_bf16 v[120:123], v[168:171], v[188:191], v[120:123]\n\t"
        "v_mfma_f32_16x16x32_bf16 v[124:127], v[172:175], v[188:191], v[124:127]\n\t"
        "s_waitcnt lgkmcnt(6)\n\t"
        "v_mfma_f32_16x16x32_bf16 v[64:67], v[128:131], v[144:147], v[64:67]\n\t"
        "ds_read_b64_tr_b16 v[176:177], v58 offset:0\n\t"
        "v_mfma_f32_16x16x32_bf16 v[68:71], v[132:135], v[144:147], v[68:71]\n\t"
        "ds_read_b64_tr_b16 v[178:179], v58 offset:1024\n\t"
        "v_mfma_f32_16x16x32_bf16 v[72:75], v[136:139], v[144:147], v[72:75]\n\t"
        "ds_read_b128 v[160:163], v56 offset:0\n\t"
        "v_mfma_f32_16x16x32_bf16 v[76:79], v[140:143], v[144:147], v[76:79]\n\t"
        "ds_read_b128 v[164:167], v56 offset:2048\n\t"
        "s_waitcnt lgkmcnt(4)\n\t"
        "v_mfma_f32_16x16x32_bf16 v[80:83], v[128:131], v[148:151], v[80:83]\n\t"
        "ds_read_b128 v[168:171], v56 offset:4096\n\t"
        "v_mfma_f32_16x16x32_bf16 v[84:87], v[132:135], v[148:151], v[84:87]\n\t"
        "ds_read_b128 v[172:175], v56 offset:6144\n\t"
        "v_mfma_f32_16x16x32_bf16 v[88:91], v[136:139], v[148:151], v[88:91]\n\t"
        "ds_read_b64_tr_b16 v[180:181], v59 offset:0\n\t"
        "v_mfma_f32_16x16x32_bf16 v[92:95], v[140:143], v[148:151], v[92:95]\n\t"
        "ds_read_b64_tr_b16 v[182:183], v59 offset:1024\n\t"
        "v_mfma_f32_16x16x32_bf16 v[96:99], v[128:131], v[152:155], v[96:99]\n\t"
        "ds_read_b64_tr_b16 v[184:185], v60 offset:0\n\t"
        "v_mfma_f32_16x16x32_bf16 v[100:103], v[132:135], v[152:155], v[100:103]\n\t"
        "ds_read_b64_tr_b16 v[186:187], v60 offset:1024\n\t"
        "v_mfma_f32_16x16x32_bf16 v[104:107], v[136:139], v[152:155], v[104:107]\n\t"
        "ds_read_b64_tr_b16 v[188:189], v61 offset:0\n\t"
        "v_mfma_f32_16x16x32_bf16 v[108:111], v[140:143], v[152:155], v[108:111]\n\t"
        "ds_read_b64_tr_b16 v[190:191], v61 offset:1024\n\t"
        "v_mfma_f32_16x16x32_bf16 v[112:115], v[128:131], v[156:159], v[112:115]\n\t"
        "v_mfma_f32_16x16x32_bf16 v[116:119], v[132:135], v[156:159], v[116:119]\n\t"
        "v_mfma_f32_16x16x32_bf16 v[120:123], v[136:139], v[156:159], v[120:123]\n\t"
        "v_mfma_f32_16x16x32_bf16 v[124:127], v[140:143], v[156:159], v[124:127]\n\t"
        "s_waitcnt lgkmcnt(6)\n\t"
        "v_mfma_f32_16x16x32_bf16 v[64:67], v[160:163], v[176:179], v[64:67]\n\t"
        "ds_read_b64_tr_b16 v[144:145], v58 offset:8192\n\t"
        "v_mfma_f32_16x16x32_bf16 v[68:71], v[164:167], v[176:179], v[68:71]\n\t"
        "ds_read_b64_tr_b16 v[146:147], v58 offset:9216\n\t"
        "v_mfma_f32_16x16x32_bf16 v[72:75], v[168:171], v[176:179], v[72:75]\n\t"
        "ds_read_b128 v[128:131], v57 offset:0\n\t"
        "v_mfma_f32_16x16x32_bf16 v[76:79], v[172:175], v[176:179], v[76:79]\n\t"
        "ds_read_b128 v[132:135], v57 offset:2048\n\t"
        "s_waitcnt lgkmcnt(4)\n\t"
        "v_mfma_f32_16x16x32_bf16 v[80:83], v[160:163], v[180:183], v[80:83]\n\t"
        "ds_read_b128 v[136:139], v57 offset:4096\n\t"
        "v_mfma_f32_16x16x32_bf16 v[84:87], v[164:167], v[180:183], v[84:87]\n\t"
        "ds_read_b128 v[140:143], v57 offset:6144\n\t"
        "v_mfma_f32_16x16x32_bf16 v[88:91], v[168:171], v[180:183], v[88:91]\n\t"
        "ds_read_b64_tr_b16 v[148:149], v59 offset:8192\n\t"
        "v_mfma_f32_16x16x32_bf16 v[92:95], v[172:175], v[180:183], v[92:95]\n\t"
        "ds_read_b64_tr_b16 v[150:151], v59 offset:9216\n\t"
        "v_mfma_f32_16x16x32_bf16 v[96:99], v[160:163], v[184:187], v[96:99]\n\t"
        "ds_read_b64_tr_b16 v[152:153], v60 offset:8192\n\t"
        "v_mfma_f32_16x16x32_bf16 v[100:103], v[164:167], v[184:187], v[100:103]\n\t"
        "ds_read_b64_tr_b16 v[154:155], v60 offset:9216\n\t"
        "v_mfma_f32_16x16x32_bf16 v[104:107], v[168:171], v[184:187], v[104:107]\n\t"
        "ds_read_b64_tr_b16 v[156:157], v61 offset:8192\n\t"
        "v_mfma_f32_16x16x32_bf16 v[108:111], v[172:175], v[184:187], v[108:111]\n\t"
        "ds_read_b64_tr_b16 v[158:159], v61 offset:9216\n\t"
        "v_mfma_f32_16x16x32_bf16 v[112:115], v[160:163], v[188:191], v[112:115]\n\t"
        "v_mfma_f32_16x16x32_bf16 v[116:119], v[164:167], v[188:191], v[116:119]\n\t"
        "v_mfma_f32_16x16x32_bf16 v[120:123], v[168:171], v[188:191], v[120:123]\n\t"
        "v_mfma_f32_16x16x32_bf16 v[124:127], v[172:175], v[188:191], v[124:127]\n\t"
        "s_waitcnt lgkmcnt(6)\n\t"
        "v_mfma_f32_16x16x32_bf16 v[64:67], v[128:131], v[144:147], v[64:67]\n\t"
        "v_mfma_f32_16x16x32_bf16 v[68:71], v[132:135], v[144:147], v[68:71]\n\t"
        "s_waitcnt vmcnt(0) lgkmcnt(0)\n\t"
        "s_barrier\n\t"
        "v_mfma_f32_16x16x32_bf16 v[72:75], v[136:139], v[144:147], v[72:75]\n\t"
        "ds_read_b64_tr_b16 v[176:177], v58 offset:32768\n\t"
        "v_mfma_f32_16x16x32_bf16 v[76:79], v[140:143], v[144:147], v[76:79]\n\t"
        "ds_read_b64_tr_b16 v[178:179], v58 offset:33792\n\t"
        "v_mfma_f32_16x16x32_bf16 v[80:83], v[128:131], v[148:151], v[80:83]\n\t"
        "ds_read_b128 v[160:163], v56 offset:32768\n\t"
        "v_mfma_f32_16x16x32_bf16 v[84:87], v[132:135], v[148:151], v[84:87]\n\t"
        "ds_read_b128 v[164:167], v56 offset:34816\n\t"
        "v_mfma_f32_16x16x32_bf16 v[88:91], v[136:139], v[148:151], v[88:91]\n\t"
        "ds_read_b128 v[168:171], v56 offset:36864\n\t"
        "v_mfma_f32_16x16x32_bf16 v[92:95], v[140:143], v[148:151], v[92:95]\n\t"
        "ds_read_b128 v[172:175], v56 offset:38912\n\t"
        "v_mfma_f32_16x16x32_bf16 v[96:99], v[128:131], v[152:155], v[96:99]\n\t"
        "ds_read_b64_tr_b16 v[180:181], v59 offset:32768\n\t"
        "v_mfma_f32_16x16x32_bf16 v[100:103], v[132:135], v[152:155], v[100:103]\n\t"
        "ds_read_b64_tr_b16 v[182:183], v59 offset:33792\n\t"
        "v_mfma_f32_16x16x32_bf16 v[104:107], v[136:139], v[152:155], v[104:107]\n\t"
        "ds_read_b64_tr_b16 v[184:185], v60 offset:32768\n\t"
        "v_mfma_f32_16x16x32_bf16 v[108:111], v[140:143], v[152:155], v[108:111]\n\t"
        "ds_read_b64_tr_b16 v[186:187], v60 offset:33792\n\t"
        "v_mfma_f32_16x16x32_bf16 v[112:115], v[128:131], v[156:159], v[112:115]\n\t"
        "ds_read_b64_tr_b16 v[188:189], v61 offset:32768\n\t"
        "v_mfma_f32_16x16x32_bf16 v[116:119], v[132:135], v[156:159], v[116:119]\n\t"
        "ds_read_b64_tr_b16 v[190:191], v61 offset:33792\n\t"
        "v_mfma_f32_16x16x32_bf16 v[120:123], v[136:139], v[156:159], v[120:123]\n\t"
        "v_mfma_f32_16x16x32_bf16 v[124:127], v[140:143], v[156:159], v[124:127]\n\t"
        "s_waitcnt lgkmcnt(6)\n\t"
        "v_mfma_f32_16x16x32_bf16 v[64:67], v[160:163], v[176:179], v[64:67]\n\t"
        "ds_read_b64_tr_b16 v[144:145], v58 offset:40960\n\t"
        "v_mfma_f32_16x16x32_bf16 v[68:71], v[164:167], v[176:179], v[68:71]\n\t"
        "ds_read_b64_tr_b16 v[146:147], v58 offset:41984\n\t"
        "v_mfma_f32_16x16x32_bf16 v[72:75], v[168:171], v[176:179], v[72:75]\n\t"
        "ds_read_b128 v[128:131], v57 offset:32768\n\t"
        "v_mfma_f32_16x16x32_bf16 v[76:79], v[172:175], v[176:179], v[76:79]\n\t"
        "ds_read_b128 v[132:135], v57 offset:34816\n\t"
        "s_waitcnt lgkmcnt(4)\n\t"
        "v_mfma_f32_16x16x32_bf16 v[80:83], v[160:163], v[180:183], v[80:83]\n\t"
        "ds_read_b128 v[136:139], v57 offset:36864\n\t"
        "v_mfma_f32_16x16x32_bf16 v[84:87], v[164:167], v[180:183], v[84:87]\n\t"
        "ds_read_b128 v[140:143], v57 offset:38912\n\t"
        "v_mfma_f32_16x16x32_bf16 v[88:91], v[168:171], v[180:183], v[88:91]\n\t"
        "ds_read_b64_tr_b16 v[148:149], v59 offset:40960\n\t"
        "v_mfma_f32_16x16x32_bf16 v[92:95], v[172:175], v[180:183], v[92:95]\n\t"
        "ds_read_b64_tr_b16 v[150:151], v59 offset:41984\n\t"
        "v_mfma_f32_16x16x32_bf16 v[96:99], v[160:163], v[184:187], v[96:99]\n\t"
        "ds_read_b64_tr_b16 v[152:153], v60 offset:40960\n\t"
        "v_mfma_f32_16x16x32_bf16 v[100:103], v[164:167], v[184:187], v[100:103]\n\t"
        "ds_read_b64_tr_b16 v[154:155], v60 offset:41984\n\t"
        "v_mfma_f32_16x16x32_bf16 v[104:107], v[168:171], v[184:187], v[104:107]\n\t"
        "ds_read_b64_tr_b16 v[156:157], v61 offset:40960\n\t"
        "v_mfma_f32_16x16x32_bf16 v[108:111], v[172:175], v[184:187], v[108:111]\n\t"
        "ds_read_b64_tr_b16 v[158:159], v61 offset:41984\n\t"
        "v_mfma_f32_16x16x32_bf16 v[112:115], v[160:163], v[188:191], v[112:115]\n\t"
        "v_add_u32_e32 v56, 0x10000, v56\n\t"
        "v_add_u32_e32 v57, 0x10000, v57\n\t"
        "v_add_u32_e32 v58, 0x10000, v58\n\t"
        "v_mfma_f32_16x16x32_bf16 v[116:119], v[164:167], v[188:191], v[116:119]\n\t"
        "v_add_u32_e32 v59, 0x10000, v59\n\t"
        "v_add_u32_e32 v60, 0x10000, v60\n\t"
        "v_add_u32_e32 v61, 0x10000, v61\n\t"
        "v_mfma_f32_16x16x32_bf16 v[120:123], v[168:171], v[188:191], v[120:123]\n\t"
        "v_mfma_f32_16x16x32_bf16 v[124:127], v[172:175], v[188:191], v[124:127]\n\t"
        "s_waitcnt lgkmcnt(6)\n\t"
        "v_mfma_f32_16x16x32_bf16 v[64:67], v[128:131], v[144:147], v[64:67]\n\t"
        "ds_read_b64_tr_b16 v[176:177], v58 offset:0\n\t"
        "v_mfma_f32_16x16x32_bf16 v[68:71], v[132:135], v[144:147], v[68:71]\n\t"
        "ds_read_b64_tr_b16 v[178:179], v58 offset:1024\n\t"
        "v_mfma_f32_16x16x32_bf16 v[72:75], v[136:139], v[144:147], v[72:75]\n\t"
        "ds_read_b128 v[160:163], v56 offset:0\n\t"
        "v_mfma_f32_16x16x32_bf16 v[76:79], v[140:143], v[144:147], v[76:79]\n\t"
        "ds_read_b128 v[164:167], v56 offset:2048\n\t"
        "s_waitcnt lgkmcnt(4)\n\t"
        "v_mfma_f32_16x16x32_bf16 v[80:83], v[128:131], v[148:151], v[80:83]\n\t"
        "ds_read_b128 v[168:171], v56 offset:4096\n\t"
        "v_mfma_f32_16x16x32_bf16 v[84:87], v[132:135], v[148:151], v[84:87]\n\t"
        "ds_read_b128 v[172:175], v56 offset:6144\n\t"
        "v_mfma_f32_16x16x32_bf16 v[88:91], v[136:139], v[148:151], v[88:91]\n\t"
        "ds_read_b64_tr_b16 v[180:181], v59 offset:0\n\t"
        "v_mfma_f32_16x16x32_bf16 v[92:95], v[140:143], v[148:151], v[92:95]\n\t"
        "ds_read_b64_tr_b16 v[182:183], v59 offset:1024\n\t"
        "v_mfma_f32_16x16x32_bf16 v[96:99], v[128:131], v[152:155], v[96:99]\n\t"
        "ds_read_b64_tr_b16 v[184:185], v60 offset:0\n\t"
        "v_mfma_f32_16x16x32_bf16 v[100:103], v[132:135], v[152:155], v[100:103]\n\t"
        "ds_read_b64_tr_b16 v[186:187], v60 offset:1024\n\t"
        "v_mfma_f32_16x16x32_bf16 v[104:107], v[136:139], v[152:155], v[104:107]\n\t"
        "ds_read_b64_tr_b16 v[188:189], v61 offset:0\n\t"
        "v_mfma_f32_16x16x32_bf16 v[108:111], v[140:143], v[152:155], v[108:111]\n\t"
        "ds_read_b64_tr_b16 v[190:191], v61 offset:1024\n\t"
        "v_mfma_f32_16x16x32_bf16 v[112:115], v[128:131], v[156:159], v[112:115]\n\t"
        "v_mfma_f32_16x16x32_bf16 v[116:119], v[132:135], v[156:159], v[116:119]\n\t"
        "v_mfma_f32_16x16x32_bf16 v[120:123], v[136:139], v[156:159], v[120:123]\n\t"
        "v_mfma_f32_16x16x32_bf16 v[124:127], v[140:143], v[156:159], v[124:127]\n\t"
        "s_waitcnt lgkmcnt(6)\n\t"
        "v_mfma_f32_16x16x32_bf16 v[64:67], v[160:163], v[176:179], v[64:67]\n\t"
        "ds_read_b64_tr_b16 v[144:145], v58 offset:8192\n\t"
        "v_mfma_f32_16x16x32_bf16 v[68:71], v[164:167], v[176:179], v[68:71]\n\t"
        "ds_read_b64_tr_b16 v[146:147], v58 offset:9216\n\t"
        "v_mfma_f32_16x16x32_bf16 v[72:75], v[168:171], v[176:179], v[72:75]\n\t"
        "ds_read_b128 v[128:131], v57 offset:0\n\t"
        "v_mfma_f32_16x16x32_bf16 v[76:79], v[172:175], v[176:179], v[76:79]\n\t"
        "ds_read_b128 v[132:135], v57 offset:2048\n\t"
        "s_waitcnt lgkmcnt(4)\n\t"
        "v_mfma_f32_16x16x32_bf16 v[80:83], v[160:163], v[180:183], v[80:83]\n\t"
        "ds_read_b128 v[136:139], v57 offset:4096\n\t"
        "v_mfma_f32_16x16x32_bf16 v[84:87], v[164:167], v[180:183], v[84:87]\n\t"
        "ds_read_b128 v[140:143], v57 offset:6144\n\t"
        "v_mfma_f32_16x16x32_bf16 v[88:91], v[168:171], v[180:183], v[88:91]\n\t"
        "ds_read_b64_tr_b16 v[148:149], v59 offset:8192\n\t"
        "v_mfma_f32_16x16x32_bf16 v[92:95], v[172:175], v[180:183], v[92:95]\n\t"
        "ds_read_b64_tr_b16 v[150:151], v59 offset:9216\n\t"
        "v_mfma_f32_16x16x32_bf16 v[96:99], v[160:163], v[184:187], v[96:99]\n\t"
        "ds_read_b64_tr_b16 v[152:153], v60 offset:8192\n\t"
        "v_mfma_f32_16x16x32_bf16 v[100:103], v[164:167], v[184:187], v[100:103]\n\t"
        "ds_read_b64_tr_b16 v[154:155], v60 offset:9216\n\t"
        "v_mfma_f32_16x16x32_bf16 v[104:107], v[168:171], v[184:187], v[104:107]\n\t"
        "ds_read_b64_tr_b16 v[156:157], v61 offset:8192\n\t"
        "v_mfma_f32_16x16x32_bf16 v[108:111], v[172:175], v[184:187], v[108:111]\n\t"
        "ds_read_b64_tr_b16 v[158:159], v61 offset:9216\n\t"
        "v_mfma_f32_16x16x32_bf16 v[112:115], v[160:163], v[188:191], v[112:115]\n\t"
        "v_add_u32_e32 v56, 0xfffe0000, v56\n\t"
        "v_add_u32_e32 v57, 0xfffe0000, v57\n\t"
        "v_add_u32_e32 v58, 0xfffe0000, v58\n\t"
        "v_mfma_f32_16x16x32_bf16 v[116:119], v[164:167], v[188:191], v[116:119]\n\t"
        "v_add_u32_e32 v59, 0xfffe0000, v59\n\t"
        "v_add_u32_e32 v60, 0xfffe0000, v60\n\t"
        "v_add_u32_e32 v61, 0xfffe0000, v61\n\t"
        "v_mfma_f32_16x16x32_bf16 v[120:123], v[168:171], v[188:191], v[120:123]\n\t"
        "v_mfma_f32_16x16x32_bf16 v[124:127], v[172:175], v[188:191], v[124:127]\n\t"
        "s_waitcnt lgkmcnt(6)\n\t"
        "v_mfma_f32_16x16x32_bf16 v[64:67], v[128:131], v[144:147], v[64:67]\n\t"
        "v_mfma_f32_16x16x32_bf16 v[68:71], v[132:135], v[144:147], v[68:71]\n\t"
        "s_waitcnt lgkmcnt(0)\n\t"
        "s_barrier\n\t"
        "v_mfma_f32_16x16x32_bf16 v[72:75], v[136:139], v[144:147], v[72:75]\n\t"
        "ds_read_b64_tr_b16 v[176:177], v58 offset:0\n\t"
        "v_mfma_f32_16x16x32_bf16 v[76:79], v[140:143], v[144:147], v[76:79]\n\t"
        "ds_read_b64_tr_b16 v[178:179], v58 offset:1024\n\t"
        "v_mfma_f32_16x16x32_bf16 v[80:83], v[128:131], v[148:151], v[80:83]\n\t"
        "ds_read_b128 v[160:163], v56 offset:0\n\t"
        "v_mfma_f32_16x16x32_bf16 v[84:87], v[132:135], v[148:151], v[84:87]\n\t"
        "ds_read_b128 v[164:167], v56 offset:2048\n\t"
        "v_mfma_f32_16x16x32_bf16 v[88:91], v[136:139], v[148:151], v[88:91]\n\t"
        "ds_read_b128 v[168:171], v56 offset:4096\n\t"
        "v_mfma_f32_16x16x32_bf16 v[92:95], v[140:143], v[148:151], v[92:95]\n\t"
        "ds_read_b128 v[172:175], v56 offset:6144\n\t"
        "v_mfma_f32_16x16x32_bf16 v[96:99], v[128:131], v[152:155], v[96:99]\n\t"
        "ds_read_b64_tr_b16 v[180:181], v59 offset:0\n\t"
        "v_mfma_f32_16x16x32_bf16 v[100:103], v[132:135], v[152:155], v[100:103]\n\t"
        "ds_read_b64_tr_b16 v[182:183], v59 offset:1024\n\t"
        "v_mfma_f32_16x16x32_bf16 v[104:107], v[136:139], v[152:155], v[104:107]\n\t"
        "ds_read_b64_tr_b16 v[184:185], v60 offset:0\n\t"
        "v_mfma_f32_16x16x32_bf16 v[108:111], v[140:143], v[152:155], v[108:111]\n\t"
        "ds_read_b64_tr_b16 v[186:187], v60 offset:1024\n\t"
        "v_mfma_f32_16x16x32_bf16 v[112:115], v[128:131], v[156:159], v[112:115]\n\t"
        "ds_read_b64_tr_b16 v[188:189], v61 offset:0\n\t"
        "v_mfma_f32_16x16x32_bf16 v[116:119], v[132:135], v[156:159], v[116:119]\n\t"
        "ds_read_b64_tr_b16 v[190:191], v61 offset:1024\n\t"
        "v_mfma_f32_16x16x32_bf16 v[120:123], v[136:139], v[156:159], v[120:123]\n\t"
        "v_mfma_f32_16x16x32_bf16 v[124:127], v[140:143], v[156:159], v[124:127]\n\t"
        "s_waitcnt lgkmcnt(6)\n\t"
        "v_mfma_f32_16x16x32_bf16 v[64:67], v[160:163], v[176:179], v[64:67]\n\t"
        "ds_read_b64_tr_b16 v[144:145], v58 offset:8192\n\t"
        "v_mfma_f32_16x16x32_bf16 v[68:71], v[164:167], v[176:179], v[68:71]\n\t"
        "ds_read_b64_tr_b16 v[146:147], v58 offset:9216\n\t"
        "v_mfma_f32_16x16x32_bf16 v[72:75], v[168:171], v[176:179], v[72:75]\n\t"
        "ds_read_b128 v[128:131], v57 offset:0\n\t"
        "v_mfma_f32_16x16x32_bf16 v[76:79], v[172:175], v[176:179], v[76:79]\n\t"
        "ds_read_b128 v[132:135], v57 offset:2048\n\t"
        "s_waitcnt lgkmcnt(4)\n\t"
        "v_mfma_f32_16x16x32_bf16 v[80:83], v[160:163], v[180:183], v[80:83]\n\t"
        "ds_read_b128 v[136:139], v57 offset:4096\n\t"
        "v_mfma_f32_16x16x32_bf16 v[84:87], v[164:167], v[180:183], v[84:87]\n\t"
        "ds_read_b128 v[140:143], v57 offset:6144\n\t"
        "v_mfma_f32_16x16x32_bf16 v[88:91], v[168:171], v[180:183], v[88:91]\n\t"
        "ds_read_b64_tr_b16 v[148:149], v59 offset:8192\n\t"
        "v_mfma_f32_16x16x32_bf16 v[92:95], v[172:175], v[180:183], v[92:95]\n\t"
        "ds_read_b64_tr_b16 v[150:151], v59 offset:9216\n\t"
        "v_mfma_f32_16x16x32_bf16 v[96:99], v[160:163], v[184:187], v[96:99]\n\t"
        "ds_read_b64_tr_b16 v[152:153], v60 offset:8192\n\t"
        "v_mfma_f32_16x16x32_bf16 v[100:103], v[164:167], v[184:187], v[100:103]\n\t"
        "ds_read_b64_tr_b16 v[154:155], v60 offset:9216\n\t"
        "v_mfma_f32_16x16x32_bf16 v[104:107], v[168:171], v[184:187], v[104:107]\n\t"
        "ds_read_b64_tr_b16 v[156:157], v61 offset:8192\n\t"
        "v_mfma_f32_16x16x32_bf16 v[108:111], v[172:175], v[184:187], v[108:111]\n\t"
        "ds_read_b64_tr_b16 v[158:159], v61 offset:9216\n\t"
        "v_mfma_f32_16x16x32_bf16 v[112:115], v[160:163], v[188:191], v[112:115]\n\t"
        "v_mfma_f32_16x16x32_bf16 v[116:119], v[164:167], v[188:191], v[116:119]\n\t"
        "v_mfma_f32_16x16x32_bf16 v[120:123], v[168:171], v[188:191], v[120:123]\n\t"
        "v_mfma_f32_16x16x32_bf16 v[124:127], v[172:175], v[188:191], v[124:127]\n\t"
        "s_waitcnt lgkmcnt(6)\n\t"
        "v_mfma_f32_16x16x32_bf16 v[64:67], v[128:131], v[144:147], v[64:67]\n\t"
        "v_mfma_f32_16x16x32_bf16 v[68:71], v[132:135], v[144:147], v[68:71]\n\t"
        "s_waitcnt lgkmcnt(0)\n\t"
        "s_barrier\n\t"
        "v_mfma_f32_16x16x32_bf16 v[72:75], v[136:139], v[144:147], v[72:75]\n\t"
        "ds_read_b64_tr_b16 v[176:177], v58 offset:32768\n\t"
        "v_mfma_f32_16x16x32_bf16 v[76:79], v[140:143], v[144:147], v[76:79]\n\t"
        "ds_read_b64_tr_b16 v[178:179], v58 offset:33792\n\t"
        "v_mfma_f32_16x16x32_bf16 v[80:83], v[128:131], v[148:151], v[80:83]\n\t"
        "ds_read_b128 v[160:163], v56 offset:32768\n\t"
        "v_mfma_f32_16x16x32_bf16 v[84:87], v[132:135], v[148:151], v[84:87]\n\t"
        "ds_read_b128 v[164:167], v56 offset:34816\n\t"
        "v_mfma_f32_16x16x32_bf16 v[88:91], v[136:139], v[148:151], v[88:91]\n\t"
        "ds_read_b128 v[168:171], v56 offset:36864\n\t"
        "v_mfma_f32_16x16x32_bf16 v[92:95], v[140:143], v[148:151], v[92:95]\n\t"
        "ds_read_b128 v[172:175], v56 offset:38912\n\t"
        "v_mfma_f32_16x16x32_bf16 v[96:99], v[128:131], v[152:155], v[96:99]\n\t"
        "ds_read_b64_tr_b16 v[180:181], v59 offset:32768\n\t"
        "v_mfma_f32_16x16x32_bf16 v[100:103], v[132:135], v[152:155], v[100:103]\n\t"
        "ds_read_b64_tr_b16 v[182:183], v59 offset:33792\n\t"
        "v_mfma_f32_16x16x32_bf16 v[104:107], v[136:139], v[152:155], v[104:107]\n\t"
        "ds_read_b64_tr_b16 v[184:185], v60 offset:32768\n\t"
        "v_mfma_f32_16x16x32_bf16 v[108:111], v[140:143], v[152:155], v[108:111]\n\t"
        "ds_read_b64_tr_b16 v[186:187], v60 offset:33792\n\t"
        "v_mfma_f32_16x16x32_bf16 v[112:115], v[128:131], v[156:159], v[112:115]\n\t"
        "ds_read_b64_tr_b16 v[188:189], v61 offset:32768\n\t"
        "v_mfma_f32_16x16x32_bf16 v[116:119], v[132:135], v[156:159], v[116:119]\n\t"
        "ds_read_b64_tr_b16 v[190:191], v61 offset:33792\n\t"
        "v_mfma_f32_16x16x32_bf16 v[120:123], v[136:139], v[156:159], v[120:123]\n\t"
        "v_mfma_f32_16x16x32_bf16 v[124:127], v[140:143], v[156:159], v[124:127]\n\t"
        "s_waitcnt lgkmcnt(6)\n\t"
        "v_mfma_f32_16x16x32_bf16 v[64:67], v[160:163], v[176:179], v[64:67]\n\t"
        "ds_read_b64_tr_b16 v[144:145], v58 offset:40960\n\t"
        "v_mfma_f32_16x16x32_bf16 v[68:71], v[164:167], v[176:179], v[68:71]\n\t"
        "ds_read_b64_tr_b16 v[146:147], v58 offset:41984\n\t"
        "v_mfma_f32_16x16x32_bf16 v[72:75], v[168:171], v[176:179], v[72:75]\n\t"
        "ds_read_b128 v[128:131], v57 offset:32768\n\t"
        "v_mfma_f32_16x16x32_bf16 v[76:79], v[172:175], v[176:179], v[76:79]\n\t"
        "ds_read_b128 v[132:135], v57 offset:34816\n\t"
        "s_waitcnt lgkmcnt(4)\n\t"
        "v_mfma_f32_16x16x32_bf16 v[80:83], v[160:163], v[180:183], v[80:83]\n\t"
        "ds_read_b128 v[136:139], v57 offset:36864\n\t"
        "v_mfma_f32_16x16x32_bf16 v[84:87], v[164:167], v[180:183], v[84:87]\n\t"
        "ds_read_b128 v[140:143], v57 offset:38912\n\t"
        "v_mfma_f32_16x16x32_bf16 v[88:91], v[168:171], v[180:183], v[88:91]\n\t"
        "ds_read_b64_tr_b16 v[148:149], v59 offset:40960\n\t"
        "v_mfma_f32_16x16x32_bf16 v[92:95], v[172:175], v[180:183], v[92:95]\n\t"
        "ds_read_b64_tr_b16 v[150:151], v59 offset:41984\n\t"
        "v_mfma_f32_16x16x32_bf16 v[96:99], v[160:163], v[184:187], v[96:99]\n\t"
        "ds_read_b64_tr_b16 v[152:153], v60 offset:40960\n\t"
        "v_mfma_f32_16x16x32_bf16 v[100:103], v[164:167], v[184:187], v[100:103]\n\t"
        "ds_read_b64_tr_b16 v[154:155], v60 offset:41984\n\t"
        "v_mfma_f32_16x16x32_bf16 v[104:107], v[168:171], v[184:187], v[104:107]\n\t"
        "ds_read_b64_tr_b16 v[156:157], v61 offset:40960\n\t"
        "v_mfma_f32_16x16x32_bf16 v[108:111], v[172:175], v[184:187], v[108:111]\n\t"
        "ds_read_b64_tr_b16 v[158:159], v61 offset:41984\n\t"
        "v_mfma_f32_16x16x32_bf16 v[112:115], v[160:163], v[188:191], v[112:115]\n\t"
        "v_add_u32_e32 v56, 0x10000, v56\n\t"
        "v_add_u32_e32 v57, 0x10000, v57\n\t"
        "v_add_u32_e32 v58, 0x10000, v58\n\t"
        "v_mfma_f32_16x16x32_bf16 v[116:119], v[164:167], v[188:191], v[116:119]\n\t"
        "v_add_u32_e32 v59, 0x10000, v59\n\t"
        "v_add_u32_e32 v60, 0x10000, v60\n\t"
        "v_add_u32_e32 v61, 0x10000, v61\n\t"
        "v_mfma_f32_16x16x32_bf16 v[120:123], v[168:171], v[188:191], v[120:123]\n\t"
        "v_mfma_f32_16x16x32_bf16 v[124:127], v[172:175], v[188:191], v[124:127]\n\t"
        "s_waitcnt lgkmcnt(6)\n\t"
        "v_mfma_f32_16x16x32_bf16 v[64:67], v[128:131], v[144:147], v[64:67]\n\t"
        "v_mfma_f32_16x16x32_bf16 v[68:71], v[132:135], v[144:147], v[68:71]\n\t"
        "s_waitcnt lgkmcnt(0)\n\t"
        "s_barrier\n\t"
        "v_mfma_f32_16x16x32_bf16 v[72:75], v[136:139], v[144:147], v[72:75]\n\t"
        "ds_read_b64_tr_b16 v[176:177], v58 offset:0\n\t"
        "v_mfma_f32_16x16x32_bf16 v[76:79], v[140:143], v[144:147], v[76:79]\n\t"
        "ds_read_b64_tr_b16 v[178:179], v58 offset:1024\n\t"
        "v_mfma_f32_16x16x32_bf16 v[80:83], v[128:131], v[148:151], v[80:83]\n\t"
        "ds_read_b128 v[160:163], v56 offset:0\n\t"
        "v_mfma_f32_16x16x32_bf16 v[84:87], v[132:135], v[148:151], v[84:87]\n\t"
        "ds_read_b128 v[164:167], v56 offset:2048\n\t"
        "v_mfma_f32_16x16x32_bf16 v[88:91], v[136:139], v[148:151], v[88:91]\n\t"
        "ds_read_b128 v[168:171], v56 offset:4096\n\t"
        "v_mfma_f32_16x16x32_bf16 v[92:95], v[140:143], v[148:151], v[92:95]\n\t"
        "ds_read_b128 v[172:175], v56 offset:6144\n\t"
        "v_mfma_f32_16x16x32_bf16 v[96:99], v[128:131], v[152:155], v[96:99]\n\t"
        "ds_read_b64_tr_b16 v[180:181], v59 offset:0\n\t"
        "v_mfma_f32_16x16x32_bf16 v[100:103], v[132:135], v[152:155], v[100:103]\n\t"
        "ds_read_b64_tr_b16 v[182:183], v59 offset:1024\n\t"
        "v_mfma_f32_16x16x32_bf16 v[104:107], v[136:139], v[152:155], v[104:107]\n\t"
        "ds_read_b64_tr_b16 v[184:185], v60 offset:0\n\t"
        "v_mfma_f32_16x16x32_bf16 v[108:111], v[140:143], v[152:155], v[108:111]\n\t"
        "ds_read_b64_tr_b16 v[186:187], v60 offset:1024\n\t"
        "v_mfma_f32_16x16x32_bf16 v[112:115], v[128:131], v[156:159], v[112:115]\n\t"
        "ds_read_b64_tr_b16 v[188:189], v61 offset:0\n\t"
        "v_mfma_f32_16x16x32_bf16 v[116:119], v[132:135], v[156:159], v[116:119]\n\t"
        "ds_read_b64_tr_b16 v[190:191], v61 offset:1024\n\t"
        "v_mfma_f32_16x16x32_bf16 v[120:123], v[136:139], v[156:159], v[120:123]\n\t"
        "v_mfma_f32_16x16x32_bf16 v[124:127], v[140:143], v[156:159], v[124:127]\n\t"
        "s_waitcnt lgkmcnt(6)\n\t"
        "v_mfma_f32_16x16x32_bf16 v[64:67], v[160:163], v[176:179], v[64:67]\n\t"
        "ds_read_b64_tr_b16 v[144:145], v58 offset:8192\n\t"
        "v_mfma_f32_16x16x32_bf16 v[68:71], v[164:167], v[176:179], v[68:71]\n\t"
        "ds_read_b64_tr_b16 v[146:147], v58 offset:9216\n\t"
        "v_mfma_f32_16x16x32_bf16 v[72:75], v[168:171], v[176:179], v[72:75]\n\t"
        "ds_read_b128 v[128:131], v57 offset:0\n\t"
        "v_mfma_f32_16x16x32_bf16 v[76:79], v[172:175], v[176:179], v[76:79]\n\t"
        "ds_read_b128 v[132:135], v57 offset:2048\n\t"
        "s_waitcnt lgkmcnt(4)\n\t"
        "v_mfma_f32_16x16x32_bf16 v[80:83], v[160:163], v[180:183], v[80:83]\n\t"
        "ds_read_b128 v[136:139], v57 offset:4096\n\t"
        "v_mfma_f32_16x16x32_bf16 v[84:87], v[164:167], v[180:183], v[84:87]\n\t"
        "ds_read_b128 v[140:143], v57 offset:6144\n\t"
        "v_mfma_f32_16x16x32_bf16 v[88:91], v[168:171], v[180:183], v[88:91]\n\t"
        "ds_read_b64_tr_b16 v[148:149], v59 offset:8192\n\t"
        "v_mfma_f32_16x16x32_bf16 v[92:95], v[172:175], v[180:183], v[92:95]\n\t"
        "ds_read_b64_tr_b16 v[150:151], v59 offset:9216\n\t"
        "v_mfma_f32_16x16x32_bf16 v[96:99], v[160:163], v[184:187], v[96:99]\n\t"
        "ds_read_b64_tr_b16 v[152:153], v60 offset:8192\n\t"
        "v_mfma_f32_16x16x32_bf16 v[100:103], v[164:167], v[184:187], v[100:103]\n\t"
        "ds_read_b64_tr_b16 v[154:155], v60 offset:9216\n\t"
        "v_mfma_f32_16x16x32_bf16 v[104:107], v[168:171], v[184:187], v[104:107]\n\t"
        "ds_read_b64_tr_b16 v[156:157], v61 offset:8192\n\t"
        "v_mfma_f32_16x16x32_bf16 v[108:111], v[172:175], v[184:187], v[108:111]\n\t"
        "ds_read_b64_tr_b16 v[158:159], v61 offset:9216\n\t"
        "v_mfma_f32_16x16x32_bf16 v[112:115], v[160:163], v[188:191], v[112:115]\n\t"
        "v_mfma_f32_16x16x32_bf16 v[116:119], v[164:167], v[188:191], v[116:119]\n\t"
        "v_mfma_f32_16x16x32_bf16 v[120:123], v[168:171], v[188:191], v[120:123]\n\t"
        "v_mfma_f32_16x16x32_bf16 v[124:127], v[172:175], v[188:191], v[124:127]\n\t"
        "s_waitcnt lgkmcnt(6)\n\t"
        "v_mfma_f32_16x16x32_bf16 v[64:67], v[128:131], v[144:147], v[64:67]\n\t"
        "v_mfma_f32_16x16x32_bf16 v[68:71], v[132:135], v[144:147], v[68:71]\n\t"
        "s_waitcnt lgkmcnt(0)\n\t"
        "s_barrier\n\t"
        "v_mfma_f32_16x16x32_bf16 v[72:75], v[136:139], v[144:147], v[72:75]\n\t"
        "ds_read_b64_tr_b16 v[176:177], v58 offset:32768\n\t"
        "v_mfma_f32_16x16x32_bf16 v[76:79], v[140:143], v[144:147], v[76:79]\n\t"
        "ds_read_b64_tr_b16 v[178:179], v58 offset:33792\n\t"
        "v_mfma_f32_16x16x32_bf16 v[80:83], v[128:131], v[148:151], v[80:83]\n\t"
        "ds_read_b128 v[160:163], v56 offset:32768\n\t"
        "v_mfma_f32_16x16x32_bf16 v[84:87], v[132:135], v[148:151], v[84:87]\n\t"
        "ds_read_b128 v[164:167], v56 offset:34816\n\t"
        "v_mfma_f32_16x16x32_bf16 v[88:91], v[136:139], v[148:151], v[88:91]\n\t"
        "ds_read_b128 v[168:171], v56 offset:36864\n\t"
        "v_mfma_f32_16x16x32_bf16 v[92:95], v[140:143], v[148:151], v[92:95]\n\t"
        "ds_read_b128 v[172:175], v56 offset:38912\n\t"
        "v_mfma_f32_16x16x32_bf16 v[96:99], v[128:131], v[152:155], v[96:99]\n\t"
        "ds_read_b64_tr_b16 v[180:181], v59 offset:32768\n\t"
        "v_mfma_f32_16x16x32_bf16 v[100:103], v[132:135], v[152:155], v[100:103]\n\t"
        "ds_read_b64_tr_b16 v[182:183], v59 offset:33792\n\t"
        "v_mfma_f32_16x16x32_bf16 v[104:107], v[136:139], v[152:155], v[104:107]\n\t"
        "ds_read_b64_tr_b16 v[184:185], v60 offset:32768\n\t"
        "v_mfma_f32_16x16x32_bf16 v[108:111], v[140:143], v[152:155], v[108:111]\n\t"
        "ds_read_b64_tr_b16 v[186:187], v60 offset:33792\n\t"
        "v_mfma_f32_16x16x32_bf16 v[112:115], v[128:131], v[156:159], v[112:115]\n\t"
        "ds_read_b64_tr_b16 v[188:189], v61 offset:32768\n\t"
        "v_mfma_f32_16x16x32_bf16 v[116:119], v[132:135], v[156:159], v[116:119]\n\t"
        "ds_read_b64_tr_b16 v[190:191], v61 offset:33792\n\t"
        "v_mfma_f32_16x16x32_bf16 v[120:123], v[136:139], v[156:159], v[120:123]\n\t"
        "v_mfma_f32_16x16x32_bf16 v[124:127], v[140:143], v[156:159], v[124:127]\n\t"
        "s_waitcnt lgkmcnt(6)\n\t"
        "v_mfma_f32_16x16x32_bf16 v[64:67], v[160:163], v[176:179], v[64:67]\n\t"
        "ds_read_b64_tr_b16 v[144:145], v58 offset:40960\n\t"
        "v_mfma_f32_16x16x32_bf16 v[68:71], v[164:167], v[176:179], v[68:71]\n\t"
        "ds_read_b64_tr_b16 v[146:147], v58 offset:41984\n\t"
        "v_mfma_f32_16x16x32_bf16 v[72:75], v[168:171], v[176:179], v[72:75]\n\t"
        "ds_read_b128 v[128:131], v57 offset:32768\n\t"
        "v_mfma_f32_16x16x32_bf16 v[76:79], v[172:175], v[176:179], v[76:79]\n\t"
        "ds_read_b128 v[132:135], v57 offset:34816\n\t"
        "s_waitcnt lgkmcnt(4)\n\t"
        "v_mfma_f32_16x16x32_bf16 v[80:83], v[160:163], v[180:183], v[80:83]\n\t"
        "ds_read_b128 v[136:139], v57 offset:36864\n\t"
        "v_mfma_f32_16x16x32_bf16 v[84:87], v[164:167], v[180:183], v[84:87]\n\t"
        "ds_read_b128 v[140:143], v57 offset:38912\n\t"
        "v_mfma_f32_16x16x32_bf16 v[88:91], v[168:171], v[180:183], v[88:91]\n\t"
        "ds_read_b64_tr_b16 v[148:149], v59 offset:40960\n\t"
        "v_mfma_f32_16x16x32_bf16 v[92:95], v[172:175], v[180:183], v[92:95]\n\t"
        "ds_read_b64_tr_b16 v[150:151], v59 offset:41984\n\t"
        "v_mfma_f32_16x16x32_bf16 v[96:99], v[160:163], v[184:187], v[96:99]\n\t"
        "ds_read_b64_tr_b16 v[152:153], v60 offset:40960\n\t"
        "v_mfma_f32_16x16x32_bf16 v[100:103], v[164:167], v[184:187], v[100:103]\n\t"
        "ds_read_b64_tr_b16 v[154:155], v60 offset:41984\n\t"
        "v_mfma_f32_16x16x32_bf16 v[104:107], v[168:171], v[184:187], v[104:107]\n\t"
        "ds_read_b64_tr_b16 v[156:157], v61 offset:40960\n\t"
        "v_mfma_f32_16x16x32_bf16 v[108:111], v[172:175], v[184:187], v[108:111]\n\t"
        "ds_read_b64_tr_b16 v[158:159], v61 offset:41984\n\t"
        "v_mfma_f32_16x16x32_bf16 v[112:115], v[160:163], v[188:191], v[112:115]\n\t"
        "v_add_u32_e32 v56, 0x10000, v56\n\t"
        "v_add_u32_e32 v57, 0x10000, v57\n\t"
        "v_add_u32_e32 v58, 0x10000, v58\n\t"
        "v_mfma_f32_16x16x32_bf16 v[116:119], v[164:167], v[188:191], v[116:119]\n\t"
        "v_add_u32_e32 v59, 0x10000, v59\n\t"
        "v_add_u32_e32 v60, 0x10000, v60\n\t"
        "v_add_u32_e32 v61, 0x10000, v61\n\t"
        "v_mfma_f32_16x16x32_bf16 v[120:123], v[168:171], v[188:191], v[120:123]\n\t"
        "v_mfma_f32_16x16x32_bf16 v[124:127], v[172:175], v[188:191], v[124:127]\n\t"
        "s_waitcnt lgkmcnt(6)\n\t"
        "v_mfma_f32_16x16x32_bf16 v[64:67], v[128:131], v[144:147], v[64:67]\n\t"
        "ds_read_b64_tr_b16 v[176:177], v58 offset:0\n\t"
        "v_mfma_f32_16x16x32_bf16 v[68:71], v[132:135], v[144:147], v[68:71]\n\t"
        "ds_read_b64_tr_b16 v[178:179], v58 offset:1024\n\t"
        "v_mfma_f32_16x16x32_bf16 v[72:75], v[136:139], v[144:147], v[72:75]\n\t"
        "ds_read_b128 v[160:163], v56 offset:0\n\t"
        "v_mfma_f32_16x16x32_bf16 v[76:79], v[140:143], v[144:147], v[76:79]\n\t"
        "ds_read_b128 v[164:167], v56 offset:2048\n\t"
        "s_waitcnt lgkmcnt(4)\n\t"
        "v_mfma_f32_16x16x32_bf16 v[80:83], v[128:131], v[148:151], v[80:83]\n\t"
        "ds_read_b128 v[168:171], v56 offset:4096\n\t"
        "v_mfma_f32_16x16x32_bf16 v[84:87], v[132:135], v[148:151], v[84:87]\n\t"
        "ds_read_b128 v[172:175], v56 offset:6144\n\t"
        "v_mfma_f32_16x16x32_bf16 v[88:91], v[136:139], v[148:151], v[88:91]\n\t"
        "ds_read_b64_tr_b16 v[180:181], v59 offset:0\n\t"
        "v_mfma_f32_16x16x32_bf16 v[92:95], v[140:143], v[148:151], v[92:95]\n\t"
        "ds_read_b64_tr_b16 v[182:183], v59 offset:1024\n\t"
        "v_mfma_f32_16x16x32_bf16 v[96:99], v[128:131], v[152:155], v[96:99]\n\t"
        "ds_read_b64_tr_b16 v[184:185], v60 offset:0\n\t"
        "v_mfma_f32_16x16x32_bf16 v[100:103], v[132:135], v[152:155], v[100:103]\n\t"
        "ds_read_b64_tr_b16 v[186:187], v60 offset:1024\n\t"
        "v_mfma_f32_16x16x32_bf16 v[104:107], v[136:139], v[152:155], v[104:107]\n\t"
        "ds_read_b64_tr_b16 v[188:189], v61 offset:0\n\t"
        "v_mfma_f32_16x16x32_bf16 v[108:111], v[140:143], v[152:155], v[108:111]\n\t"
        "ds_read_b64_tr_b16 v[190:191], v61 offset:1024\n\t"
        "v_mfma_f32_16x16x32_bf16 v[112:115], v[128:131], v[156:159], v[112:115]\n\t"
        "v_mfma_f32_16x16x32_bf16 v[116:119], v[132:135], v[156:159], v[116:119]\n\t"
        "v_mfma_f32_16x16x32_bf16 v[120:123], v[136:139], v[156:159], v[120:123]\n\t"
        "v_mfma_f32_16x16x32_bf16 v[124:127], v[140:143], v[156:159], v[124:127]\n\t"
        "s_waitcnt lgkmcnt(6)\n\t"
        "v_mfma_f32_16x16x32_bf16 v[64:67], v[160:163], v[176:179], v[64:67]\n\t"
        "ds_read_b64_tr_b16 v[144:145], v58 offset:8192\n\t"
        "v_mfma_f32_16x16x32_bf16 v[68:71], v[164:167], v[176:179], v[68:71]\n\t"
        "ds_read_b64_tr_b16 v[146:147], v58 offset:9216\n\t"
        "v_mfma_f32_16x16x32_bf16 v[72:75], v[168:171], v[176:179], v[72:75]\n\t"
        "ds_read_b128 v[128:131], v57 offset:0\n\t"
        "v_mfma_f32_16x16x32_bf16 v[76:79], v[172:175], v[176:179], v[76:79]\n\t"
        "ds_read_b128 v[132:135], v57 offset:2048\n\t"
        "s_waitcnt lgkmcnt(4)\n\t"
        "v_mfma_f32_16x16x32_bf16 v[80:83], v[160:163], v[180:183], v[80:83]\n\t"
        "ds_read_b128 v[136:139], v57 offset:4096\n\t"
        "v_mfma_f32_16x16x32_bf16 v[84:87], v[164:167], v[180:183], v[84:87]\n\t"
        "ds_read_b128 v[140:143], v57 offset:6144\n\t"
        "v_mfma_f32_16x16x32_bf16 v[88:91], v[168:171], v[180:183], v[88:91]\n\t"
        "ds_read_b64_tr_b16 v[148:149], v59 offset:8192\n\t"
        "v_mfma_f32_16x16x32_bf16 v[92:95], v[172:175], v[180:183], v[92:95]\n\t"
        "ds_read_b64_tr_b16 v[150:151], v59 offset:9216\n\t"
        "v_mfma_f32_16x16x32_bf16 v[96:99], v[160:163], v[184:187], v[96:99]\n\t"
        "ds_read_b64_tr_b16 v[152:153], v60 offset:8192\n\t"
        "v_mfma_f32_16x16x32_bf16 v[100:103], v[164:167], v[184:187], v[100:103]\n\t"
        "ds_read_b64_tr_b16 v[154:155], v60 offset:9216\n\t"
        "v_mfma_f32_16x16x32_bf16 v[104:107], v[168:171], v[184:187], v[104:107]\n\t"
        "ds_read_b64_tr_b16 v[156:157], v61 offset:8192\n\t"
        "v_mfma_f32_16x16x32_bf16 v[108:111], v[172:175], v[184:187], v[108:111]\n\t"
        "ds_read_b64_tr_b16 v[158:159], v61 offset:9216\n\t"
        "v_mfma_f32_16x16x32_bf16 v[112:115], v[160:163], v[188:191], v[112:115]\n\t"
        "v_mfma_f32_16x16x32_bf16 v[116:119], v[164:167], v[188:191], v[116:119]\n\t"
        "v_mfma_f32_16x16x32_bf16 v[120:123], v[168:171], v[188:191], v[120:123]\n\t"
        "v_mfma_f32_16x16x32_bf16 v[124:127], v[172:175], v[188:191], v[124:127]\n\t"
        "s_waitcnt lgkmcnt(6)\n\t"
        "v_mfma_f32_16x16x32_bf16 v[64:67], v[128:131], v[144:147], v[64:67]\n\t"
        "v_mfma_f32_16x16x32_bf16 v[68:71], v[132:135], v[144:147], v[68:71]\n\t"
        "v_mfma_f32_16x16x32_bf16 v[72:75], v[136:139], v[144:147], v[72:75]\n\t"
        "v_mfma_f32_16x16x32_bf16 v[76:79], v[140:143], v[144:147], v[76:79]\n\t"
        "s_waitcnt lgkmcnt(0)\n\t"
        "v_mfma_f32_16x16x32_bf16 v[80:83], v[128:131], v[148:151], v[80:83]\n\t"
        "v_mfma_f32_16x16x32_bf16 v[84:87], v[132:135], v[148:151], v[84:87]\n\t"
        "v_mfma_f32_16x16x32_bf16 v[88:91], v[136:139], v[148:151], v[88:91]\n\t"
        "v_mfma_f32_16x16x32_bf16 v[92:95], v[140:143], v[148:151], v[92:95]\n\t"
        "v_mfma_f32_16x16x32_bf16 v[96:99], v[128:131], v[152:155], v[96:99]\n\t"
        "v_cvt_pk_bf16_f32 v160, v64, v65\n\t"
        "v_cvt_pk_bf16_f32 v161, v66, v67\n\t"
        "v_mfma_f32_16x16x32_bf16 v[100:103], v[132:135], v[152:155], v[100:103]\n\t"
        "v_cvt_pk_bf16_f32 v162, v68, v69\n\t"
        "v_cvt_pk_bf16_f32 v163, v70, v71\n\t"
        "v_mfma_f32_16x16x32_bf16 v[104:107], v[136:139], v[152:155], v[104:107]\n\t"
        "v_cvt_pk_bf16_f32 v164, v72, v73\n\t"
        "v_cvt_pk_bf16_f32 v165, v74, v75\n\t"
        "v_mfma_f32_16x16x32_bf16 v[108:111], v[140:143], v[152:155], v[108:111]\n\t"
        "v_cvt_pk_bf16_f32 v166, v76, v77\n\t"
        "v_cvt_pk_bf16_f32 v167, v78, v79\n\t"
        "v_mfma_f32_16x16x32_bf16 v[112:115], v[128:131], v[156:159], v[112:115]\n\t"
        "v_cvt_pk_bf16_f32 v168, v80, v81\n\t"
        "v_cvt_pk_bf16_f32 v169, v82, v83\n\t"
        "v_mfma_f32_16x16x32_bf16 v[116:119], v[132:135], v[156:159], v[116:119]\n\t"
        "v_cvt_pk_bf16_f32 v170, v84, v85\n\t"
        "v_cvt_pk_bf16_f32 v171, v86, v87\n\t"
        "v_mfma_f32_16x16x32_bf16 v[120:123], v[136:139], v[156:159], v[120:123]\n\t"
        "v_cvt_pk_bf16_f32 v172, v88, v89\n\t"
        "v_cvt_pk_bf16_f32 v173, v90, v91\n\t"
        "v_mfma_f32_16x16x32_bf16 v[124:127], v[140:143], v[156:159], v[124:127]\n\t"
        "v_cvt_pk_bf16_f32 v174, v92, v93\n\t"
        "v_cvt_pk_bf16_f32 v175, v94, v95\n\t"
        ".Lge_%=:\n\t"
        "v_cvt_pk_bf16_f32 v176, v96, v97\n\t"
        "v_cvt_pk_bf16_f32 v177, v98, v99\n\t"
        "v_cvt_pk_bf16_f32 v178, v100, v101\n\t"
        "v_cvt_pk_bf16_f32 v179, v102, v103\n\t"
        "v_cvt_pk_bf16_f32 v180, v104, v105\n\t"
        "v_cvt_pk_bf16_f32 v181, v106, v107\n\t"
        "v_cvt_pk_bf16_f32 v182, v108, v109\n\t"
        "v_cvt_pk_bf16_f32 v183, v110, v111\n\t"
        "v_cvt_pk_bf16_f32 v184, v112, v113\n\t"
        "v_cvt_pk_bf16_f32 v185, v114, v115\n\t"
        "v_cvt_pk_bf16_f32 v186, v116, v117\n\t"
        "v_cvt_pk_bf16_f32 v187, v118, v119\n\t"
        "v_cvt_pk_bf16_f32 v188, v120, v121\n\t"
        "v_cvt_pk_bf16_f32 v189, v122, v123\n\t"
        "v_cvt_pk_bf16_f32 v190, v124, v125\n\t"
        "v_cvt_pk_bf16_f32 v191, v126, v127\n\t"
            : GEMM_OUT : [ct] "s"(ct) : "v64","v65","v66","v67","v68","v69","v70","v71","v72","v73","v74","v75","v76","v77","v78","v79","v80","v81","v82","v83","v84","v85","v86","v87","v88","v89","v90","v91","v92","v93","v94","v95","v96","v97","v98","v99","v100","v101","v102","v103","v104","v105","v106","v107","v108","v109","v110","v111","v112","v113","v114","v115","v116","v117","v118","v119","v120","v121","v122","v123","v124","v125","v126","v127","v128","v129","v130","v131","v132","v133","v134","v135","v136","v137","v138","v139","v140","v141","v142","v143","v144","v145","v146","v147","v148","v149","v150","v151","v152","v153","v154","v155","v156","v157","v158","v159", "scc", "memory");
        } else {
            mkso(); mkwo(); mkxo(); consts0();
            u32 wb = u * 4096 + lane * 16, tch = min(lane >> 1, 9) * 3840 + (lane & 1) * 128;
            mkga();
            asm volatile("s_barrier\n\t"
        "s_cmp_ge_u32 %[ct], 10\n\t"
        "s_cbranch_scc1 .Lgv_%=\n\t"
        "ds_read_b64_tr_b16 v[176:177], v58 offset:0\n\t"
        "ds_read_b64_tr_b16 v[178:179], v58 offset:1024\n\t"
        "ds_read_b128 v[160:163], v56 offset:0\n\t"
        "ds_read_b128 v[164:167], v56 offset:2048\n\t"
        "ds_read_b128 v[168:171], v56 offset:4096\n\t"
        "ds_read_b128 v[172:175], v56 offset:6144\n\t"
        "ds_read_b64_tr_b16 v[180:181], v59 offset:0\n\t"
        "ds_read_b64_tr_b16 v[182:183], v59 offset:1024\n\t"
        "ds_read_b64_tr_b16 v[184:185], v60 offset:0\n\t"
        "ds_read_b64_tr_b16 v[186:187], v60 offset:1024\n\t"
        "ds_read_b64_tr_b16 v[188:189], v61 offset:0\n\t"
        "ds_read_b64_tr_b16 v[190:191], v61 offset:1024\n\t"
        "s_waitcnt lgkmcnt(6)\n\t"
        "v_mfma_f32_16x16x32_bf16 v[64:67], v[176:179], v[160:163], 0\n\t"
        "ds_read_b64_tr_b16 v[144:145], v58 offset:8192\n\t"
        "v_mfma_f32_16x16x32_bf16 v[68:71], v[176:179], v[164:167], 0\n\t"
        "ds_read_b64_tr_b16 v[146:147], v58 offset:9216\n\t"
        "v_mfma_f32_16x16x32_bf16 v[72:75], v[176:179], v[168:171], 0\n\t"
        "ds_read_b128 v[128:131], v57 offset:0\n\t"
        "v_mfma_f32_16x16x32_bf16 v[76:79], v[176:179], v[172:175], 0\n\t"
        "ds_read_b128 v[132:135], v57 offset:2048\n\t"
        "s_waitcnt lgkmcnt(4)\n\t"
        "v_mfma_f32_16x16x32_bf16 v[80:83], v[180:183], v[160:163], 0\n\t"
        "ds_read_b128 v[136:139], v57 offset:4096\n\t"
        "v_mfma_f32_16x16x32_bf16 v[84:87], v[180:183], v[164:167], 0\n\t"
        "ds_read_b128 v[140:143], v57 offset:6144\n\t"
        "v_mfma_f32_16x16x32_bf16 v[88:91], v[180:183], v[168:171], 0\n\t"
        "ds_read_b64_tr_b16 v[148:149], v59 offset:8192\n\t"
        "v_mfma_f32_16x16x32_bf16 v[92:95], v[180:183], v[172:175], 0\n\t"
        "ds_read_b64_tr_b16 v[150:151], v59 offset:9216\n\t"
        "v_mfma_f32_16x16x32_bf16 v[96:99], v[184:187], v[160:163], 0\n\t"
        "ds_read_b64_tr_b16 v[152:153], v60 offset:8192\n\t"
        "v_mfma_f32_16x16x32_bf16 v[100:103], v[184:187], v[164:167], 0\n\t"
        "ds_read_b64_tr_b16 v[154:155], v60 offset:9216\n\t"
        "v_mfma_f32_16x16x32_bf16 v[104:107], v[184:187], v[168:171], 0\n\t"
        "ds_read_b64_tr_b16 v[156:157], v61 offset:8192\n\t"
        "v_mfma_f32_16x16x32_bf16 v[108:111], v[184:187], v[172:175], 0\n\t"
        "ds_read_b64_tr_b16 v[158:159], v61 offset:9216\n\t"
        "v_mfma_f32_16x16x32_bf16 v[112:115], v[188:191], v[160:163], 0\n\t"
        "ds_read_b128 v[192:195], v62 offset:0\n\t"
        "v_mfma_f32_16x16x32_bf16 v[116:119], v[188:191], v[164:167], 0\n\t"
        "ds_read_b128 v[196:199], v62 offset:1024\n\t"
        "v_mfma_f32_16x16x32_bf16 v[120:123], v[188:191], v[168:171], 0\n\t"
        "v_mfma_f32_16x16x32_bf16 v[124:127], v[188:191], v[172:175], 0\n\t"
        "s_waitcnt lgkmcnt(8)\n\t"
        "v_mfma_f32_16x16x32_bf16 v[64:67], v[144:147], v[128:131], v[64:67]\n\t"
        "v_mfma_f32_16x16x32_bf16 v[68:71], v[144:147], v[132:135], v[68:71]\n\t"
        "s_waitcnt vmcnt(0) lgkmcnt(0)\n\t"
        "s_barrier\n\t"
        "v_mfma_f32_16x16x32_bf16 v[72:75], v[144:147], v[136:139], v[72:75]\n\t"
        "ds_read_b64_tr_b16 v[176:177], v58 offset:32768\n\t"
        "v_mfma_f32_16x16x32_bf16 v[76:79], v[144:147], v[140:143], v[76:79]\n\t"
        "ds_read_b64_tr_b16 v[178:179], v58 offset:33792\n\t"
        "v_mfma_f32_16x16x32_bf16 v[80:83], v[148:151], v[128:131], v[80:83]\n\t"
        "ds_read_b128 v[160:163], v56 offset:32768\n\t"
        "v_mfma_f32_16x16x32_bf16 v[84:87], v[148:151], v[132:135], v[84:87]\n\t"
        "ds_read_b128 v[164:167], v56 offset:34816\n\t"
        "v_mfma_f32_16x16x32_bf16 v[88:91], v[148:151], v[136:139], v[88:91]\n\t"
        "ds_read_b128 v[168:171], v56 offset:36864\n\t"
        "v_mfma_f32_16x16x32_bf16 v[92:95], v[148:151], v[140:143], v[92:95]\n\t"
        "ds_read_b128 v[172:175], v56 offset:38912\n\t"
        "v_mfma_f32_16x16x32_bf16 v[96:99], v[152:155], v[128:131], v[96:99]\n\t"
        "ds_read_b64_tr_b16 v[180:181], v59 offset:32768\n\t"
        "v_mfma_f32_16x16x32_bf16 v[100:103], v[152:155], v[132:135], v[100:103]\n\t"
        "ds_read_b64_tr_b16 v[182:183], v59 offset:33792\n\t"
        "v_mfma_f32_16x16x32_bf16 v[104:107], v[152:155], v[136:139], v[104:107]\n\t"
        "ds_read_b64_tr_b16 v[184:185], v60 offset:32768\n\t"
        "v_mfma_f32_16x16x32_bf16 v[108:111], v[152:155], v[140:143], v[108:111]\n\t"
        "ds_read_b64_tr_b16 v[186:187], v60 offset:33792\n\t"
        "v_mfma_f32_16x16x32_bf16 v[112:115], v[156:159], v[128:131], v[112:115]\n\t"
        "ds_read_b64_tr_b16 v[188:189], v61 offset:32768\n\t"
        "v_mfma_f32_16x16x32_bf16 v[116:119], v[156:159], v[132:135], v[116:119]\n\t"
        "ds_read_b64_tr_b16 v[190:191], v61 offset:33792\n\t"
        "v_mfma_f32_16x16x32_bf16 v[120:123], v[156:159], v[136:139], v[120:123]\n\t"
        "ds_read_b128 v[200:203], v62 offset:2048\n\t"
        "v_mfma_f32_16x16x32_bf16 v[124:127], v[156:159], v[140:143], v[124:127]\n\t"
        "ds_read_b128 v[204:207], v62 offset:3072\n\t"
        "s_waitcnt lgkmcnt(8)\n\t"
        "v_mfma_f32_16x16x32_bf16 v[64:67], v[176:179], v[160:163], v[64:67]\n\t"
        "ds_read_b64_tr_b16 v[144:145], v58 offset:40960\n\t"
        "v_mfma_f32_16x16x32_bf16 v[68:71], v[176:179], v[164:167], v[68:71]\n\t"
        "ds_read_b64_tr_b16 v[146:147], v58 offset:41984\n\t"
        "v_mfma_f32_16x16x32_bf16 v[72:75], v[176:179], v[168:171], v[72:75]\n\t"
        "ds_read_b128 v[128:131], v57 offset:32768\n\t"
        "v_mfma_f32_16x16x32_bf16 v[76:79], v[176:179], v[172:175], v[76:79]\n\t"
        "ds_read_b128 v[132:135], v57 offset:34816\n\t"
        "s_waitcnt lgkmcnt(6)\n\t"
        "v_mfma_f32_16x16x32_bf16 v[80:83], v[180:183], v[160:163], v[80:83]\n\t"
        "ds_read_b128 v[136:139], v57 offset:36864\n\t"
        "v_mfma_f32_16x16x32_bf16 v[84:87], v[180:183], v[164:167], v[84:87]\n\t"
        "ds_read_b128 v[140:143], v57 offset:38912\n\t"
        "v_mfma_f32_16x16x32_bf16 v[88:91], v[180:183], v[168:171], v[88:91]\n\t"
        "ds_read_b64_tr_b16 v[148:149], v59 offset:40960\n\t"
        "v_mfma_f32_16x16x32_bf16 v[92:95], v[180:183], v[172:175], v[92:95]\n\t"
        "ds_read_b64_tr_b16 v[150:151], v59 offset:41984\n\t"
        "v_mfma_f32_16x16x32_bf16 v[96:99], v[184:187], v[160:163], v[96:99]\n\t"
        "ds_read_b64_tr_b16 v[152:153], v60 offset:40960\n\t"
        "v_mfma_f32_16x16x32_bf16 v[100:103], v[184:187], v[164:167], v[100:103]\n\t"
        "ds_read_b64_tr_b16 v[154:155], v60 offset:41984\n\t"
        "v_mfma_f32_16x16x32_bf16 v[104:107], v[184:187], v[168:171], v[104:107]\n\t"
        "ds_read_b64_tr_b16 v[156:157], v61 offset:40960\n\t"
        "v_mfma_f32_16x16x32_bf16 v[108:111], v[184:187], v[172:175], v[108:111]\n\t"
        "ds_read_b64_tr_b16 v[158:159], v61 offset:41984\n\t"
        "v_mfma_f32_16x16x32_bf16 v[112:115], v[188:191], v[160:163], v[112:115]\n\t"
        "v_add_u32_e32 v56, 0x10000, v56\n\t"
        "v_add_u32_e32 v57, 0x10000, v57\n\t"
        "v_add_u32_e32 v58, 0x10000, v58\n\t"
        "v_mfma_f32_16x16x32_bf16 v[116:119], v[188:191], v[164:167], v[116:119]\n\t"
        "v_add_u32_e32 v59, 0x10000, v59\n\t"
        "v_add_u32_e32 v60, 0x10000, v60\n\t"
        "v_add_u32_e32 v61, 0x10000, v61\n\t"
        "v_mfma_f32_16x16x32_bf16 v[120:123], v[188:191], v[168:171], v[120:123]\n\t"
        "ds_read_b128 v[208:211], v62 offset:32768\n\t"
        "v_mfma_f32_16x16x32_bf16 v[124:127], v[188:191], v[172:175], v[124:127]\n\t"
        "ds_read_b128 v[212:215], v62 offset:33792\n\t"
        "s_waitcnt lgkmcnt(8)\n\t"
        "v_mfma_f32_16x16x32_bf16 v[64:67], v[144:147], v[128:131], v[64:67]\n\t"
        "v_mfma_f32_16x16x32_bf16 v[68:71], v[144:147], v[132:135], v[68:71]\n\t"
        "s_waitcnt lgkmcnt(0)\n\t"
        "s_barrier\n\t"
        "v_mfma_f32_16x16x32_bf16 v[72:75], v[144:147], v[136:139], v[72:75]\n\t"
        "ds_read_b64_tr_b16 v[176:177], v58 offset:0\n\t"
        "v_mfma_f32_16x16x32_bf16 v[76:79], v[144:147], v[140:143], v[76:79]\n\t"
        "ds_read_b64_tr_b16 v[178:179], v58 offset:1024\n\t"
        "v_mfma_f32_16x16x32_bf16 v[80:83], v[148:151], v[128:131], v[80:83]\n\t"
        "ds_read_b128 v[160:163], v56 offset:0\n\t"
        "v_mfma_f32_16x16x32_bf16 v[84:87], v[148:151], v[132:135], v[84:87]\n\t"
        "ds_read_b128 v[164:167], v56 offset:2048\n\t"
        "v_mfma_f32_16x16x32_bf16 v[88:91], v[148:151], v[136:139], v[88:91]\n\t"
        "ds_read_b128 v[168:171], v56 offset:4096\n\t"
        "v_mfma_f32_16x16x32_bf16 v[92:95], v[148:151], v[140:143], v[92:95]\n\t"
        "ds_read_b128 v[172:175], v56 offset:6144\n\t"
        "v_mfma_f32_16x16x32_bf16 v[96:99], v[152:155], v[128:131], v[96:99]\n\t"
        "ds_read_b64_tr_b16 v[180:181], v59 offset:0\n\t"
        "v_mfma_f32_16x16x32_bf16 v[100:103], v[152:155], v[132:135], v[100:103]\n\t"
        "ds_read_b64_tr_b16 v[182:183], v59 offset:1024\n\t"
        "v_mfma_f32_16x16x32_bf16 v[104:107], v[152:155], v[136:139], v[104:107]\n\t"
        "ds_read_b64_tr_b16 v[184:185], v60 offset:0\n\t"
        "v_mfma_f32_16x16x32_bf16 v[108:111], v[152:155], v[140:143], v[108:111]\n\t"
        "ds_read_b64_tr_b16 v[186:187], v60 offset:1024\n\t"
        "v_mfma_f32_16x16x32_bf16 v[112:115], v[156:159], v[128:131], v[112:115]\n\t"
        "ds_read_b64_tr_b16 v[188:189], v61 offset:0\n\t"
        "v_mfma_f32_16x16x32_bf16 v[116:119], v[156:159], v[132:135], v[116:119]\n\t"
        "ds_read_b64_tr_b16 v[190:191], v61 offset:1024\n\t"
        "v_mfma_f32_16x16x32_bf16 v[120:123], v[156:159], v[136:139], v[120:123]\n\t"
        "ds_read_b128 v[216:219], v62 offset:34816\n\t"
        "v_mfma_f32_16x16x32_bf16 v[124:127], v[156:159], v[140:143], v[124:127]\n\t"
        "ds_read_b128 v[220:223], v62 offset:35840\n\t"
        "s_waitcnt lgkmcnt(8)\n\t"
        "v_mfma_f32_16x16x32_bf16 v[64:67], v[176:179], v[160:163], v[64:67]\n\t"
        "ds_read_b64_tr_b16 v[144:145], v58 offset:8192\n\t"
        "v_mfma_f32_16x16x32_bf16 v[68:71], v[176:179], v[164:167], v[68:71]\n\t"
        "ds_read_b64_tr_b16 v[146:147], v58 offset:9216\n\t"
        "v_mfma_f32_16x16x32_bf16 v[72:75], v[176:179], v[168:171], v[72:75]\n\t"
        "ds_read_b128 v[128:131], v57 offset:0\n\t"
        "v_mfma_f32_16x16x32_bf16 v[76:79], v[176:179], v[172:175], v[76:79]\n\t"
        "ds_read_b128 v[132:135], v57 offset:2048\n\t"
        "s_waitcnt lgkmcnt(6)\n\t"
        "v_mfma_f32_16x16x32_bf16 v[80:83], v[180:183], v[160:163], v[80:83]\n\t"
        "ds_read_b128 v[136:139], v57 offset:4096\n\t"
        "v_mfma_f32_16x16x32_bf16 v[84:87], v[180:183], v[164:167], v[84:87]\n\t"
        "ds_read_b128 v[140:143], v57 offset:6144\n\t"
        "v_mfma_f32_16x16x32_bf16 v[88:91], v[180:183], v[168:171], v[88:91]\n\t"
        "ds_read_b64_tr_b16 v[148:149], v59 offset:8192\n\t"
        "v_mfma_f32_16x16x32_bf16 v[92:95], v[180:183], v[172:175], v[92:95]\n\t"
        "ds_read_b64_tr_b16 v[150:151], v59 offset:9216\n\t"
        "v_mfma_f32_16x16x32_bf16 v[96:99], v[184:187], v[160:163], v[96:99]\n\t"
        "ds_read_b64_tr_b16 v[152:153], v60 offset:8192\n\t"
        "v_mfma_f32_16x16x32_bf16 v[100:103], v[184:187], v[164:167], v[100:103]\n\t"
        "ds_read_b64_tr_b16 v[154:155], v60 offset:9216\n\t"
        "v_mfma_f32_16x16x32_bf16 v[104:107], v[184:187], v[168:171], v[104:107]\n\t"
        "ds_read_b64_tr_b16 v[156:157], v61 offset:8192\n\t"
        "v_mfma_f32_16x16x32_bf16 v[108:111], v[184:187], v[172:175], v[108:111]\n\t"
        "ds_read_b64_tr_b16 v[158:159], v61 offset:9216\n\t"
        "v_mfma_f32_16x16x32_bf16 v[112:115], v[188:191], v[160:163], v[112:115]\n\t"
        "v_add_u32_e32 v62, 0x10000, v62\n\t"
        "ds_read_b128 v[224:227], v62 offset:0\n\t"
        "v_mfma_f32_16x16x32_bf16 v[116:119], v[188:191], v[164:167], v[116:119]\n\t"
        "ds_read_b128 v[228:231], v62 offset:1024\n\t"
        "v_mfma_f32_16x16x32_bf16 v[120:123], v[188:191], v[168:171], v[120:123]\n\t"
        "v_mfma_f32_16x16x32_bf16 v[124:127], v[188:191], v[172:175], v[124:127]\n\t"
        "s_waitcnt lgkmcnt(8)\n\t"
        "v_mfma_f32_16x16x32_bf16 v[64:67], v[144:147], v[128:131], v[64:67]\n\t"
        "v_mfma_f32_16x16x32_bf16 v[68:71], v[144:147], v[132:135], v[68:71]\n\t"
        "s_waitcnt lgkmcnt(0)\n\t"
        "s_barrier\n\t"
        "v_mfma_f32_16x16x32_bf16 v[72:75], v[144:147], v[136:139], v[72:75]\n\t"
        "ds_read_b64_tr_b16 v[176:177], v58 offset:32768\n\t"
        "v_mfma_f32_16x16x32_bf16 v[76:79], v[144:147], v[140:143], v[76:79]\n\t"
        "ds_read_b64_tr_b16 v[178:179], v58 offset:33792\n\t"
        "v_mfma_f32_16x16x32_bf16 v[80:83], v[148:151], v[128:131], v[80:83]\n\t"
        "ds_read_b128 v[160:163], v56 offset:32768\n\t"
        "v_mfma_f32_16x16x32_bf16 v[84:87], v[148:151], v[132:135], v[84:87]\n\t"
        "ds_read_b128 v[164:167], v56 offset:34816\n\t"
        "v_mfma_f32_16x16x32_bf16 v[88:91], v[148:151], v[136:139], v[88:91]\n\t"
        "ds_read_b128 v[168:171], v56 offset:36864\n\t"
        "v_mfma_f32_16x16x32_bf16 v[92:95], v[148:151], v[140:143], v[92:95]\n\t"
        "ds_read_b128 v[172:175], v56 offset:38912\n\t"
        "v_mfma_f32_16x16x32_bf16 v[96:99], v[152:155], v[128:131], v[96:99]\n\t"
        "ds_read_b64_tr_b16 v[180:181], v59 offset:32768\n\t"
        "v_mfma_f32_16x16x32_bf16 v[100:103], v[152:155], v[132:135], v[100:103]\n\t"
        "ds_read_b64_tr_b16 v[182:183], v59 offset:33792\n\t"
        "v_mfma_f32_16x16x32_bf16 v[104:107], v[152:155], v[136:139], v[104:107]\n\t"
        "ds_read_b64_tr_b16 v[184:185], v60 offset:32768\n\t"
        "v_mfma_f32_16x16x32_bf16 v[108:111], v[152:155], v[140:143], v[108:111]\n\t"
        "ds_read_b64_tr_b16 v[186:187], v60 offset:33792\n\t"
        "v_mfma_f32_16x16x32_bf16 v[112:115], v[156:159], v[128:131], v[112:115]\n\t"
        "ds_read_b64_tr_b16 v[188:189], v61 offset:32768\n\t"
        "v_mfma_f32_16x16x32_bf16 v[116:119], v[156:159], v[132:135], v[116:119]\n\t"
        "ds_read_b64_tr_b16 v[190:191], v61 offset:33792\n\t"
        "v_mfma_f32_16x16x32_bf16 v[120:123], v[156:159], v[136:139], v[120:123]\n\t"
        "ds_read_b128 v[232:235], v62 offset:2048\n\t"
        "v_mfma_f32_16x16x32_bf16 v[124:127], v[156:159], v[140:143], v[124:127]\n\t"
        "ds_read_b128 v[236:239], v62 offset:3072\n\t"
        "s_waitcnt lgkmcnt(8)\n\t"
        "v_mfma_f32_16x16x32_bf16 v[64:67], v[176:179], v[160:163], v[64:67]\n\t"
        "ds_read_b64_tr_b16 v[144:145], v58 offset:40960\n\t"
        "v_mfma_f32_16x16x32_bf16 v[68:71], v[176:179], v[164:167], v[68:71]\n\t"
        "ds_read_b64_tr_b16 v[146:147], v58 offset:41984\n\t"
        "v_mfma_f32_16x16x32_bf16 v[72:75], v[176:179], v[168:171], v[72:75]\n\t"
        "ds_read_b128 v[128:131], v57 offset:32768\n\t"
        "v_mfma_f32_16x16x32_bf16 v[76:79], v[176:179], v[172:175], v[76:79]\n\t"
        "ds_read_b128 v[132:135], v57 offset:34816\n\t"
        "s_waitcnt lgkmcnt(6)\n\t"
        "v_mfma_f32_16x16x32_bf16 v[80:83], v[180:183], v[160:163], v[80:83]\n\t"
        "ds_read_b128 v[136:139], v57 offset:36864\n\t"
        "v_mfma_f32_16x16x32_bf16 v[84:87], v[180:183], v[164:167], v[84:87]\n\t"
        "ds_read_b128 v[140:143], v57 offset:38912\n\t"
        "v_mfma_f32_16x16x32_bf16 v[88:91], v[180:183], v[168:171], v[88:91]\n\t"
        "ds_read_b64_tr_b16 v[148:149], v59 offset:40960\n\t"
        "v_mfma_f32_16x16x32_bf16 v[92:95], v[180:183], v[172:175], v[92:95]\n\t"
        "ds_read_b64_tr_b16 v[150:151], v59 offset:41984\n\t"
        "v_mfma_f32_16x16x32_bf16 v[96:99], v[184:187], v[160:163], v[96:99]\n\t"
        "ds_read_b64_tr_b16 v[152:153], v60 offset:40960\n\t"
        "v_mfma_f32_16x16x32_bf16 v[100:103], v[184:187], v[164:167], v[100:103]\n\t"
        "ds_read_b64_tr_b16 v[154:155], v60 offset:41984\n\t"
        "v_mfma_f32_16x16x32_bf16 v[104:107], v[184:187], v[168:171], v[104:107]\n\t"
        "ds_read_b64_tr_b16 v[156:157], v61 offset:40960\n\t"
        "v_mfma_f32_16x16x32_bf16 v[108:111], v[184:187], v[172:175], v[108:111]\n\t"
        "ds_read_b64_tr_b16 v[158:159], v61 offset:41984\n\t"
        "v_mfma_f32_16x16x32_bf16 v[112:115], v[188:191], v[160:163], v[112:115]\n\t"
        "v_add_u32_e32 v56, 0x10000, v56\n\t"
        "v_add_u32_e32 v57, 0x10000, v57\n\t"
        "v_add_u32_e32 v58, 0x10000, v58\n\t"
        "v_mfma_f32_16x16x32_bf16 v[116:119], v[188:191], v[164:167], v[116:119]\n\t"
        "v_add_u32_e32 v59, 0x10000, v59\n\t"
        "v_add_u32_e32 v60, 0x10000, v60\n\t"
        "v_add_u32_e32 v61, 0x10000, v61\n\t"
        "v_mfma_f32_16x16x32_bf16 v[120:123], v[188:191], v[168:171], v[120:123]\n\t"
        "v_mfma_f32_16x16x32_bf16 v[124:127], v[188:191], v[172:175], v[124:127]\n\t"
        "s_waitcnt lgkmcnt(6)\n\t"
        "v_mfma_f32_16x16x32_bf16 v[64:67], v[144:147], v[128:131], v[64:67]\n\t"
        "v_mfma_f32_16x16x32_bf16 v[68:71], v[144:147], v[132:135], v[68:71]\n\t"
        "s_waitcnt lgkmcnt(0)\n\t"
        "s_barrier\n\t"
        "v_mfma_f32_16x16x32_bf16 v[72:75], v[144:147], v[136:139], v[72:75]\n\t"
        "ds_read_b64_tr_b16 v[176:177], v58 offset:0\n\t"
        "v_mfma_f32_16x16x32_bf16 v[76:79], v[144:147], v[140:143], v[76:79]\n\t"
        "ds_read_b64_tr_b16 v[178:179], v58 offset:1024\n\t"
        "v_mfma_f32_16x16x32_bf16 v[80:83], v[148:151], v[128:131], v[80:83]\n\t"
        "ds_read_b128 v[160:163], v56 offset:0\n\t"
        "v_mfma_f32_16x16x32_bf16 v[84:87], v[148:151], v[132:135], v[84:87]\n\t"
        "ds_read_b128 v[164:167], v56 offset:2048\n\t"
        "v_mfma_f32_16x16x32_bf16 v[88:91], v[148:151], v[136:139], v[88:91]\n\t"
        "ds_read_b128 v[168:171], v56 offset:4096\n\t"
        "v_mfma_f32_16x16x32_bf16 v[92:95], v[148:151], v[140:143], v[92:95]\n\t"
        "ds_read_b128 v[172:175], v56 offset:6144\n\t"
        "v_mfma_f32_16x16x32_bf16 v[96:99], v[152:155], v[128:131], v[96:99]\n\t"
        "ds_read_b64_tr_b16 v[180:181], v59 offset:0\n\t"
        "v_mfma_f32_16x16x32_bf16 v[100:103], v[152:155], v[132:135], v[100:103]\n\t"
        "ds_read_b64_tr_b16 v[182:183], v59 offset:1024\n\t"
        "v_mfma_f32_16x16x32_bf16 v[104:107], v[152:155], v[136:139], v[104:107]\n\t"
        "ds_read_b64_tr_b16 v[184:185], v60 offset:0\n\t"
        "v_mfma_f32_16x16x32_bf16 v[108:111], v[152:155], v[140:143], v[108:111]\n\t"
        "ds_read_b64_tr_b16 v[186:187], v60 offset:1024\n\t"
        "v_mfma_f32_16x16x32_bf16 v[112:115], v[156:159], v[128:131], v[112:115]\n\t"
        "ds_read_b64_tr_b16 v[188:189], v61 offset:0\n\t"
        "v_mfma_f32_16x16x32_bf16 v[116:119], v[156:159], v[132:135], v[116:119]\n\t"
        "ds_read_b64_tr_b16 v[190:191], v61 offset:1024\n\t"
        "v_mfma_f32_16x16x32_bf16 v[120:123], v[156:159], v[136:139], v[120:123]\n\t"
        "v_mfma_f32_16x16x32_bf16 v[124:127], v[156:159], v[140:143], v[124:127]\n\t"
        "s_waitcnt lgkmcnt(6)\n\t"
        "v_mfma_f32_16x16x32_bf16 v[64:67], v[176:179], v[160:163], v[64:67]\n\t"
        "ds_read_b64_tr_b16 v[144:145], v58 offset:8192\n\t"
        "v_mfma_f32_16x16x32_bf16 v[68:71], v[176:179], v[164:167], v[68:71]\n\t"
        "ds_read_b64_tr_b16 v[146:147], v58 offset:9216\n\t"
        "v_mfma_f32_16x16x32_bf16 v[72:75], v[176:179], v[168:171], v[72:75]\n\t"
        "ds_read_b128 v[128:131], v57 offset:0\n\t"
        "v_mfma_f32_16x16x32_bf16 v[76:79], v[176:179], v[172:175], v[76:79]\n\t"
        "ds_read_b128 v[132:135], v57 offset:2048\n\t"
        "s_waitcnt lgkmcnt(4)\n\t"
        "v_mfma_f32_16x16x32_bf16 v[80:83], v[180:183], v[160:163], v[80:83]\n\t"
        "ds_read_b128 v[136:139], v57 offset:4096\n\t"
        "v_mfma_f32_16x16x32_bf16 v[84:87], v[180:183], v[164:167], v[84:87]\n\t"
        "ds_read_b128 v[140:143], v57 offset:6144\n\t"
        "v_mfma_f32_16x16x32_bf16 v[88:91], v[180:183], v[168:171], v[88:91]\n\t"
        "ds_read_b64_tr_b16 v[148:149], v59 offset:8192\n\t"
        "v_mfma_f32_16x16x32_bf16 v[92:95], v[180:183], v[172:175], v[92:95]\n\t"
        "ds_read_b64_tr_b16 v[150:151], v59 offset:9216\n\t"
        "v_mfma_f32_16x16x32_bf16 v[96:99], v[184:187], v[160:163], v[96:99]\n\t"
        "ds_read_b64_tr_b16 v[152:153], v60 offset:8192\n\t"
        "v_mfma_f32_16x16x32_bf16 v[100:103], v[184:187], v[164:167], v[100:103]\n\t"
        "ds_read_b64_tr_b16 v[154:155], v60 offset:9216\n\t"
        "v_mfma_f32_16x16x32_bf16 v[104:107], v[184:187], v[168:171], v[104:107]\n\t"
        "ds_read_b64_tr_b16 v[156:157], v61 offset:8192\n\t"
        "v_mfma_f32_16x16x32_bf16 v[108:111], v[184:187], v[172:175], v[108:111]\n\t"
        "ds_read_b64_tr_b16 v[158:159], v61 offset:9216\n\t"
        "v_mfma_f32_16x16x32_bf16 v[112:115], v[188:191], v[160:163], v[112:115]\n\t"
        "v_add_u32_e32 v56, 0xfffe0000, v56\n\t"
        "v_add_u32_e32 v57, 0xfffe0000, v57\n\t"
        "v_add_u32_e32 v58, 0xfffe0000, v58\n\t"
        "v_mfma_f32_16x16x32_bf16 v[116:119], v[188:191], v[164:167], v[116:119]\n\t"
        "v_add_u32_e32 v59, 0xfffe0000, v59\n\t"
        "v_add_u32_e32 v60, 0xfffe0000, v60\n\t"
        "v_add_u32_e32 v61, 0xfffe0000, v61\n\t"
        "v_mfma_f32_16x16x32_bf16 v[120:123], v[188:191], v[168:171], v[120:123]\n\t"
        "v_mfma_f32_16x16x32_bf16 v[124:127], v[188:191], v[172:175], v[124:127]\n\t"
        "s_waitcnt lgkmcnt(6)\n\t"
        "v_mfma_f32_16x16x32_bf16 v[64:67], v[144:147], v[128:131], v[64:67]\n\t"
        "v_mfma_f32_16x16x32_bf16 v[68:71], v[144:147], v[132:135], v[68:71]\n\t"
        "s_waitcnt lgkmcnt(0)\n\t"
        "s_barrier\n\t"
        "v_mfma_f32_16x16x32_bf16 v[72:75], v[144:147], v[136:139], v[72:75]\n\t"
        "ds_read_b64_tr_b16 v[176:177], v58 offset:0\n\t"
        "v_mfma_f32_16x16x32_bf16 v[76:79], v[144:147], v[140:143], v[76:79]\n\t"
        "ds_read_b64_tr_b16 v[178:179], v58 offset:1024\n\t"
        "v_mfma_f32_16x16x32_bf16 v[80:83], v[148:151], v[128:131], v[80:83]\n\t"
        "ds_read_b128 v[160:163], v56 offset:0\n\t"
        "v_mfma_f32_16x16x32_bf16 v[84:87], v[148:151], v[132:135], v[84:87]\n\t"
        "ds_read_b128 v[164:167], v56 offset:2048\n\t"
        "v_mfma_f32_16x16x32_bf16 v[88:91], v[148:151], v[136:139], v[88:91]\n\t"
        "ds_read_b128 v[168:171], v56 offset:4096\n\t"
        "v_mfma_f32_16x16x32_bf16 v[92:95], v[148:151], v[140:143], v[92:95]\n\t"
        "ds_read_b128 v[172:175], v56 offset:6144\n\t"
        "v_mfma_f32_16x16x32_bf16 v[96:99], v[152:155], v[128:131], v[96:99]\n\t"
        "ds_read_b64_tr_b16 v[180:181], v59 offset:0\n\t"
        "v_mfma_f32_16x16x32_bf16 v[100:103], v[152:155], v[132:135], v[100:103]\n\t"
        "ds_read_b64_tr_b16 v[182:183], v59 offset:1024\n\t"
        "v_mfma_f32_16x16x32_bf16 v[104:107], v[152:155], v[136:139], v[104:107]\n\t"
        "ds_read_b64_tr_b16 v[184:185], v60 offset:0\n\t"
        "v_mfma_f32_16x16x32_bf16 v[108:111], v[152:155], v[140:143], v[108:111]\n\t"
        "ds_read_b64_tr_b16 v[186:187], v60 offset:1024\n\t"
        "v_mfma_f32_16x16x32_bf16 v[112:115], v[156:159], v[128:131], v[112:115]\n\t"
        "ds_read_b64_tr_b16 v[188:189], v61 offset:0\n\t"
        "v_mfma_f32_16x16x32_bf16 v[116:119], v[156:159], v[132:135], v[116:119]\n\t"
        "ds_read_b64_tr_b16 v[190:191], v61 offset:1024\n\t"
        "v_mfma_f32_16x16x32_bf16 v[120:123], v[156:159], v[136:139], v[120:123]\n\t"
        "v_mfma_f32_16x16x32_bf16 v[124:127], v[156:159], v[140:143], v[124:127]\n\t"
        "s_waitcnt lgkmcnt(6)\n\t"
        "v_mfma_f32_16x16x32_bf16 v[64:67], v[176:179], v[160:163], v[64:67]\n\t"
        "ds_read_b64_tr_b16 v[144:145], v58 offset:8192\n\t"
        "v_mfma_f32_16x16x32_bf16 v[68:71], v[176:179], v[164:167], v[68:71]\n\t"
        "ds_read_b64_tr_b16 v[146:147], v58 offset:9216\n\t"
        "v_mfma_f32_16x16x32_bf16 v[72:75], v[176:179], v[168:171], v[72:75]\n\t"
        "ds_read_b128 v[128:131], v57 offset:0\n\t"
        "v_mfma_f32_16x16x32_bf16 v[76:79], v[176:179], v[172:175], v[76:79]\n\t"
        "ds_read_b128 v[132:135], v57 offset:2048\n\t"
        "s_waitcnt lgkmcnt(4)\n\t"
        "v_mfma_f32_16x16x32_bf16 v[80:83], v[180:183], v[160:163], v[80:83]\n\t"
        "ds_read_b128 v[136:139], v57 offset:4096\n\t"
        "v_mfma_f32_16x16x32_bf16 v[84:87], v[180:183], v[164:167], v[84:87]\n\t"
        "ds_read_b128 v[140:143], v57 offset:6144\n\t"
        "v_mfma_f32_16x16x32_bf16 v[88:91], v[180:183], v[168:171], v[88:91]\n\t"
        "ds_read_b64_tr_b16 v[148:149], v59 offset:8192\n\t"
        "v_mfma_f32_16x16x32_bf16 v[92:95], v[180:183], v[172:175], v[92:95]\n\t"
        "ds_read_b64_tr_b16 v[150:151], v59 offset:9216\n\t"
        "v_mfma_f32_16x16x32_bf16 v[96:99], v[184:187], v[160:163], v[96:99]\n\t"
        "ds_read_b64_tr_b16 v[152:153], v60 offset:8192\n\t"
        "v_mfma_f32_16x16x32_bf16 v[100:103], v[184:187], v[164:167], v[100:103]\n\t"
        "ds_read_b64_tr_b16 v[154:155], v60 offset:9216\n\t"
        "v_mfma_f32_16x16x32_bf16 v[104:107], v[184:187], v[168:171], v[104:107]\n\t"
        "ds_read_b64_tr_b16 v[156:157], v61 offset:8192\n\t"
        "v_mfma_f32_16x16x32_bf16 v[108:111], v[184:187], v[172:175], v[108:111]\n\t"
        "ds_read_b64_tr_b16 v[158:159], v61 offset:9216\n\t"
        "v_mfma_f32_16x16x32_bf16 v[112:115], v[188:191], v[160:163], v[112:115]\n\t"
        "v_mfma_f32_16x16x32_bf16 v[116:119], v[188:191], v[164:167], v[116:119]\n\t"
        "v_mfma_f32_16x16x32_bf16 v[120:123], v[188:191], v[168:171], v[120:123]\n\t"
        "v_mfma_f32_16x16x32_bf16 v[124:127], v[188:191], v[172:175], v[124:127]\n\t"
        "s_waitcnt lgkmcnt(6)\n\t"
        "v_mfma_f32_16x16x32_bf16 v[64:67], v[144:147], v[128:131], v[64:67]\n\t"
        "v_mfma_f32_16x16x32_bf16 v[68:71], v[144:147], v[132:135], v[68:71]\n\t"
        "s_waitcnt lgkmcnt(0)\n\t"
        "s_barrier\n\t"
        "v_mfma_f32_16x16x32_bf16 v[72:75], v[144:147], v[136:139], v[72:75]\n\t"
        "ds_read_b64_tr_b16 v[176:177], v58 offset:32768\n\t"
        "v_mfma_f32_16x16x32_bf16 v[76:79], v[144:147], v[140:143], v[76:79]\n\t"
        "ds_read_b64_tr_b16 v[178:179], v58 offset:33792\n\t"
        "v_mfma_f32_16x16x32_bf16 v[80:83], v[148:151], v[128:131], v[80:83]\n\t"
        "ds_read_b128 v[160:163], v56 offset:32768\n\t"
        "v_mfma_f32_16x16x32_bf16 v[84:87], v[148:151], v[132:135], v[84:87]\n\t"
        "ds_read_b128 v[164:167], v56 offset:34816\n\t"
        "v_mfma_f32_16x16x32_bf16 v[88:91], v[148:151], v[136:139], v[88:91]\n\t"
        "ds_read_b128 v[168:171], v56 offset:36864\n\t"
        "v_mfma_f32_16x16x32_bf16 v[92:95], v[148:151], v[140:143], v[92:95]\n\t"
        "ds_read_b128 v[172:175], v56 offset:38912\n\t"
        "v_mfma_f32_16x16x32_bf16 v[96:99], v[152:155], v[128:131], v[96:99]\n\t"
        "ds_read_b64_tr_b16 v[180:181], v59 offset:32768\n\t"
        "v_mfma_f32_16x16x32_bf16 v[100:103], v[152:155], v[132:135], v[100:103]\n\t"
        "ds_read_b64_tr_b16 v[182:183], v59 offset:33792\n\t"
        "v_mfma_f32_16x16x32_bf16 v[104:107], v[152:155], v[136:139], v[104:107]\n\t"
        "ds_read_b64_tr_b16 v[184:185], v60 offset:32768\n\t"
        "v_mfma_f32_16x16x32_bf16 v[108:111], v[152:155], v[140:143], v[108:111]\n\t"
        "ds_read_b64_tr_b16 v[186:187], v60 offset:33792\n\t"
        "v_mfma_f32_16x16x32_bf16 v[112:115], v[156:159], v[128:131], v[112:115]\n\t"
        "ds_read_b64_tr_b16 v[188:189], v61 offset:32768\n\t"
        "v_mfma_f32_16x16x32_bf16 v[116:119], v[156:159], v[132:135], v[116:119]\n\t"
        "ds_read_b64_tr_b16 v[190:191], v61 offset:33792\n\t"
        "v_mfma_f32_16x16x32_bf16 v[120:123], v[156:159], v[136:139], v[120:123]\n\t"
        "v_mfma_f32_16x16x32_bf16 v[124:127], v[156:159], v[140:143], v[124:127]\n\t"
        "s_waitcnt lgkmcnt(6)\n\t"
        "v_mfma_f32_16x16x32_bf16 v[64:67], v[176:179], v[160:163], v[64:67]\n\t"
        "ds_read_b64_tr_b16 v[144:145], v58 offset:40960\n\t"
        "v_mfma_f32_16x16x32_bf16 v[68:71], v[176:179], v[164:167], v[68:71]\n\t"
        "ds_read_b64_tr_b16 v[146:147], v58 offset:41984\n\t"
        "v_mfma_f32_16x16x32_bf16 v[72:75], v[176:179], v[168:171], v[72:75]\n\t"
        "ds_read_b128 v[128:131], v57 offset:32768\n\t"
        "v_mfma_f32_16x16x32_bf16 v[76:79], v[176:179], v[172:175], v[76:79]\n\t"
        "ds_read_b128 v[132:135], v57 offset:34816\n\t"
        "s_waitcnt lgkmcnt(4)\n\t"
        "v_mfma_f32_16x16x32_bf16 v[80:83], v[180:183], v[160:163], v[80:83]\n\t"
        "ds_read_b128 v[136:139], v57 offset:36864\n\t"
        "v_mfma_f32_16x16x32_bf16 v[84:87], v[180:183], v[164:167], v[84:87]\n\t"
        "ds_read_b128 v[140:143], v57 offset:38912\n\t"
        "v_mfma_f32_16x16x32_bf16 v[88:91], v[180:183], v[168:171], v[88:91]\n\t"
        "ds_read_b64_tr_b16 v[148:149], v59 offset:40960\n\t"
        "v_mfma_f32_16x16x32_bf16 v[92:95], v[180:183], v[172:175], v[92:95]\n\t"
        "ds_read_b64_tr_b16 v[150:151], v59 offset:41984\n\t"
        "v_mfma_f32_16x16x32_bf16 v[96:99], v[184:187], v[160:163], v[96:99]\n\t"
        "ds_read_b64_tr_b16 v[152:153], v60 offset:40960\n\t"
        "v_mfma_f32_16x16x32_bf16 v[100:103], v[184:187], v[164:167], v[100:103]\n\t"
        "ds_read_b64_tr_b16 v[154:155], v60 offset:41984\n\t"
        "v_mfma_f32_16x16x32_bf16 v[104:107], v[184:187], v[168:171], v[104:107]\n\t"
        "ds_read_b64_tr_b16 v[156:157], v61 offset:40960\n\t"
        "v_mfma_f32_16x16x32_bf16 v[108:111], v[184:187], v[172:175], v[108:111]\n\t"
        "ds_read_b64_tr_b16 v[158:159], v61 offset:41984\n\t"
        "v_mfma_f32_16x16x32_bf16 v[112:115], v[188:191], v[160:163], v[112:115]\n\t"
        "v_add_u32_e32 v56, 0x10000, v56\n\t"
        "v_add_u32_e32 v57, 0x10000, v57\n\t"
        "v_add_u32_e32 v58, 0x10000, v58\n\t"
        "v_mfma_f32_16x16x32_bf16 v[116:119], v[188:191], v[164:167], v[116:119]\n\t"
        "v_add_u32_e32 v59, 0x10000, v59\n\t"
        "v_add_u32_e32 v60, 0x10000, v60\n\t"
        "v_add_u32_e32 v61, 0x10000, v61\n\t"
        "v_mfma_f32_16x16x32_bf16 v[120:123], v[188:191], v[168:171], v[120:123]\n\t"
        "v_mfma_f32_16x16x32_bf16 v[124:127], v[188:191], v[172:175], v[124:127]\n\t"
        "s_waitcnt lgkmcnt(6)\n\t"
        "v_mfma_f32_16x16x32_bf16 v[64:67], v[144:147], v[128:131], v[64:67]\n\t"
        "v_mfma_f32_16x16x32_bf16 v[68:71], v[144:147], v[132:135], v[68:71]\n\t"
        "s_waitcnt lgkmcnt(0)\n\t"
        "s_barrier\n\t"
        "v_mfma_f32_16x16x32_bf16 v[72:75], v[144:147], v[136:139], v[72:75]\n\t"
        "ds_read_b64_tr_b16 v[176:177], v58 offset:0\n\t"
        "v_mfma_f32_16x16x32_bf16 v[76:79], v[144:147], v[140:143], v[76:79]\n\t"
        "ds_read_b64_tr_b16 v[178:179], v58 offset:1024\n\t"
        "v_mfma_f32_16x16x32_bf16 v[80:83], v[148:151], v[128:131], v[80:83]\n\t"
        "ds_read_b128 v[160:163], v56 offset:0\n\t"
        "v_mfma_f32_16x16x32_bf16 v[84:87], v[148:151], v[132:135], v[84:87]\n\t"
        "ds_read_b128 v[164:167], v56 offset:2048\n\t"
        "v_mfma_f32_16x16x32_bf16 v[88:91], v[148:151], v[136:139], v[88:91]\n\t"
        "ds_read_b128 v[168:171], v56 offset:4096\n\t"
        "v_mfma_f32_16x16x32_bf16 v[92:95], v[148:151], v[140:143], v[92:95]\n\t"
        "ds_read_b128 v[172:175], v56 offset:6144\n\t"
        "v_mfma_f32_16x16x32_bf16 v[96:99], v[152:155], v[128:131], v[96:99]\n\t"
        "ds_read_b64_tr_b16 v[180:181], v59 offset:0\n\t"
        "v_mfma_f32_16x16x32_bf16 v[100:103], v[152:155], v[132:135], v[100:103]\n\t"
        "ds_read_b64_tr_b16 v[182:183], v59 offset:1024\n\t"
        "v_mfma_f32_16x16x32_bf16 v[104:107], v[152:155], v[136:139], v[104:107]\n\t"
        "ds_read_b64_tr_b16 v[184:185], v60 offset:0\n\t"
        "v_mfma_f32_16x16x32_bf16 v[108:111], v[152:155], v[140:143], v[108:111]\n\t"
        "ds_read_b64_tr_b16 v[186:187], v60 offset:1024\n\t"
        "v_mfma_f32_16x16x32_bf16 v[112:115], v[156:159], v[128:131], v[112:115]\n\t"
        "ds_read_b64_tr_b16 v[188:189], v61 offset:0\n\t"
        "v_mfma_f32_16x16x32_bf16 v[116:119], v[156:159], v[132:135], v[116:119]\n\t"
        "ds_read_b64_tr_b16 v[190:191], v61 offset:1024\n\t"
        "v_mfma_f32_16x16x32_bf16 v[120:123], v[156:159], v[136:139], v[120:123]\n\t"
        "v_mfma_f32_16x16x32_bf16 v[124:127], v[156:159], v[140:143], v[124:127]\n\t"
        "s_waitcnt lgkmcnt(6)\n\t"
        "v_mfma_f32_16x16x32_bf16 v[64:67], v[176:179], v[160:163], v[64:67]\n\t"
        "ds_read_b64_tr_b16 v[144:145], v58 offset:8192\n\t"
        "v_mfma_f32_16x16x32_bf16 v[68:71], v[176:179], v[164:167], v[68:71]\n\t"
        "ds_read_b64_tr_b16 v[146:147], v58 offset:9216\n\t"
        "v_mfma_f32_16x16x32_bf16 v[72:75], v[176:179], v[168:171], v[72:75]\n\t"
        "ds_read_b128 v[128:131], v57 offset:0\n\t"
        "v_mfma_f32_16x16x32_bf16 v[76:79], v[176:179], v[172:175], v[76:79]\n\t"
        "ds_read_b128 v[132:135], v57 offset:2048\n\t"
        "s_waitcnt lgkmcnt(4)\n\t"
        "v_mfma_f32_16x16x32_bf16 v[80:83], v[180:183], v[160:163], v[80:83]\n\t"
        "ds_read_b128 v[136:139], v57 offset:4096\n\t"
        "v_mfma_f32_16x16x32_bf16 v[84:87], v[180:183], v[164:167], v[84:87]\n\t"
        "ds_read_b128 v[140:143], v57 offset:6144\n\t"
        "v_mfma_f32_16x16x32_bf16 v[88:91], v[180:183], v[168:171], v[88:91]\n\t"
        "ds_read_b64_tr_b16 v[148:149], v59 offset:8192\n\t"
        "v_mfma_f32_16x16x32_bf16 v[92:95], v[180:183], v[172:175], v[92:95]\n\t"
        "ds_read_b64_tr_b16 v[150:151], v59 offset:9216\n\t"
        "v_mfma_f32_16x16x32_bf16 v[96:99], v[184:187], v[160:163], v[96:99]\n\t"
        "ds_read_b64_tr_b16 v[152:153], v60 offset:8192\n\t"
        "v_mfma_f32_16x16x32_bf16 v[100:103], v[184:187], v[164:167], v[100:103]\n\t"
        "ds_read_b64_tr_b16 v[154:155], v60 offset:9216\n\t"
        "v_mfma_f32_16x16x32_bf16 v[104:107], v[184:187], v[168:171], v[104:107]\n\t"
        "ds_read_b64_tr_b16 v[156:157], v61 offset:8192\n\t"
        "v_mfma_f32_16x16x32_bf16 v[108:111], v[184:187], v[172:175], v[108:111]\n\t"
        "ds_read_b64_tr_b16 v[158:159], v61 offset:9216\n\t"
        "v_mfma_f32_16x16x32_bf16 v[112:115], v[188:191], v[160:163], v[112:115]\n\t"
        "v_mfma_f32_16x16x32_bf16 v[116:119], v[188:191], v[164:167], v[116:119]\n\t"
        "v_mfma_f32_16x16x32_bf16 v[120:123], v[188:191], v[168:171], v[120:123]\n\t"
        "v_mfma_f32_16x16x32_bf16 v[124:127], v[188:191], v[172:175], v[124:127]\n\t"
        "s_waitcnt lgkmcnt(6)\n\t"
        "v_mfma_f32_16x16x32_bf16 v[64:67], v[144:147], v[128:131], v[64:67]\n\t"
        "v_mfma_f32_16x16x32_bf16 v[68:71], v[144:147], v[132:135], v[68:71]\n\t"
        "s_waitcnt lgkmcnt(0)\n\t"
        "s_barrier\n\t"
        "v_mfma_f32_16x16x32_bf16 v[72:75], v[144:147], v[136:139], v[72:75]\n\t"
        "ds_read_b64_tr_b16 v[176:177], v58 offset:32768\n\t"
        "v_mfma_f32_16x16x32_bf16 v[76:79], v[144:147], v[140:143], v[76:79]\n\t"
        "ds_read_b64_tr_b16 v[178:179], v58 offset:33792\n\t"
        "v_mfma_f32_16x16x32_bf16 v[80:83], v[148:151], v[128:131], v[80:83]\n\t"
        "ds_read_b128 v[160:163], v56 offset:32768\n\t"
        "v_mfma_f32_16x16x32_bf16 v[84:87], v[148:151], v[132:135], v[84:87]\n\t"
        "ds_read_b128 v[164:167], v56 offset:34816\n\t"
        "v_mfma_f32_16x16x32_bf16 v[88:91], v[148:151], v[136:139], v[88:91]\n\t"
        "ds_read_b128 v[168:171], v56 offset:36864\n\t"
        "v_mfma_f32_16x16x32_bf16 v[92:95], v[148:151], v[140:143], v[92:95]\n\t"
        "ds_read_b128 v[172:175], v56 offset:38912\n\t"
        "v_mfma_f32_16x16x32_bf16 v[96:99], v[152:155], v[128:131], v[96:99]\n\t"
        "ds_read_b64_tr_b16 v[180:181], v59 offset:32768\n\t"
        "v_mfma_f32_16x16x32_bf16 v[100:103], v[152:155], v[132:135], v[100:103]\n\t"
        "ds_read_b64_tr_b16 v[182:183], v59 offset:33792\n\t"
        "v_mfma_f32_16x16x32_bf16 v[104:107], v[152:155], v[136:139], v[104:107]\n\t"
        "ds_read_b64_tr_b16 v[184:185], v60 offset:32768\n\t"
        "v_mfma_f32_16x16x32_bf16 v[108:111], v[152:155], v[140:143], v[108:111]\n\t"
        "ds_read_b64_tr_b16 v[186:187], v60 offset:33792\n\t"
        "v_mfma_f32_16x16x32_bf16 v[112:115], v[156:159], v[128:131], v[112:115]\n\t"
        "ds_read_b64_tr_b16 v[188:189], v61 offset:32768\n\t"
        "v_mfma_f32_16x16x32_bf16 v[116:119], v[156:159], v[132:135], v[116:119]\n\t"
        "ds_read_b64_tr_b16 v[190:191], v61 offset:33792\n\t"
        "v_mfma_f32_16x16x32_bf16 v[120:123], v[156:159], v[136:139], v[120:123]\n\t"
        "v_mfma_f32_16x16x32_bf16 v[124:127], v[156:159], v[140:143], v[124:127]\n\t"
        "s_waitcnt lgkmcnt(6)\n\t"
        "v_mfma_f32_16x16x32_bf16 v[64:67], v[176:179], v[160:163], v[64:67]\n\t"
        "ds_read_b64_tr_b16 v[144:145], v58 offset:40960\n\t"
        "v_mfma_f32_16x16x32_bf16 v[68:71], v[176:179], v[164:167], v[68:71]\n\t"
        "ds_read_b64_tr_b16 v[146:147], v58 offset:41984\n\t"
        "v_mfma_f32_16x16x32_bf16 v[72:75], v[176:179], v[168:171], v[72:75]\n\t"
        "ds_read_b128 v[128:131], v57 offset:32768\n\t"
        "v_mfma_f32_16x16x32_bf16 v[76:79], v[176:179], v[172:175], v[76:79]\n\t"
        "ds_read_b128 v[132:135], v57 offset:34816\n\t"
        "s_waitcnt lgkmcnt(4)\n\t"
        "v_mfma_f32_16x16x32_bf16 v[80:83], v[180:183], v[160:163], v[80:83]\n\t"
        "ds_read_b128 v[136:139], v57 offset:36864\n\t"
        "v_mfma_f32_16x16x32_bf16 v[84:87], v[180:183], v[164:167], v[84:87]\n\t"
        "ds_read_b128 v[140:143], v57 offset:38912\n\t"
        "v_mfma_f32_16x16x32_bf16 v[88:91], v[180:183], v[168:171], v[88:91]\n\t"
        "ds_read_b64_tr_b16 v[148:149], v59 offset:40960\n\t"
        "v_mfma_f32_16x16x32_bf16 v[92:95], v[180:183], v[172:175], v[92:95]\n\t"
        "ds_read_b64_tr_b16 v[150:151], v59 offset:41984\n\t"
        "v_mfma_f32_16x16x32_bf16 v[96:99], v[184:187], v[160:163], v[96:99]\n\t"
        "ds_read_b64_tr_b16 v[152:153], v60 offset:40960\n\t"
        "v_mfma_f32_16x16x32_bf16 v[100:103], v[184:187], v[164:167], v[100:103]\n\t"
        "ds_read_b64_tr_b16 v[154:155], v60 offset:41984\n\t"
        "v_mfma_f32_16x16x32_bf16 v[104:107], v[184:187], v[168:171], v[104:107]\n\t"
        "ds_read_b64_tr_b16 v[156:157], v61 offset:40960\n\t"
        "v_mfma_f32_16x16x32_bf16 v[108:111], v[184:187], v[172:175], v[108:111]\n\t"
        "ds_read_b64_tr_b16 v[158:159], v61 offset:41984\n\t"
        "v_mfma_f32_16x16x32_bf16 v[112:115], v[188:191], v[160:163], v[112:115]\n\t"
        "v_add_u32_e32 v56, 0x10000, v56\n\t"
        "v_add_u32_e32 v57, 0x10000, v57\n\t"
        "v_add_u32_e32 v58, 0x10000, v58\n\t"
        "v_mfma_f32_16x16x32_bf16 v[116:119], v[188:191], v[164:167], v[116:119]\n\t"
        "v_add_u32_e32 v59, 0x10000, v59\n\t"
        "v_add_u32_e32 v60, 0x10000, v60\n\t"
        "v_add_u32_e32 v61, 0x10000, v61\n\t"
        "v_mfma_f32_16x16x32_bf16 v[120:123], v[188:191], v[168:171], v[120:123]\n\t"
        "v_mfma_f32_16x16x32_bf16 v[124:127], v[188:191], v[172:175], v[124:127]\n\t"
        "s_waitcnt lgkmcnt(6)\n\t"
        "v_mfma_f32_16x16x32_bf16 v[64:67], v[144:147], v[128:131], v[64:67]\n\t"
        "v_mfma_f32_16x16x32_bf16 v[68:71], v[144:147], v[132:135], v[68:71]\n\t"
        "s_waitcnt lgkmcnt(0)\n\t"
        "s_barrier\n\t"
        "v_mfma_f32_16x16x32_bf16 v[72:75], v[144:147], v[136:139], v[72:75]\n\t"
        "ds_read_b64_tr_b16 v[176:177], v58 offset:0\n\t"
        "v_mfma_f32_16x16x32_bf16 v[76:79], v[144:147], v[140:143], v[76:79]\n\t"
        "ds_read_b64_tr_b16 v[178:179], v58 offset:1024\n\t"
        "v_mfma_f32_16x16x32_bf16 v[80:83], v[148:151], v[128:131], v[80:83]\n\t"
        "ds_read_b128 v[160:163], v56 offset:0\n\t"
        "v_mfma_f32_16x16x32_bf16 v[84:87], v[148:151], v[132:135], v[84:87]\n\t"
        "ds_read_b128 v[164:167], v56 offset:2048\n\t"
        "v_mfma_f32_16x16x32_bf16 v[88:91], v[148:151], v[136:139], v[88:91]\n\t"
        "ds_read_b128 v[168:171], v56 offset:4096\n\t"
        "v_mfma_f32_16x16x32_bf16 v[92:95], v[148:151], v[140:143], v[92:95]\n\t"
        "ds_read_b128 v[172:175], v56 offset:6144\n\t"
        "v_mfma_f32_16x16x32_bf16 v[96:99], v[152:155], v[128:131], v[96:99]\n\t"
        "ds_read_b64_tr_b16 v[180:181], v59 offset:0\n\t"
        "v_mfma_f32_16x16x32_bf16 v[100:103], v[152:155], v[132:135], v[100:103]\n\t"
        "ds_read_b64_tr_b16 v[182:183], v59 offset:1024\n\t"
        "v_mfma_f32_16x16x32_bf16 v[104:107], v[152:155], v[136:139], v[104:107]\n\t"
        "ds_read_b64_tr_b16 v[184:185], v60 offset:0\n\t"
        "v_mfma_f32_16x16x32_bf16 v[108:111], v[152:155], v[140:143], v[108:111]\n\t"
        "ds_read_b64_tr_b16 v[186:187], v60 offset:1024\n\t"
        "v_mfma_f32_16x16x32_bf16 v[112:115], v[156:159], v[128:131], v[112:115]\n\t"
        "ds_read_b64_tr_b16 v[188:189], v61 offset:0\n\t"
        "v_mfma_f32_16x16x32_bf16 v[116:119], v[156:159], v[132:135], v[116:119]\n\t"
        "ds_read_b64_tr_b16 v[190:191], v61 offset:1024\n\t"
        "v_mfma_f32_16x16x32_bf16 v[120:123], v[156:159], v[136:139], v[120:123]\n\t"
        "v_mfma_f32_16x16x32_bf16 v[124:127], v[156:159], v[140:143], v[124:127]\n\t"
        "s_waitcnt lgkmcnt(6)\n\t"
        "v_mfma_f32_16x16x32_bf16 v[64:67], v[176:179], v[160:163], v[64:67]\n\t"
        "ds_read_b64_tr_b16 v[144:145], v58 offset:8192\n\t"
        "v_mfma_f32_16x16x32_bf16 v[68:71], v[176:179], v[164:167], v[68:71]\n\t"
        "ds_read_b64_tr_b16 v[146:147], v58 offset:9216\n\t"
        "v_mfma_f32_16x16x32_bf16 v[72:75], v[176:179], v[168:171], v[72:75]\n\t"
        "ds_read_b128 v[128:131], v57 offset:0\n\t"
        "v_mfma_f32_16x16x32_bf16 v[76:79], v[176:179], v[172:175], v[76:79]\n\t"
        "ds_read_b128 v[132:135], v57 offset:2048\n\t"
        "s_waitcnt lgkmcnt(4)\n\t"
        "v_mfma_f32_16x16x32_bf16 v[80:83], v[180:183], v[160:163], v[80:83]\n\t"
        "ds_read_b128 v[136:139], v57 offset:4096\n\t"
        "v_mfma_f32_16x16x32_bf16 v[84:87], v[180:183], v[164:167], v[84:87]\n\t"
        "ds_read_b128 v[140:143], v57 offset:6144\n\t"
        "v_mfma_f32_16x16x32_bf16 v[88:91], v[180:183], v[168:171], v[88:91]\n\t"
        "ds_read_b64_tr_b16 v[148:149], v59 offset:8192\n\t"
        "v_mfma_f32_16x16x32_bf16 v[92:95], v[180:183], v[172:175], v[92:95]\n\t"
        "ds_read_b64_tr_b16 v[150:151], v59 offset:9216\n\t"
        "v_mfma_f32_16x16x32_bf16 v[96:99], v[184:187], v[160:163], v[96:99]\n\t"
        "ds_read_b64_tr_b16 v[152:153], v60 offset:8192\n\t"
        "v_mfma_f32_16x16x32_bf16 v[100:103], v[184:187], v[164:167], v[100:103]\n\t"
        "ds_read_b64_tr_b16 v[154:155], v60 offset:9216\n\t"
        "v_mfma_f32_16x16x32_bf16 v[104:107], v[184:187], v[168:171], v[104:107]\n\t"
        "ds_read_b64_tr_b16 v[156:157], v61 offset:8192\n\t"
        "v_mfma_f32_16x16x32_bf16 v[108:111], v[184:187], v[172:175], v[108:111]\n\t"
        "ds_read_b64_tr_b16 v[158:159], v61 offset:9216\n\t"
        "v_mfma_f32_16x16x32_bf16 v[112:115], v[188:191], v[160:163], v[112:115]\n\t"
        "v_mfma_f32_16x16x32_bf16 v[116:119], v[188:191], v[164:167], v[116:119]\n\t"
        "v_mfma_f32_16x16x32_bf16 v[120:123], v[188:191], v[168:171], v[120:123]\n\t"
        "v_mfma_f32_16x16x32_bf16 v[124:127], v[188:191], v[172:175], v[124:127]\n\t"
        "s_waitcnt lgkmcnt(6)\n\t"
        "v_mfma_f32_16x16x32_bf16 v[64:67], v[144:147], v[128:131], v[64:67]\n\t"
        "v_mfma_f32_16x16x32_bf16 v[68:71], v[144:147], v[132:135], v[68:71]\n\t"
        "s_waitcnt lgkmcnt(0)\n\t"
        "s_barrier\n\t"
        "v_mfma_f32_16x16x32_bf16 v[72:75], v[144:147], v[136:139], v[72:75]\n\t"
        "v_mfma_f32_16x16x32_bf16 v[76:79], v[144:147], v[140:143], v[76:79]\n\t"
        "v_mfma_f32_16x16x32_bf16 v[80:83], v[148:151], v[128:131], v[80:83]\n\t"
        "v_mfma_f32_16x16x32_bf16 v[84:87], v[148:151], v[132:135], v[84:87]\n\t"
        "v_mfma_f32_16x16x32_bf16 v[88:91], v[148:151], v[136:139], v[88:91]\n\t"
        "v_mfma_f32_16x16x32_bf16 v[92:95], v[148:151], v[140:143], v[92:95]\n\t"
        "v_mfma_f32_16x16x32_bf16 v[96:99], v[152:155], v[128:131], v[96:99]\n\t"
        "v_cvt_pk_bf16_f32 v160, v64, v65\n\t"
        "v_cvt_pk_bf16_f32 v161, v66, v67\n\t"
        "v_mfma_f32_16x16x32_bf16 v[100:103], v[152:155], v[132:135], v[100:103]\n\t"
        "v_cvt_pk_bf16_f32 v162, v68, v69\n\t"
        "v_cvt_pk_bf16_f32 v163, v70, v71\n\t"
        "v_mfma_f32_16x16x32_bf16 v[104:107], v[152:155], v[136:139], v[104:107]\n\t"
        "v_cvt_pk_bf16_f32 v164, v72, v73\n\t"
        "v_cvt_pk_bf16_f32 v165, v74, v75\n\t"
        "v_mfma_f32_16x16x32_bf16 v[108:111], v[152:155], v[140:143], v[108:111]\n\t"
        "v_cvt_pk_bf16_f32 v166, v76, v77\n\t"
        "v_cvt_pk_bf16_f32 v167, v78, v79\n\t"
        "v_mfma_f32_16x16x32_bf16 v[112:115], v[156:159], v[128:131], v[112:115]\n\t"
        "v_cvt_pk_bf16_f32 v168, v80, v81\n\t"
        "v_cvt_pk_bf16_f32 v169, v82, v83\n\t"
        "v_mfma_f32_16x16x32_bf16 v[116:119], v[156:159], v[132:135], v[116:119]\n\t"
        "v_cvt_pk_bf16_f32 v170, v84, v85\n\t"
        "v_cvt_pk_bf16_f32 v171, v86, v87\n\t"
        "v_mfma_f32_16x16x32_bf16 v[120:123], v[156:159], v[136:139], v[120:123]\n\t"
        "v_cvt_pk_bf16_f32 v172, v88, v89\n\t"
        "v_cvt_pk_bf16_f32 v173, v90, v91\n\t"
        "v_mfma_f32_16x16x32_bf16 v[124:127], v[156:159], v[140:143], v[124:127]\n\t"
        "v_cvt_pk_bf16_f32 v174, v92, v93\n\t"
        "v_cvt_pk_bf16_f32 v175, v94, v95\n\t"
        "s_branch .Lge_%=\n\t"
        ".Lgv_%=:\n\t"
        "ds_read_b64_tr_b16 v[176:177], v58 offset:0\n\t"
        "ds_read_b64_tr_b16 v[178:179], v58 offset:1024\n\t"
        "ds_read_b128 v[160:163], v56 offset:0\n\t"
        "ds_read_b128 v[164:167], v56 offset:2048\n\t"
        "ds_read_b128 v[168:171], v56 offset:4096\n\t"
        "ds_read_b128 v[172:175], v56 offset:6144\n\t"
        "ds_read_b64_tr_b16 v[180:181], v59 offset:0\n\t"
        "ds_read_b64_tr_b16 v[182:183], v59 offset:1024\n\t"
        "ds_read_b64_tr_b16 v[184:185], v60 offset:0\n\t"
        "ds_read_b64_tr_b16 v[186:187], v60 offset:1024\n\t"
        "ds_read_b64_tr_b16 v[188:189], v61 offset:0\n\t"
        "ds_read_b64_tr_b16 v[190:191], v61 offset:1024\n\t"
        "s_waitcnt lgkmcnt(6)\n\t"
        "v_mfma_f32_16x16x32_bf16 v[64:67], v[160:163], v[176:179], 0\n\t"
        "ds_read_b64_tr_b16 v[144:145], v58 offset:8192\n\t"
        "v_mfma_f32_16x16x32_bf16 v[68:71], v[164:167], v[176:179], 0\n\t"
        "ds_read_b64_tr_b16 v[146:147], v58 offset:9216\n\t"
        "v_mfma_f32_16x16x32_bf16 v[72:75], v[168:171], v[176:179], 0\n\t"
        "ds_read_b128 v[128:131], v57 offset:0\n\t"
        "v_mfma_f32_16x16x32_bf16 v[76:79], v[172:175], v[176:179], 0\n\t"
        "ds_read_b128 v[132:135], v57 offset:2048\n\t"
        "s_waitcnt lgkmcnt(4)\n\t"
        "v_mfma_f32_16x16x32_bf16 v[80:83], v[160:163], v[180:183], 0\n\t"
        "ds_read_b128 v[136:139], v57 offset:4096\n\t"
        "v_mfma_f32_16x16x32_bf16 v[84:87], v[164:167], v[180:183], 0\n\t"
        "ds_read_b128 v[140:143], v57 offset:6144\n\t"
        "v_mfma_f32_16x16x32_bf16 v[88:91], v[168:171], v[180:183], 0\n\t"
        "ds_read_b64_tr_b16 v[148:149], v59 offset:8192\n\t"
        "v_mfma_f32_16x16x32_bf16 v[92:95], v[172:175], v[180:183], 0\n\t"
        "ds_read_b64_tr_b16 v[150:151], v59 offset:9216\n\t"
        "v_mfma_f32_16x16x32_bf16 v[96:99], v[160:163], v[184:187], 0\n\t"
        "ds_read_b64_tr_b16 v[152:153], v60 offset:8192\n\t"
        "v_mfma_f32_16x16x32_bf16 v[100:103], v[164:167], v[184:187], 0\n\t"
        "ds_read_b64_tr_b16 v[154:155], v60 offset:9216\n\t"
        "v_mfma_f32_16x16x32_bf16 v[104:107], v[168:171], v[184:187], 0\n\t"
        "ds_read_b64_tr_b16 v[156:157], v61 offset:8192\n\t"
        "v_mfma_f32_16x16x32_bf16 v[108:111], v[172:175], v[184:187], 0\n\t"
        "ds_read_b64_tr_b16 v[158:159], v61 offset:9216\n\t"
        "v_mfma_f32_16x16x32_bf16 v[112:115], v[160:163], v[188:191], 0\n\t"
        "ds_read_b128 v[192:195], v62 offset:0\n\t"
        "v_mfma_f32_16x16x32_bf16 v[116:119], v[164:167], v[188:191], 0\n\t"
        "ds_read_b128 v[196:199], v62 offset:1024\n\t"
        "v_mfma_f32_16x16x32_bf16 v[120:123], v[168:171], v[188:191], 0\n\t"
        "v_mfma_f32_16x16x32_bf16 v[124:127], v[172:175], v[188:191], 0\n\t"
        "s_waitcnt lgkmcnt(8)\n\t"
        "v_mfma_f32_16x16x32_bf16 v[64:67], v[128:131], v[144:147], v[64:67]\n\t"
        "v_mfma_f32_16x16x32_bf16 v[68:71], v[132:135], v[144:147], v[68:71]\n\t"
        "s_waitcnt vmcnt(0) lgkmcnt(0)\n\t"
        "s_barrier\n\t"
        "v_mfma_f32_16x16x32_bf16 v[72:75], v[136:139], v[144:147], v[72:75]\n\t"
        "ds_read_b64_tr_b16 v[176:177], v58 offset:32768\n\t"
        "v_mfma_f32_16x16x32_bf16 v[76:79], v[140:143], v[144:147], v[76:79]\n\t"
        "ds_read_b64_tr_b16 v[178:179], v58 offset:33792\n\t"
        "v_mfma_f32_16x16x32_bf16 v[80:83], v[128:131], v[148:151], v[80:83]\n\t"
        "ds_read_b128 v[160:163], v56 offset:32768\n\t"
        "v_mfma_f32_16x16x32_bf16 v[84:87], v[132:135], v[148:151], v[84:87]\n\t"
        "ds_read_b128 v[164:167], v56 offset:34816\n\t"
        "v_mfma_f32_16x16x32_bf16 v[88:91], v[136:139], v[148:151], v[88:91]\n\t"
        "ds_read_b128 v[168:171], v56 offset:36864\n\t"
        "v_mfma_f32_16x16x32_bf16 v[92:95], v[140:143], v[148:151], v[92:95]\n\t"
        "ds_read_b128 v[172:175], v56 offset:38912\n\t"
        "v_mfma_f32_16x16x32_bf16 v[96:99], v[128:131], v[152:155], v[96:99]\n\t"
        "ds_read_b64_tr_b16 v[180:181], v59 offset:32768\n\t"
        "v_mfma_f32_16x16x32_bf16 v[100:103], v[132:135], v[152:155], v[100:103]\n\t"
        "ds_read_b64_tr_b16 v[182:183], v59 offset:33792\n\t"
        "v_mfma_f32_16x16x32_bf16 v[104:107], v[136:139], v[152:155], v[104:107]\n\t"
        "ds_read_b64_tr_b16 v[184:185], v60 offset:32768\n\t"
        "v_mfma_f32_16x16x32_bf16 v[108:111], v[140:143], v[152:155], v[108:111]\n\t"
        "ds_read_b64_tr_b16 v[186:187], v60 offset:33792\n\t"
        "v_mfma_f32_16x16x32_bf16 v[112:115], v[128:131], v[156:159], v[112:115]\n\t"
        "ds_read_b64_tr_b16 v[188:189], v61 offset:32768\n\t"
        "v_mfma_f32_16x16x32_bf16 v[116:119], v[132:135], v[156:159], v[116:119]\n\t"
        "ds_read_b64_tr_b16 v[190:191], v61 offset:33792\n\t"
        "v_mfma_f32_16x16x32_bf16 v[120:123], v[136:139], v[156:159], v[120:123]\n\t"
        "ds_read_b128 v[200:203], v62 offset:2048\n\t"
        "v_mfma_f32_16x16x32_bf16 v[124:127], v[140:143], v[156:159], v[124:127]\n\t"
        "ds_read_b128 v[204:207], v62 offset:3072\n\t"
        "s_waitcnt lgkmcnt(8)\n\t"
        "v_mfma_f32_16x16x32_bf16 v[64:67], v[160:163], v[176:179], v[64:67]\n\t"
        "ds_read_b64_tr_b16 v[144:145], v58 offset:40960\n\t"
        "v_mfma_f32_16x16x32_bf16 v[68:71], v[164:167], v[176:179], v[68:71]\n\t"
        "ds_read_b64_tr_b16 v[146:147], v58 offset:41984\n\t"
        "v_mfma_f32_16x16x32_bf16 v[72:75], v[168:171], v[176:179], v[72:75]\n\t"
        "ds_read_b128 v[128:131], v57 offset:32768\n\t"
        "v_mfma_f32_16x16x32_bf16 v[76:79], v[172:175], v[176:179], v[76:79]\n\t"
        "ds_read_b128 v[132:135], v57 offset:34816\n\t"
        "s_waitcnt lgkmcnt(6)\n\t"
        "v_mfma_f32_16x16x32_bf16 v[80:83], v[160:163], v[180:183], v[80:83]\n\t"
        "ds_read_b128 v[136:139], v57 offset:36864\n\t"
        "v_mfma_f32_16x16x32_bf16 v[84:87], v[164:167], v[180:183], v[84:87]\n\t"
        "ds_read_b128 v[140:143], v57 offset:38912\n\t"
        "v_mfma_f32_16x16x32_bf16 v[88:91], v[168:171], v[180:183], v[88:91]\n\t"
        "ds_read_b64_tr_b16 v[148:149], v59 offset:40960\n\t"
        "v_mfma_f32_16x16x32_bf16 v[92:95], v[172:175], v[180:183], v[92:95]\n\t"
        "ds_read_b64_tr_b16 v[150:151], v59 offset:41984\n\t"
        "v_mfma_f32_16x16x32_bf16 v[96:99], v[160:163], v[184:187], v[96:99]\n\t"
        "ds_read_b64_tr_b16 v[152:153], v60 offset:40960\n\t"
        "v_mfma_f32_16x16x32_bf16 v[100:103], v[164:167], v[184:187], v[100:103]\n\t"
        "ds_read_b64_tr_b16 v[154:155], v60 offset:41984\n\t"
        "v_mfma_f32_16x16x32_bf16 v[104:107], v[168:171], v[184:187], v[104:107]\n\t"
        "ds_read_b64_tr_b16 v[156:157], v61 offset:40960\n\t"
        "v_mfma_f32_16x16x32_bf16 v[108:111], v[172:175], v[184:187], v[108:111]\n\t"
        "ds_read_b64_tr_b16 v[158:159], v61 offset:41984\n\t"
        "v_mfma_f32_16x16x32_bf16 v[112:115], v[160:163], v[188:191], v[112:115]\n\t"
        "v_add_u32_e32 v56, 0x10000, v56\n\t"
        "v_add_u32_e32 v57, 0x10000, v57\n\t"
        "v_add_u32_e32 v58, 0x10000, v58\n\t"
        "v_mfma_f32_16x16x32_bf16 v[116:119], v[164:167], v[188:191], v[116:119]\n\t"
        "v_add_u32_e32 v59, 0x10000, v59\n\t"
        "v_add_u32_e32 v60, 0x10000, v60\n\t"
        "v_add_u32_e32 v61, 0x10000, v61\n\t"
        "v_mfma_f32_16x16x32_bf16 v[120:123], v[168:171], v[188:191], v[120:123]\n\t"
        "ds_read_b128 v[208:211], v62 offset:32768\n\t"
        "v_mfma_f32_16x16x32_bf16 v[124:127], v[172:175], v[188:191], v[124:127]\n\t"
        "ds_read_b128 v[212:215], v62 offset:33792\n\t"
        "s_waitcnt lgkmcnt(8)\n\t"
        "v_mfma_f32_16x16x32_bf16 v[64:67], v[128:131], v[144:147], v[64:67]\n\t"
        "v_mfma_f32_16x16x32_bf16 v[68:71], v[132:135], v[144:147], v[68:71]\n\t"
        "s_waitcnt lgkmcnt(0)\n\t"
        "s_barrier\n\t"
        "v_mfma_f32_16x16x32_bf16 v[72:75], v[136:139], v[144:147], v[72:75]\n\t"
        "ds_read_b64_tr_b16 v[176:177], v58 offset:0\n\t"
        "v_mfma_f32_16x16x32_bf16 v[76:79], v[140:143], v[144:147], v[76:79]\n\t"
        "ds_read_b64_tr_b16 v[178:179], v58 offset:1024\n\t"
        "v_mfma_f32_16x16x32_bf16 v[80:83], v[128:131], v[148:151], v[80:83]\n\t"
        "ds_read_b128 v[160:163], v56 offset:0\n\t"
        "v_mfma_f32_16x16x32_bf16 v[84:87], v[132:135], v[148:151], v[84:87]\n\t"
        "ds_read_b128 v[164:167], v56 offset:2048\n\t"
        "v_mfma_f32_16x16x32_bf16 v[88:91], v[136:139], v[148:151], v[88:91]\n\t"
        "ds_read_b128 v[168:171], v56 offset:4096\n\t"
        "v_mfma_f32_16x16x32_bf16 v[92:95], v[140:143], v[148:151], v[92:95]\n\t"
        "ds_read_b128 v[172:175], v56 offset:6144\n\t"
        "v_mfma_f32_16x16x32_bf16 v[96:99], v[128:131], v[152:155], v[96:99]\n\t"
        "ds_read_b64_tr_b16 v[180:181], v59 offset:0\n\t"
        "v_mfma_f32_16x16x32_bf16 v[100:103], v[132:135], v[152:155], v[100:103]\n\t"
        "ds_read_b64_tr_b16 v[182:183], v59 offset:1024\n\t"
        "v_mfma_f32_16x16x32_bf16 v[104:107], v[136:139], v[152:155], v[104:107]\n\t"
        "ds_read_b64_tr_b16 v[184:185], v60 offset:0\n\t"
        "v_mfma_f32_16x16x32_bf16 v[108:111], v[140:143], v[152:155], v[108:111]\n\t"
        "ds_read_b64_tr_b16 v[186:187], v60 offset:1024\n\t"
        "v_mfma_f32_16x16x32_bf16 v[112:115], v[128:131], v[156:159], v[112:115]\n\t"
        "ds_read_b64_tr_b16 v[188:189], v61 offset:0\n\t"
        "v_mfma_f32_16x16x32_bf16 v[116:119], v[132:135], v[156:159], v[116:119]\n\t"
        "ds_read_b64_tr_b16 v[190:191], v61 offset:1024\n\t"
        "v_mfma_f32_16x16x32_bf16 v[120:123], v[136:139], v[156:159], v[120:123]\n\t"
        "ds_read_b128 v[216:219], v62 offset:34816\n\t"
        "v_mfma_f32_16x16x32_bf16 v[124:127], v[140:143], v[156:159], v[124:127]\n\t"
        "ds_read_b128 v[220:223], v62 offset:35840\n\t"
        "s_waitcnt lgkmcnt(8)\n\t"
        "v_mfma_f32_16x16x32_bf16 v[64:67], v[160:163], v[176:179], v[64:67]\n\t"
        "ds_read_b64_tr_b16 v[144:145], v58 offset:8192\n\t"
        "v_mfma_f32_16x16x32_bf16 v[68:71], v[164:167], v[176:179], v[68:71]\n\t"
        "ds_read_b64_tr_b16 v[146:147], v58 offset:9216\n\t"
        "v_mfma_f32_16x16x32_bf16 v[72:75], v[168:171], v[176:179], v[72:75]\n\t"
        "ds_read_b128 v[128:131], v57 offset:0\n\t"
        "v_mfma_f32_16x16x32_bf16 v[76:79], v[172:175], v[176:179], v[76:79]\n\t"
        "ds_read_b128 v[132:135], v57 offset:2048\n\t"
        "s_waitcnt lgkmcnt(6)\n\t"
        "v_mfma_f32_16x16x32_bf16 v[80:83], v[160:163], v[180:183], v[80:83]\n\t"
        "ds_read_b128 v[136:139], v57 offset:4096\n\t"
        "v_mfma_f32_16x16x32_bf16 v[84:87], v[164:167], v[180:183], v[84:87]\n\t"
        "ds_read_b128 v[140:143], v57 offset:6144\n\t"
        "v_mfma_f32_16x16x32_bf16 v[88:91], v[168:171], v[180:183], v[88:91]\n\t"
        "ds_read_b64_tr_b16 v[148:149], v59 offset:8192\n\t"
        "v_mfma_f32_16x16x32_bf16 v[92:95], v[172:175], v[180:183], v[92:95]\n\t"
        "ds_read_b64_tr_b16 v[150:151], v59 offset:9216\n\t"
        "v_mfma_f32_16x16x32_bf16 v[96:99], v[160:163], v[184:187], v[96:99]\n\t"
        "ds_read_b64_tr_b16 v[152:153], v60 offset:8192\n\t"
        "v_mfma_f32_16x16x32_bf16 v[100:103], v[164:167], v[184:187], v[100:103]\n\t"
        "ds_read_b64_tr_b16 v[154:155], v60 offset:9216\n\t"
        "v_mfma_f32_16x16x32_bf16 v[104:107], v[168:171], v[184:187], v[104:107]\n\t"
        "ds_read_b64_tr_b16 v[156:157], v61 offset:8192\n\t"
        "v_mfma_f32_16x16x32_bf16 v[108:111], v[172:175], v[184:187], v[108:111]\n\t"
        "ds_read_b64_tr_b16 v[158:159], v61 offset:9216\n\t"
        "v_mfma_f32_16x16x32_bf16 v[112:115], v[160:163], v[188:191], v[112:115]\n\t"
        "v_add_u32_e32 v62, 0x10000, v62\n\t"
        "ds_read_b128 v[224:227], v62 offset:0\n\t"
        "v_mfma_f32_16x16x32_bf16 v[116:119], v[164:167], v[188:191], v[116:119]\n\t"
        "ds_read_b128 v[228:231], v62 offset:1024\n\t"
        "v_mfma_f32_16x16x32_bf16 v[120:123], v[168:171], v[188:191], v[120:123]\n\t"
        "v_mfma_f32_16x16x32_bf16 v[124:127], v[172:175], v[188:191], v[124:127]\n\t"
        "s_waitcnt lgkmcnt(8)\n\t"
        "v_mfma_f32_16x16x32_bf16 v[64:67], v[128:131], v[144:147], v[64:67]\n\t"
        "v_mfma_f32_16x16x32_bf16 v[68:71], v[132:135], v[144:147], v[68:71]\n\t"
        "s_waitcnt lgkmcnt(0)\n\t"
        "s_barrier\n\t"
        "v_mfma_f32_16x16x32_bf16 v[72:75], v[136:139], v[144:147], v[72:75]\n\t"
        "ds_read_b64_tr_b16 v[176:177], v58 offset:32768\n\t"
        "v_mfma_f32_16x16x32_bf16 v[76:79], v[140:143], v[144:147], v[76:79]\n\t"
        "ds_read_b64_tr_b16 v[178:179], v58 offset:33792\n\t"
        "v_mfma_f32_16x16x32_bf16 v[80:83], v[128:131], v[148:151], v[80:83]\n\t"
        "ds_read_b128 v[160:163], v56 offset:32768\n\t"
        "v_mfma_f32_16x16x32_bf16 v[84:87], v[132:135], v[148:151], v[84:87]\n\t"
        "ds_read_b128 v[164:167], v56 offset:34816\n\t"
        "v_mfma_f32_16x16x32_bf16 v[88:91], v[136:139], v[148:151], v[88:91]\n\t"
        "ds_read_b128 v[168:171], v56 offset:36864\n\t"
        "v_mfma_f32_16x16x32_bf16 v[92:95], v[140:143], v[148:151], v[92:95]\n\t"
        "ds_read_b128 v[172:175], v56 offset:38912\n\t"
        "v_mfma_f32_16x16x32_bf16 v[96:99], v[128:131], v[152:155], v[96:99]\n\t"
        "ds_read_b64_tr_b16 v[180:181], v59 offset:32768\n\t"
        "v_mfma_f32_16x16x32_bf16 v[100:103], v[132:135], v[152:155], v[100:103]\n\t"
        "ds_read_b64_tr_b16 v[182:183], v59 offset:33792\n\t"
        "v_mfma_f32_16x16x32_bf16 v[104:107], v[136:139], v[152:155], v[104:107]\n\t"
        "ds_read_b64_tr_b16 v[184:185], v60 offset:32768\n\t"
        "v_mfma_f32_16x16x32_bf16 v[108:111], v[140:143], v[152:155], v[108:111]\n\t"
        "ds_read_b64_tr_b16 v[186:187], v60 offset:33792\n\t"
        "v_mfma_f32_16x16x32_bf16 v[112:115], v[128:131], v[156:159], v[112:115]\n\t"
        "ds_read_b64_tr_b16 v[188:189], v61 offset:32768\n\t"
        "v_mfma_f32_16x16x32_bf16 v[116:119], v[132:135], v[156:159], v[116:119]\n\t"
        "ds_read_b64_tr_b16 v[190:191], v61 offset:33792\n\t"
        "v_mfma_f32_16x16x32_bf16 v[120:123], v[136:139], v[156:159], v[120:123]\n\t"
        "ds_read_b128 v[232:235], v62 offset:2048\n\t"
        "v_mfma_f32_16x16x32_bf16 v[124:127], v[140:143], v[156:159], v[124:127]\n\t"
        "ds_read_b128 v[236:239], v62 offset:3072\n\t"
        "s_waitcnt lgkmcnt(8)\n\t"
        "v_mfma_f32_16x16x32_bf16 v[64:67], v[160:163], v[176:179], v[64:67]\n\t"
        "ds_read_b64_tr_b16 v[144:145], v58 offset:40960\n\t"
        "v_mfma_f32_16x16x32_bf16 v[68:71], v[164:167], v[176:179], v[68:71]\n\t"
        "ds_read_b64_tr_b16 v[146:147], v58 offset:41984\n\t"
        "v_mfma_f32_16x16x32_bf16 v[72:75], v[168:171], v[176:179], v[72:75]\n\t"
        "ds_read_b128 v[128:131], v57 offset:32768\n\t"
        "v_mfma_f32_16x16x32_bf16 v[76:79], v[172:175], v[176:179], v[76:79]\n\t"
        "ds_read_b128 v[132:135], v57 offset:34816\n\t"
        "s_waitcnt lgkmcnt(6)\n\t"
        "v_mfma_f32_16x16x32_bf16 v[80:83], v[160:163], v[180:183], v[80:83]\n\t"
        "ds_read_b128 v[136:139], v57 offset:36864\n\t"
        "v_mfma_f32_16x16x32_bf16 v[84:87], v[164:167], v[180:183], v[84:87]\n\t"
        "ds_read_b128 v[140:143], v57 offset:38912\n\t"
        "v_mfma_f32_16x16x32_bf16 v[88:91], v[168:171], v[180:183], v[88:91]\n\t"
        "ds_read_b64_tr_b16 v[148:149], v59 offset:40960\n\t"
        "v_mfma_f32_16x16x32_bf16 v[92:95], v[172:175], v[180:183], v[92:95]\n\t"
        "ds_read_b64_tr_b16 v[150:151], v59 offset:41984\n\t"
        "v_mfma_f32_16x16x32_bf16 v[96:99], v[160:163], v[184:187], v[96:99]\n\t"
        "ds_read_b64_tr_b16 v[152:153], v60 offset:40960\n\t"
        "v_mfma_f32_16x16x32_bf16 v[100:103], v[164:167], v[184:187], v[100:103]\n\t"
        "ds_read_b64_tr_b16 v[154:155], v60 offset:41984\n\t"
        "v_mfma_f32_16x16x32_bf16 v[104:107], v[168:171], v[184:187], v[104:107]\n\t"
        "ds_read_b64_tr_b16 v[156:157], v61 offset:40960\n\t"
        "v_mfma_f32_16x16x32_bf16 v[108:111], v[172:175], v[184:187], v[108:111]\n\t"
        "ds_read_b64_tr_b16 v[158:159], v61 offset:41984\n\t"
        "v_mfma_f32_16x16x32_bf16 v[112:115], v[160:163], v[188:191], v[112:115]\n\t"
        "v_add_u32_e32 v56, 0x10000, v56\n\t"
        "v_add_u32_e32 v57, 0x10000, v57\n\t"
        "v_add_u32_e32 v58, 0x10000, v58\n\t"
        "v_mfma_f32_16x16x32_bf16 v[116:119], v[164:167], v[188:191], v[116:119]\n\t"
        "v_add_u32_e32 v59, 0x10000, v59\n\t"
        "v_add_u32_e32 v60, 0x10000, v60\n\t"
        "v_add_u32_e32 v61, 0x10000, v61\n\t"
        "v_mfma_f32_16x16x32_bf16 v[120:123], v[168:171], v[188:191], v[120:123]\n\t"
        "v_mfma_f32_16x16x32_bf16 v[124:127], v[172:175], v[188:191], v[124:127]\n\t"
        "s_waitcnt lgkmcnt(6)\n\t"
        "v_mfma_f32_16x16x32_bf16 v[64:67], v[128:131], v[144:147], v[64:67]\n\t"
        "v_mfma_f32_16x16x32_bf16 v[68:71], v[132:135], v[144:147], v[68:71]\n\t"
        "s_waitcnt lgkmcnt(0)\n\t"
        "s_barrier\n\t"
        "v_mfma_f32_16x16x32_bf16 v[72:75], v[136:139], v[144:147], v[72:75]\n\t"
        "ds_read_b64_tr_b16 v[176:177], v58 offset:0\n\t"
        "v_mfma_f32_16x16x32_bf16 v[76:79], v[140:143], v[144:147], v[76:79]\n\t"
        "ds_read_b64_tr_b16 v[178:179], v58 offset:1024\n\t"
        "v_mfma_f32_16x16x32_bf16 v[80:83], v[128:131], v[148:151], v[80:83]\n\t"
        "ds_read_b128 v[160:163], v56 offset:0\n\t"
        "v_mfma_f32_16x16x32_bf16 v[84:87], v[132:135], v[148:151], v[84:87]\n\t"
        "ds_read_b128 v[164:167], v56 offset:2048\n\t"
        "v_mfma_f32_16x16x32_bf16 v[88:91], v[136:139], v[148:151], v[88:91]\n\t"
        "ds_read_b128 v[168:171], v56 offset:4096\n\t"
        "v_mfma_f32_16x16x32_bf16 v[92:95], v[140:143], v[148:151], v[92:95]\n\t"
        "ds_read_b128 v[172:175], v56 offset:6144\n\t"
        "v_mfma_f32_16x16x32_bf16 v[96:99], v[128:131], v[152:155], v[96:99]\n\t"
        "ds_read_b64_tr_b16 v[180:181], v59 offset:0\n\t"
        "v_mfma_f32_16x16x32_bf16 v[100:103], v[132:135], v[152:155], v[100:103]\n\t"
        "ds_read_b64_tr_b16 v[182:183], v59 offset:1024\n\t"
        "v_mfma_f32_16x16x32_bf16 v[104:107], v[136:139], v[152:155], v[104:107]\n\t"
        "ds_read_b64_tr_b16 v[184:185], v60 offset:0\n\t"
        "v_mfma_f32_16x16x32_bf16 v[108:111], v[140:143], v[152:155], v[108:111]\n\t"
        "ds_read_b64_tr_b16 v[186:187], v60 offset:1024\n\t"
        "v_mfma_f32_16x16x32_bf16 v[112:115], v[128:131], v[156:159], v[112:115]\n\t"
        "ds_read_b64_tr_b16 v[188:189], v61 offset:0\n\t"
        "v_mfma_f32_16x16x32_bf16 v[116:119], v[132:135], v[156:159], v[116:119]\n\t"
        "ds_read_b64_tr_b16 v[190:191], v61 offset:1024\n\t"
        "v_mfma_f32_16x16x32_bf16 v[120:123], v[136:139], v[156:159], v[120:123]\n\t"
        "v_mfma_f32_16x16x32_bf16 v[124:127], v[140:143], v[156:159], v[124:127]\n\t"
        "s_waitcnt lgkmcnt(6)\n\t"
        "v_mfma_f32_16x16x32_bf16 v[64:67], v[160:163], v[176:179], v[64:67]\n\t"
        "ds_read_b64_tr_b16 v[144:145], v58 offset:8192\n\t"
        "v_mfma_f32_16x16x32_bf16 v[68:71], v[164:167], v[176:179], v[68:71]\n\t"
        "ds_read_b64_tr_b16 v[146:147], v58 offset:9216\n\t"
        "v_mfma_f32_16x16x32_bf16 v[72:75], v[168:171], v[176:179], v[72:75]\n\t"
        "ds_read_b128 v[128:131], v57 offset:0\n\t"
        "v_mfma_f32_16x16x32_bf16 v[76:79], v[172:175], v[176:179], v[76:79]\n\t"
        "ds_read_b128 v[132:135], v57 offset:2048\n\t"
        "s_waitcnt lgkmcnt(4)\n\t"
        "v_mfma_f32_16x16x32_bf16 v[80:83], v[160:163], v[180:183], v[80:83]\n\t"
        "ds_read_b128 v[136:139], v57 offset:4096\n\t"
        "v_mfma_f32_16x16x32_bf16 v[84:87], v[164:167], v[180:183], v[84:87]\n\t"
        "ds_read_b128 v[140:143], v57 offset:6144\n\t"
        "v_mfma_f32_16x16x32_bf16 v[88:91], v[168:171], v[180:183], v[88:91]\n\t"
        "ds_read_b64_tr_b16 v[148:149], v59 offset:8192\n\t"
        "v_mfma_f32_16x16x32_bf16 v[92:95], v[172:175], v[180:183], v[92:95]\n\t"
        "ds_read_b64_tr_b16 v[150:151], v59 offset:9216\n\t"
        "v_mfma_f32_16x16x32_bf16 v[96:99], v[160:163], v[184:187], v[96:99]\n\t"
        "ds_read_b64_tr_b16 v[152:153], v60 offset:8192\n\t"
        "v_mfma_f32_16x16x32_bf16 v[100:103], v[164:167], v[184:187], v[100:103]\n\t"
        "ds_read_b64_tr_b16 v[154:155], v60 offset:9216\n\t"
        "v_mfma_f32_16x16x32_bf16 v[104:107], v[168:171], v[184:187], v[104:107]\n\t"
        "ds_read_b64_tr_b16 v[156:157], v61 offset:8192\n\t"
        "v_mfma_f32_16x16x32_bf16 v[108:111], v[172:175], v[184:187], v[108:111]\n\t"
        "ds_read_b64_tr_b16 v[158:159], v61 offset:9216\n\t"
        "v_mfma_f32_16x16x32_bf16 v[112:115], v[160:163], v[188:191], v[112:115]\n\t"
        "v_add_u32_e32 v56, 0xfffe0000, v56\n\t"
        "v_add_u32_e32 v57, 0xfffe0000, v57\n\t"
        "v_add_u32_e32 v58, 0xfffe0000, v58\n\t"
        "v_mfma_f32_16x16x32_bf16 v[116:119], v[164:167], v[188:191], v[116:119]\n\t"
        "v_add_u32_e32 v59, 0xfffe0000, v59\n\t"
        "v_add_u32_e32 v60, 0xfffe0000, v60\n\t"
        "v_add_u32_e32 v61, 0xfffe0000, v61\n\t"
        "v_mfma_f32_16x16x32_bf16 v[120:123], v[168:171], v[188:191], v[120:123]\n\t"
        "v_mfma_f32_16x16x32_bf16 v[124:127], v[172:175], v[188:191], v[124:127]\n\t"
        "s_waitcnt lgkmcnt(6)\n\t"
        "v_mfma_f32_16x16x32_bf16 v[64:67], v[128:131], v[144:147], v[64:67]\n\t"
        "v_mfma_f32_16x16x32_bf16 v[68:71], v[132:135], v[144:147], v[68:71]\n\t"
        "s_waitcnt lgkmcnt(0)\n\t"
        "s_barrier\n\t"
        "v_mfma_f32_16x16x32_bf16 v[72:75], v[136:139], v[144:147], v[72:75]\n\t"
        "ds_read_b64_tr_b16 v[176:177], v58 offset:0\n\t"
        "v_mfma_f32_16x16x32_bf16 v[76:79], v[140:143], v[144:147], v[76:79]\n\t"
        "ds_read_b64_tr_b16 v[178:179], v58 offset:1024\n\t"
        "v_mfma_f32_16x16x32_bf16 v[80:83], v[128:131], v[148:151], v[80:83]\n\t"
        "ds_read_b128 v[160:163], v56 offset:0\n\t"
        "v_mfma_f32_16x16x32_bf16 v[84:87], v[132:135], v[148:151], v[84:87]\n\t"
        "ds_read_b128 v[164:167], v56 offset:2048\n\t"
        "v_mfma_f32_16x16x32_bf16 v[88:91], v[136:139], v[148:151], v[88:91]\n\t"
        "ds_read_b128 v[168:171], v56 offset:4096\n\t"
        "v_mfma_f32_16x16x32_bf16 v[92:95], v[140:143], v[148:151], v[92:95]\n\t"
        "ds_read_b128 v[172:175], v56 offset:6144\n\t"
        "v_mfma_f32_16x16x32_bf16 v[96:99], v[128:131], v[152:155], v[96:99]\n\t"
        "ds_read_b64_tr_b16 v[180:181], v59 offset:0\n\t"
        "v_mfma_f32_16x16x32_bf16 v[100:103], v[132:135], v[152:155], v[100:103]\n\t"
        "ds_read_b64_tr_b16 v[182:183], v59 offset:1024\n\t"
        "v_mfma_f32_16x16x32_bf16 v[104:107], v[136:139], v[152:155], v[104:107]\n\t"
        "ds_read_b64_tr_b16 v[184:185], v60 offset:0\n\t"
        "v_mfma_f32_16x16x32_bf16 v[108:111], v[140:143], v[152:155], v[108:111]\n\t"
        "ds_read_b64_tr_b16 v[186:187], v60 offset:1024\n\t"
        "v_mfma_f32_16x16x32_bf16 v[112:115], v[128:131], v[156:159], v[112:115]\n\t"
        "ds_read_b64_tr_b16 v[188:189], v61 offset:0\n\t"
        "v_mfma_f32_16x16x32_bf16 v[116:119], v[132:135], v[156:159], v[116:119]\n\t"
        "ds_read_b64_tr_b16 v[190:191], v61 offset:1024\n\t"
        "v_mfma_f32_16x16x32_bf16 v[120:123], v[136:139], v[156:159], v[120:123]\n\t"
        "v_mfma_f32_16x16x32_bf16 v[124:127], v[140:143], v[156:159], v[124:127]\n\t"
        "s_waitcnt lgkmcnt(6)\n\t"
        "v_mfma_f32_16x16x32_bf16 v[64:67], v[160:163], v[176:179], v[64:67]\n\t"
        "ds_read_b64_tr_b16 v[144:145], v58 offset:8192\n\t"
        "v_mfma_f32_16x16x32_bf16 v[68:71], v[164:167], v[176:179], v[68:71]\n\t"
        "ds_read_b64_tr_b16 v[146:147], v58 offset:9216\n\t"
        "v_mfma_f32_16x16x32_bf16 v[72:75], v[168:171], v[176:179], v[72:75]\n\t"
        "ds_read_b128 v[128:131], v57 offset:0\n\t"
        "v_mfma_f32_16x16x32_bf16 v[76:79], v[172:175], v[176:179], v[76:79]\n\t"
        "ds_read_b128 v[132:135], v57 offset:2048\n\t"
        "s_waitcnt lgkmcnt(4)\n\t"
        "v_mfma_f32_16x16x32_bf16 v[80:83], v[160:163], v[180:183], v[80:83]\n\t"
        "ds_read_b128 v[136:139], v57 offset:4096\n\t"
        "v_mfma_f32_16x16x32_bf16 v[84:87], v[164:167], v[180:183], v[84:87]\n\t"
        "ds_read_b128 v[140:143], v57 offset:6144\n\t"
        "v_mfma_f32_16x16x32_bf16 v[88:91], v[168:171], v[180:183], v[88:91]\n\t"
        "ds_read_b64_tr_b16 v[148:149], v59 offset:8192\n\t"
        "v_mfma_f32_16x16x32_bf16 v[92:95], v[172:175], v[180:183], v[92:95]\n\t"
        "ds_read_b64_tr_b16 v[150:151], v59 offset:9216\n\t"
        "v_mfma_f32_16x16x32_bf16 v[96:99], v[160:163], v[184:187], v[96:99]\n\t"
        "ds_read_b64_tr_b16 v[152:153], v60 offset:8192\n\t"
        "v_mfma_f32_16x16x32_bf16 v[100:103], v[164:167], v[184:187], v[100:103]\n\t"
        "ds_read_b64_tr_b16 v[154:155], v60 offset:9216\n\t"
        "v_mfma_f32_16x16x32_bf16 v[104:107], v[168:171], v[184:187], v[104:107]\n\t"
        "ds_read_b64_tr_b16 v[156:157], v61 offset:8192\n\t"
        "v_mfma_f32_16x16x32_bf16 v[108:111], v[172:175], v[184:187], v[108:111]\n\t"
        "ds_read_b64_tr_b16 v[158:159], v61 offset:9216\n\t"
        "v_mfma_f32_16x16x32_bf16 v[112:115], v[160:163], v[188:191], v[112:115]\n\t"
        "v_mfma_f32_16x16x32_bf16 v[116:119], v[164:167], v[188:191], v[116:119]\n\t"
        "v_mfma_f32_16x16x32_bf16 v[120:123], v[168:171], v[188:191], v[120:123]\n\t"
        "v_mfma_f32_16x16x32_bf16 v[124:127], v[172:175], v[188:191], v[124:127]\n\t"
        "s_waitcnt lgkmcnt(6)\n\t"
        "v_mfma_f32_16x16x32_bf16 v[64:67], v[128:131], v[144:147], v[64:67]\n\t"
        "v_mfma_f32_16x16x32_bf16 v[68:71], v[132:135], v[144:147], v[68:71]\n\t"
        "s_waitcnt lgkmcnt(0)\n\t"
        "s_barrier\n\t"
        "v_mfma_f32_16x16x32_bf16 v[72:75], v[136:139], v[144:147], v[72:75]\n\t"
        "ds_read_b64_tr_b16 v[176:177], v58 offset:32768\n\t"
        "v_mfma_f32_16x16x32_bf16 v[76:79], v[140:143], v[144:147], v[76:79]\n\t"
        "ds_read_b64_tr_b16 v[178:179], v58 offset:33792\n\t"
        "v_mfma_f32_16x16x32_bf16 v[80:83], v[128:131], v[148:151], v[80:83]\n\t"
        "ds_read_b128 v[160:163], v56 offset:32768\n\t"
        "v_mfma_f32_16x16x32_bf16 v[84:87], v[132:135], v[148:151], v[84:87]\n\t"
        "ds_read_b128 v[164:167], v56 offset:34816\n\t"
        "v_mfma_f32_16x16x32_bf16 v[88:91], v[136:139], v[148:151], v[88:91]\n\t"
        "ds_read_b128 v[168:171], v56 offset:36864\n\t"
        "v_mfma_f32_16x16x32_bf16 v[92:95], v[140:143], v[148:151], v[92:95]\n\t"
        "ds_read_b128 v[172:175], v56 offset:38912\n\t"
        "v_mfma_f32_16x16x32_bf16 v[96:99], v[128:131], v[152:155], v[96:99]\n\t"
        "ds_read_b64_tr_b16 v[180:181], v59 offset:32768\n\t"
        "v_mfma_f32_16x16x32_bf16 v[100:103], v[132:135], v[152:155], v[100:103]\n\t"
        "ds_read_b64_tr_b16 v[182:183], v59 offset:33792\n\t"
        "v_mfma_f32_16x16x32_bf16 v[104:107], v[136:139], v[152:155], v[104:107]\n\t"
        "ds_read_b64_tr_b16 v[184:185], v60 offset:32768\n\t"
        "v_mfma_f32_16x16x32_bf16 v[108:111], v[140:143], v[152:155], v[108:111]\n\t"
        "ds_read_b64_tr_b16 v[186:187], v60 offset:33792\n\t"
        "v_mfma_f32_16x16x32_bf16 v[112:115], v[128:131], v[156:159], v[112:115]\n\t"
        "ds_read_b64_tr_b16 v[188:189], v61 offset:32768\n\t"
        "v_mfma_f32_16x16x32_bf16 v[116:119], v[132:135], v[156:159], v[116:119]\n\t"
        "ds_read_b64_tr_b16 v[190:191], v61 offset:33792\n\t"
        "v_mfma_f32_16x16x32_bf16 v[120:123], v[136:139], v[156:159], v[120:123]\n\t"
        "v_mfma_f32_16x16x32_bf16 v[124:127], v[140:143], v[156:159], v[124:127]\n\t"
        "s_waitcnt lgkmcnt(6)\n\t"
        "v_mfma_f32_16x16x32_bf16 v[64:67], v[160:163], v[176:179], v[64:67]\n\t"
        "ds_read_b64_tr_b16 v[144:145], v58 offset:40960\n\t"
        "v_mfma_f32_16x16x32_bf16 v[68:71], v[164:167], v[176:179], v[68:71]\n\t"
        "ds_read_b64_tr_b16 v[146:147], v58 offset:41984\n\t"
        "v_mfma_f32_16x16x32_bf16 v[72:75], v[168:171], v[176:179], v[72:75]\n\t"
        "ds_read_b128 v[128:131], v57 offset:32768\n\t"
        "v_mfma_f32_16x16x32_bf16 v[76:79], v[172:175], v[176:179], v[76:79]\n\t"
        "ds_read_b128 v[132:135], v57 offset:34816\n\t"
        "s_waitcnt lgkmcnt(4)\n\t"
        "v_mfma_f32_16x16x32_bf16 v[80:83], v[160:163], v[180:183], v[80:83]\n\t"
        "ds_read_b128 v[136:139], v57 offset:36864\n\t"
        "v_mfma_f32_16x16x32_bf16 v[84:87], v[164:167], v[180:183], v[84:87]\n\t"
        "ds_read_b128 v[140:143], v57 offset:38912\n\t"
        "v_mfma_f32_16x16x32_bf16 v[88:91], v[168:171], v[180:183], v[88:91]\n\t"
        "ds_read_b64_tr_b16 v[148:149], v59 offset:40960\n\t"
        "v_mfma_f32_16x16x32_bf16 v[92:95], v[172:175], v[180:183], v[92:95]\n\t"
        "ds_read_b64_tr_b16 v[150:151], v59 offset:41984\n\t"
        "v_mfma_f32_16x16x32_bf16 v[96:99], v[160:163], v[184:187], v[96:99]\n\t"
        "ds_read_b64_tr_b16 v[152:153], v60 offset:40960\n\t"
        "v_mfma_f32_16x16x32_bf16 v[100:103], v[164:167], v[184:187], v[100:103]\n\t"
        "ds_read_b64_tr_b16 v[154:155], v60 offset:41984\n\t"
        "v_mfma_f32_16x16x32_bf16 v[104:107], v[168:171], v[184:187], v[104:107]\n\t"
        "ds_read_b64_tr_b16 v[156:157], v61 offset:40960\n\t"
        "v_mfma_f32_16x16x32_bf16 v[108:111], v[172:175], v[184:187], v[108:111]\n\t"
        "ds_read_b64_tr_b16 v[158:159], v61 offset:41984\n\t"
        "v_mfma_f32_16x16x32_bf16 v[112:115], v[160:163], v[188:191], v[112:115]\n\t"
        "v_add_u32_e32 v56, 0x10000, v56\n\t"
        "v_add_u32_e32 v57, 0x10000, v57\n\t"
        "v_add_u32_e32 v58, 0x10000, v58\n\t"
        "v_mfma_f32_16x16x32_bf16 v[116:119], v[164:167], v[188:191], v[116:119]\n\t"
        "v_add_u32_e32 v59, 0x10000, v59\n\t"
        "v_add_u32_e32 v60, 0x10000, v60\n\t"
        "v_add_u32_e32 v61, 0x10000, v61\n\t"
        "v_mfma_f32_16x16x32_bf16 v[120:123], v[168:171], v[188:191], v[120:123]\n\t"
        "v_mfma_f32_16x16x32_bf16 v[124:127], v[172:175], v[188:191], v[124:127]\n\t"
        "s_waitcnt lgkmcnt(6)\n\t"
        "v_mfma_f32_16x16x32_bf16 v[64:67], v[128:131], v[144:147], v[64:67]\n\t"
        "v_mfma_f32_16x16x32_bf16 v[68:71], v[132:135], v[144:147], v[68:71]\n\t"
        "s_waitcnt lgkmcnt(0)\n\t"
        "s_barrier\n\t"
        "v_mfma_f32_16x16x32_bf16 v[72:75], v[136:139], v[144:147], v[72:75]\n\t"
        "ds_read_b64_tr_b16 v[176:177], v58 offset:0\n\t"
        "v_mfma_f32_16x16x32_bf16 v[76:79], v[140:143], v[144:147], v[76:79]\n\t"
        "ds_read_b64_tr_b16 v[178:179], v58 offset:1024\n\t"
        "v_mfma_f32_16x16x32_bf16 v[80:83], v[128:131], v[148:151], v[80:83]\n\t"
        "ds_read_b128 v[160:163], v56 offset:0\n\t"
        "v_mfma_f32_16x16x32_bf16 v[84:87], v[132:135], v[148:151], v[84:87]\n\t"
        "ds_read_b128 v[164:167], v56 offset:2048\n\t"
        "v_mfma_f32_16x16x32_bf16 v[88:91], v[136:139], v[148:151], v[88:91]\n\t"
        "ds_read_b128 v[168:171], v56 offset:4096\n\t"
        "v_mfma_f32_16x16x32_bf16 v[92:95], v[140:143], v[148:151], v[92:95]\n\t"
        "ds_read_b128 v[172:175], v56 offset:6144\n\t"
        "v_mfma_f32_16x16x32_bf16 v[96:99], v[128:131], v[152:155], v[96:99]\n\t"
        "ds_read_b64_tr_b16 v[180:181], v59 offset:0\n\t"
        "v_mfma_f32_16x16x32_bf16 v[100:103], v[132:135], v[152:155], v[100:103]\n\t"
        "ds_read_b64_tr_b16 v[182:183], v59 offset:1024\n\t"
        "v_mfma_f32_16x16x32_bf16 v[104:107], v[136:139], v[152:155], v[104:107]\n\t"
        "ds_read_b64_tr_b16 v[184:185], v60 offset:0\n\t"
        "v_mfma_f32_16x16x32_bf16 v[108:111], v[140:143], v[152:155], v[108:111]\n\t"
        "ds_read_b64_tr_b16 v[186:187], v60 offset:1024\n\t"
        "v_mfma_f32_16x16x32_bf16 v[112:115], v[128:131], v[156:159], v[112:115]\n\t"
        "ds_read_b64_tr_b16 v[188:189], v61 offset:0\n\t"
        "v_mfma_f32_16x16x32_bf16 v[116:119], v[132:135], v[156:159], v[116:119]\n\t"
        "ds_read_b64_tr_b16 v[190:191], v61 offset:1024\n\t"
        "v_mfma_f32_16x16x32_bf16 v[120:123], v[136:139], v[156:159], v[120:123]\n\t"
        "v_mfma_f32_16x16x32_bf16 v[124:127], v[140:143], v[156:159], v[124:127]\n\t"
        "s_waitcnt lgkmcnt(6)\n\t"
        "v_mfma_f32_16x16x32_bf16 v[64:67], v[160:163], v[176:179], v[64:67]\n\t"
        "ds_read_b64_tr_b16 v[144:145], v58 offset:8192\n\t"
        "v_mfma_f32_16x16x32_bf16 v[68:71], v[164:167], v[176:179], v[68:71]\n\t"
        "ds_read_b64_tr_b16 v[146:147], v58 offset:9216\n\t"
        "v_mfma_f32_16x16x32_bf16 v[72:75], v[168:171], v[176:179], v[72:75]\n\t"
        "ds_read_b128 v[128:131], v57 offset:0\n\t"
        "v_mfma_f32_16x16x32_bf16 v[76:79], v[172:175], v[176:179], v[76:79]\n\t"
        "ds_read_b128 v[132:135], v57 offset:2048\n\t"
        "s_waitcnt lgkmcnt(4)\n\t"
        "v_mfma_f32_16x16x32_bf16 v[80:83], v[160:163], v[180:183], v[80:83]\n\t"
        "ds_read_b128 v[136:139], v57 offset:4096\n\t"
        "v_mfma_f32_16x16x32_bf16 v[84:87], v[164:167], v[180:183], v[84:87]\n\t"
        "ds_read_b128 v[140:143], v57 offset:6144\n\t"
        "v_mfma_f32_16x16x32_bf16 v[88:91], v[168:171], v[180:183], v[88:91]\n\t"
        "ds_read_b64_tr_b16 v[148:149], v59 offset:8192\n\t"
        "v_mfma_f32_16x16x32_bf16 v[92:95], v[172:175], v[180:183], v[92:95]\n\t"
        "ds_read_b64_tr_b16 v[150:151], v59 offset:9216\n\t"
        "v_mfma_f32_16x16x32_bf16 v[96:99], v[160:163], v[184:187], v[96:99]\n\t"
        "ds_read_b64_tr_b16 v[152:153], v60 offset:8192\n\t"
        "v_mfma_f32_16x16x32_bf16 v[100:103], v[164:167], v[184:187], v[100:103]\n\t"
        "ds_read_b64_tr_b16 v[154:155], v60 offset:9216\n\t"
        "v_mfma_f32_16x16x32_bf16 v[104:107], v[168:171], v[184:187], v[104:107]\n\t"
        "ds_read_b64_tr_b16 v[156:157], v61 offset:8192\n\t"
        "v_mfma_f32_16x16x32_bf16 v[108:111], v[172:175], v[184:187], v[108:111]\n\t"
        "ds_read_b64_tr_b16 v[158:159], v61 offset:9216\n\t"
        "v_mfma_f32_16x16x32_bf16 v[112:115], v[160:163], v[188:191], v[112:115]\n\t"
        "v_mfma_f32_16x16x32_bf16 v[116:119], v[164:167], v[188:191], v[116:119]\n\t"
        "v_mfma_f32_16x16x32_bf16 v[120:123], v[168:171], v[188:191], v[120:123]\n\t"
        "v_mfma_f32_16x16x32_bf16 v[124:127], v[172:175], v[188:191], v[124:127]\n\t"
        "s_waitcnt lgkmcnt(6)\n\t"
        "v_mfma_f32_16x16x32_bf16 v[64:67], v[128:131], v[144:147], v[64:67]\n\t"
        "v_mfma_f32_16x16x32_bf16 v[68:71], v[132:135], v[144:147], v[68:71]\n\t"
        "s_waitcnt lgkmcnt(0)\n\t"
        "s_barrier\n\t"
        "v_mfma_f32_16x16x32_bf16 v[72:75], v[136:139], v[144:147], v[72:75]\n\t"
        "ds_read_b64_tr_b16 v[176:177], v58 offset:32768\n\t"
        "v_mfma_f32_16x16x32_bf16 v[76:79], v[140:143], v[144:147], v[76:79]\n\t"
        "ds_read_b64_tr_b16 v[178:179], v58 offset:33792\n\t"
        "v_mfma_f32_16x16x32_bf16 v[80:83], v[128:131], v[148:151], v[80:83]\n\t"
        "ds_read_b128 v[160:163], v56 offset:32768\n\t"
        "v_mfma_f32_16x16x32_bf16 v[84:87], v[132:135], v[148:151], v[84:87]\n\t"
        "ds_read_b128 v[164:167], v56 offset:34816\n\t"
        "v_mfma_f32_16x16x32_bf16 v[88:91], v[136:139], v[148:151], v[88:91]\n\t"
        "ds_read_b128 v[168:171], v56 offset:36864\n\t"
        "v_mfma_f32_16x16x32_bf16 v[92:95], v[140:143], v[148:151], v[92:95]\n\t"
        "ds_read_b128 v[172:175], v56 offset:38912\n\t"
        "v_mfma_f32_16x16x32_bf16 v[96:99], v[128:131], v[152:155], v[96:99]\n\t"
        "ds_read_b64_tr_b16 v[180:181], v59 offset:32768\n\t"
        "v_mfma_f32_16x16x32_bf16 v[100:103], v[132:135], v[152:155], v[100:103]\n\t"
        "ds_read_b64_tr_b16 v[182:183], v59 offset:33792\n\t"
        "v_mfma_f32_16x16x32_bf16 v[104:107], v[136:139], v[152:155], v[104:107]\n\t"
        "ds_read_b64_tr_b16 v[184:185], v60 offset:32768\n\t"
        "v_mfma_f32_16x16x32_bf16 v[108:111], v[140:143], v[152:155], v[108:111]\n\t"
        "ds_read_b64_tr_b16 v[186:187], v60 offset:33792\n\t"
        "v_mfma_f32_16x16x32_bf16 v[112:115], v[128:131], v[156:159], v[112:115]\n\t"
        "ds_read_b64_tr_b16 v[188:189], v61 offset:32768\n\t"
        "v_mfma_f32_16x16x32_bf16 v[116:119], v[132:135], v[156:159], v[116:119]\n\t"
        "ds_read_b64_tr_b16 v[190:191], v61 offset:33792\n\t"
        "v_mfma_f32_16x16x32_bf16 v[120:123], v[136:139], v[156:159], v[120:123]\n\t"
        "v_mfma_f32_16x16x32_bf16 v[124:127], v[140:143], v[156:159], v[124:127]\n\t"
        "s_waitcnt lgkmcnt(6)\n\t"
        "v_mfma_f32_16x16x32_bf16 v[64:67], v[160:163], v[176:179], v[64:67]\n\t"
        "ds_read_b64_tr_b16 v[144:145], v58 offset:40960\n\t"
        "v_mfma_f32_16x16x32_bf16 v[68:71], v[164:167], v[176:179], v[68:71]\n\t"
        "ds_read_b64_tr_b16 v[146:147], v58 offset:41984\n\t"
        "v_mfma_f32_16x16x32_bf16 v[72:75], v[168:171], v[176:179], v[72:75]\n\t"
        "ds_read_b128 v[128:131], v57 offset:32768\n\t"
        "v_mfma_f32_16x16x32_bf16 v[76:79], v[172:175], v[176:179], v[76:79]\n\t"
        "ds_read_b128 v[132:135], v57 offset:34816\n\t"
        "s_waitcnt lgkmcnt(4)\n\t"
        "v_mfma_f32_16x16x32_bf16 v[80:83], v[160:163], v[180:183], v[80:83]\n\t"
        "ds_read_b128 v[136:139], v57 offset:36864\n\t"
        "v_mfma_f32_16x16x32_bf16 v[84:87], v[164:167], v[180:183], v[84:87]\n\t"
        "ds_read_b128 v[140:143], v57 offset:38912\n\t"
        "v_mfma_f32_16x16x32_bf16 v[88:91], v[168:171], v[180:183], v[88:91]\n\t"
        "ds_read_b64_tr_b16 v[148:149], v59 offset:40960\n\t"
        "v_mfma_f32_16x16x32_bf16 v[92:95], v[172:175], v[180:183], v[92:95]\n\t"
        "ds_read_b64_tr_b16 v[150:151], v59 offset:41984\n\t"
        "v_mfma_f32_16x16x32_bf16 v[96:99], v[160:163], v[184:187], v[96:99]\n\t"
        "ds_read_b64_tr_b16 v[152:153], v60 offset:40960\n\t"
        "v_mfma_f32_16x16x32_bf16 v[100:103], v[164:167], v[184:187], v[100:103]\n\t"
        "ds_read_b64_tr_b16 v[154:155], v60 offset:41984\n\t"
        "v_mfma_f32_16x16x32_bf16 v[104:107], v[168:171], v[184:187], v[104:107]\n\t"
        "ds_read_b64_tr_b16 v[156:157], v61 offset:40960\n\t"
        "v_mfma_f32_16x16x32_bf16 v[108:111], v[172:175], v[184:187], v[108:111]\n\t"
        "ds_read_b64_tr_b16 v[158:159], v61 offset:41984\n\t"
        "v_mfma_f32_16x16x32_bf16 v[112:115], v[160:163], v[188:191], v[112:115]\n\t"
        "v_add_u32_e32 v56, 0x10000, v56\n\t"
        "v_add_u32_e32 v57, 0x10000, v57\n\t"
        "v_add_u32_e32 v58, 0x10000, v58\n\t"
        "v_mfma_f32_16x16x32_bf16 v[116:119], v[164:167], v[188:191], v[116:119]\n\t"
        "v_add_u32_e32 v59, 0x10000, v59\n\t"
        "v_add_u32_e32 v60, 0x10000, v60\n\t"
        "v_add_u32_e32 v61, 0x10000, v61\n\t"
        "v_mfma_f32_16x16x32_bf16 v[120:123], v[168:171], v[188:191], v[120:123]\n\t"
        "v_mfma_f32_16x16x32_bf16 v[124:127], v[172:175], v[188:191], v[124:127]\n\t"
        "s_waitcnt lgkmcnt(6)\n\t"
        "v_mfma_f32_16x16x32_bf16 v[64:67], v[128:131], v[144:147], v[64:67]\n\t"
        "v_mfma_f32_16x16x32_bf16 v[68:71], v[132:135], v[144:147], v[68:71]\n\t"
        "s_waitcnt lgkmcnt(0)\n\t"
        "s_barrier\n\t"
        "v_mfma_f32_16x16x32_bf16 v[72:75], v[136:139], v[144:147], v[72:75]\n\t"
        "ds_read_b64_tr_b16 v[176:177], v58 offset:0\n\t"
        "v_mfma_f32_16x16x32_bf16 v[76:79], v[140:143], v[144:147], v[76:79]\n\t"
        "ds_read_b64_tr_b16 v[178:179], v58 offset:1024\n\t"
        "v_mfma_f32_16x16x32_bf16 v[80:83], v[128:131], v[148:151], v[80:83]\n\t"
        "ds_read_b128 v[160:163], v56 offset:0\n\t"
        "v_mfma_f32_16x16x32_bf16 v[84:87], v[132:135], v[148:151], v[84:87]\n\t"
        "ds_read_b128 v[164:167], v56 offset:2048\n\t"
        "v_mfma_f32_16x16x32_bf16 v[88:91], v[136:139], v[148:151], v[88:91]\n\t"
        "ds_read_b128 v[168:171], v56 offset:4096\n\t"
        "v_mfma_f32_16x16x32_bf16 v[92:95], v[140:143], v[148:151], v[92:95]\n\t"
        "ds_read_b128 v[172:175], v56 offset:6144\n\t"
        "v_mfma_f32_16x16x32_bf16 v[96:99], v[128:131], v[152:155], v[96:99]\n\t"
        "ds_read_b64_tr_b16 v[180:181], v59 offset:0\n\t"
        "v_mfma_f32_16x16x32_bf16 v[100:103], v[132:135], v[152:155], v[100:103]\n\t"
        "ds_read_b64_tr_b16 v[182:183], v59 offset:1024\n\t"
        "v_mfma_f32_16x16x32_bf16 v[104:107], v[136:139], v[152:155], v[104:107]\n\t"
        "ds_read_b64_tr_b16 v[184:185], v60 offset:0\n\t"
        "v_mfma_f32_16x16x32_bf16 v[108:111], v[140:143], v[152:155], v[108:111]\n\t"
        "ds_read_b64_tr_b16 v[186:187], v60 offset:1024\n\t"
        "v_mfma_f32_16x16x32_bf16 v[112:115], v[128:131], v[156:159], v[112:115]\n\t"
        "ds_read_b64_tr_b16 v[188:189], v61 offset:0\n\t"
        "v_mfma_f32_16x16x32_bf16 v[116:119], v[132:135], v[156:159], v[116:119]\n\t"
        "ds_read_b64_tr_b16 v[190:191], v61 offset:1024\n\t"
        "v_mfma_f32_16x16x32_bf16 v[120:123], v[136:139], v[156:159], v[120:123]\n\t"
        "v_mfma_f32_16x16x32_bf16 v[124:127], v[140:143], v[156:159], v[124:127]\n\t"
        "s_waitcnt lgkmcnt(6)\n\t"
        "v_mfma_f32_16x16x32_bf16 v[64:67], v[160:163], v[176:179], v[64:67]\n\t"
        "ds_read_b64_tr_b16 v[144:145], v58 offset:8192\n\t"
        "v_mfma_f32_16x16x32_bf16 v[68:71], v[164:167], v[176:179], v[68:71]\n\t"
        "ds_read_b64_tr_b16 v[146:147], v58 offset:9216\n\t"
        "v_mfma_f32_16x16x32_bf16 v[72:75], v[168:171], v[176:179], v[72:75]\n\t"
        "ds_read_b128 v[128:131], v57 offset:0\n\t"
        "v_mfma_f32_16x16x32_bf16 v[76:79], v[172:175], v[176:179], v[76:79]\n\t"
        "ds_read_b128 v[132:135], v57 offset:2048\n\t"
        "s_waitcnt lgkmcnt(4)\n\t"
        "v_mfma_f32_16x16x32_bf16 v[80:83], v[160:163], v[180:183], v[80:83]\n\t"
        "ds_read_b128 v[136:139], v57 offset:4096\n\t"
        "v_mfma_f32_16x16x32_bf16 v[84:87], v[164:167], v[180:183], v[84:87]\n\t"
        "ds_read_b128 v[140:143], v57 offset:6144\n\t"
        "v_mfma_f32_16x16x32_bf16 v[88:91], v[168:171], v[180:183], v[88:91]\n\t"
        "ds_read_b64_tr_b16 v[148:149], v59 offset:8192\n\t"
        "v_mfma_f32_16x16x32_bf16 v[92:95], v[172:175], v[180:183], v[92:95]\n\t"
        "ds_read_b64_tr_b16 v[150:151], v59 offset:9216\n\t"
        "v_mfma_f32_16x16x32_bf16 v[96:99], v[160:163], v[184:187], v[96:99]\n\t"
        "ds_read_b64_tr_b16 v[152:153], v60 offset:8192\n\t"
        "v_mfma_f32_16x16x32_bf16 v[100:103], v[164:167], v[184:187], v[100:103]\n\t"
        "ds_read_b64_tr_b16 v[154:155], v60 offset:9216\n\t"
        "v_mfma_f32_16x16x32_bf16 v[104:107], v[168:171], v[184:187], v[104:107]\n\t"
        "ds_read_b64_tr_b16 v[156:157], v61 offset:8192\n\t"
        "v_mfma_f32_16x16x32_bf16 v[108:111], v[172:175], v[184:187], v[108:111]\n\t"
        "ds_read_b64_tr_b16 v[158:159], v61 offset:9216\n\t"
        "v_mfma_f32_16x16x32_bf16 v[112:115], v[160:163], v[188:191], v[112:115]\n\t"
        "v_mfma_f32_16x16x32_bf16 v[116:119], v[164:167], v[188:191], v[116:119]\n\t"
        "v_mfma_f32_16x16x32_bf16 v[120:123], v[168:171], v[188:191], v[120:123]\n\t"
        "v_mfma_f32_16x16x32_bf16 v[124:127], v[172:175], v[188:191], v[124:127]\n\t"
        "s_waitcnt lgkmcnt(6)\n\t"
        "v_mfma_f32_16x16x32_bf16 v[64:67], v[128:131], v[144:147], v[64:67]\n\t"
        "v_mfma_f32_16x16x32_bf16 v[68:71], v[132:135], v[144:147], v[68:71]\n\t"
        "s_waitcnt lgkmcnt(0)\n\t"
        "s_barrier\n\t"
        "v_mfma_f32_16x16x32_bf16 v[72:75], v[136:139], v[144:147], v[72:75]\n\t"
        "v_mfma_f32_16x16x32_bf16 v[76:79], v[140:143], v[144:147], v[76:79]\n\t"
        "v_mfma_f32_16x16x32_bf16 v[80:83], v[128:131], v[148:151], v[80:83]\n\t"
        "v_mfma_f32_16x16x32_bf16 v[84:87], v[132:135], v[148:151], v[84:87]\n\t"
        "v_mfma_f32_16x16x32_bf16 v[88:91], v[136:139], v[148:151], v[88:91]\n\t"
        "v_mfma_f32_16x16x32_bf16 v[92:95], v[140:143], v[148:151], v[92:95]\n\t"
        "v_mfma_f32_16x16x32_bf16 v[96:99], v[128:131], v[152:155], v[96:99]\n\t"
        "v_cvt_pk_bf16_f32 v160, v64, v65\n\t"
        "v_cvt_pk_bf16_f32 v161, v66, v67\n\t"
        "v_mfma_f32_16x16x32_bf16 v[100:103], v[132:135], v[152:155], v[100:103]\n\t"
        "v_cvt_pk_bf16_f32 v162, v68, v69\n\t"
        "v_cvt_pk_bf16_f32 v163, v70, v71\n\t"
        "v_mfma_f32_16x16x32_bf16 v[104:107], v[136:139], v[152:155], v[104:107]\n\t"
        "v_cvt_pk_bf16_f32 v164, v72, v73\n\t"
        "v_cvt_pk_bf16_f32 v165, v74, v75\n\t"
        "v_mfma_f32_16x16x32_bf16 v[108:111], v[140:143], v[152:155], v[108:111]\n\t"
        "v_cvt_pk_bf16_f32 v166, v76, v77\n\t"
        "v_cvt_pk_bf16_f32 v167, v78, v79\n\t"
        "v_mfma_f32_16x16x32_bf16 v[112:115], v[128:131], v[156:159], v[112:115]\n\t"
        "v_cvt_pk_bf16_f32 v168, v80, v81\n\t"
        "v_cvt_pk_bf16_f32 v169, v82, v83\n\t"
        "v_mfma_f32_16x16x32_bf16 v[116:119], v[132:135], v[156:159], v[116:119]\n\t"
        "v_cvt_pk_bf16_f32 v170, v84, v85\n\t"
        "v_cvt_pk_bf16_f32 v171, v86, v87\n\t"
        "v_mfma_f32_16x16x32_bf16 v[120:123], v[136:139], v[156:159], v[120:123]\n\t"
        "v_cvt_pk_bf16_f32 v172, v88, v89\n\t"
        "v_cvt_pk_bf16_f32 v173, v90, v91\n\t"
        "v_mfma_f32_16x16x32_bf16 v[124:127], v[140:143], v[156:159], v[124:127]\n\t"
        "v_cvt_pk_bf16_f32 v174, v92, v93\n\t"
        "v_cvt_pk_bf16_f32 v175, v94, v95\n\t"
        ".Lge_%=:\n\t"
        "v_cvt_pk_bf16_f32 v176, v96, v97\n\t"
        "v_cvt_pk_bf16_f32 v177, v98, v99\n\t"
        "v_cvt_pk_bf16_f32 v178, v100, v101\n\t"
        "v_cvt_pk_bf16_f32 v179, v102, v103\n\t"
        "v_cvt_pk_bf16_f32 v180, v104, v105\n\t"
        "v_cvt_pk_bf16_f32 v181, v106, v107\n\t"
        "v_cvt_pk_bf16_f32 v182, v108, v109\n\t"
        "v_cvt_pk_bf16_f32 v183, v110, v111\n\t"
        "v_cvt_pk_bf16_f32 v184, v112, v113\n\t"
        "v_cvt_pk_bf16_f32 v185, v114, v115\n\t"
        "v_cvt_pk_bf16_f32 v186, v116, v117\n\t"
        "v_cvt_pk_bf16_f32 v187, v118, v119\n\t"
        "v_cvt_pk_bf16_f32 v188, v120, v121\n\t"
        "v_cvt_pk_bf16_f32 v189, v122, v123\n\t"
        "v_cvt_pk_bf16_f32 v190, v124, v125\n\t"
        "v_cvt_pk_bf16_f32 v191, v126, v127\n\t"
        "v_mov_b32_e32 v24, 0\n\t"
        "s_barrier\n\t"
        "global_load_dword v24, v24, %[ws] sc1\n\t"
        "global_load_dword v128, %[tch], %[tb]\n\t"
        "global_store_dwordx4 %[so0], v[160:163], %[d0] sc1\n\t"
        "global_store_dwordx4 %[so0], v[164:167], %[d1] sc1\n\t"
        "global_store_dwordx4 %[so1], v[168:171], %[d0] sc1\n\t"
        "global_store_dwordx4 %[so1], v[172:175], %[d1] sc1\n\t"
        "global_store_dwordx4 %[so2], v[176:179], %[d0] sc1\n\t"
        "global_store_dwordx4 %[so2], v[180:183], %[d1] sc1\n\t"
        "global_store_dwordx4 %[so3], v[184:187], %[d0] sc1\n\t"
        "global_store_dwordx4 %[so3], v[188:191], %[d1] sc1\n\t"
        "s_barrier\n\t"
        "s_add_u32 m0, %[ub], 16384\n\t"
        "s_add_u32 s98, %[wlo], 0\n\t"
        "s_addc_u32 s99, %[whi], 0\n\t"
        "global_load_lds_dwordx4 %[wo0], s[98:99]\n\t"
        "global_load_lds_dwordx4 %[wo1], s[98:99] offset:1024\n\t"
        "global_load_lds_dwordx4 %[wo2], s[98:99] offset:2048\n\t"
        "global_load_lds_dwordx4 %[wo3], s[98:99] offset:3072\n\t"
        "v_add_u32_e32 v62, 0xffff0000, v62\n\t"
        "ds_write_b128 v62, v[192:195] offset:0\n\t"
        "ds_write_b128 v62, v[196:199] offset:1024\n\t"
        "ds_write_b128 v62, v[200:203] offset:2048\n\t"
        "ds_write_b128 v62, v[204:207] offset:3072\n\t"
        "ds_write_b128 v62, v[208:211] offset:32768\n\t"
        "ds_write_b128 v62, v[212:215] offset:33792\n\t"
        "ds_write_b128 v62, v[216:219] offset:34816\n\t"
        "ds_write_b128 v62, v[220:223] offset:35840\n\t"
        "v_add_u32_e32 v62, 0x10000, v62\n\t"
        "ds_write_b128 v62, v[224:227] offset:0\n\t"
        "ds_write_b128 v62, v[228:231] offset:1024\n\t"
        "ds_write_b128 v62, v[232:235] offset:2048\n\t"
        "ds_write_b128 v62, v[236:239] offset:3072\n\t"
        "s_add_u32 m0, %[ub], 49152\n\t"
        "s_add_u32 s98, %[wlo], 245760\n\t"
        "s_addc_u32 s99, %[whi], 0\n\t"
        "global_load_lds_dwordx4 %[wo0], s[98:99]\n\t"
        "global_load_lds_dwordx4 %[wo1], s[98:99] offset:1024\n\t"
        "global_load_lds_dwordx4 %[wo2], s[98:99] offset:2048\n\t"
        "global_load_lds_dwordx4 %[wo3], s[98:99] offset:3072\n\t"
        "s_add_u32 m0, %[ub], 81920\n\t"
        "s_add_u32 s98, %[wlo], 491520\n\t"
        "s_addc_u32 s99, %[whi], 0\n\t"
        "global_load_lds_dwordx4 %[wo0], s[98:99]\n\t"
        "global_load_lds_dwordx4 %[wo1], s[98:99] offset:1024\n\t"
        "global_load_lds_dwordx4 %[wo2], s[98:99] offset:2048\n\t"
        "global_load_lds_dwordx4 %[wo3], s[98:99] offset:3072\n\t"
        "s_waitcnt vmcnt(8) lgkmcnt(0)\n\t"
        "v_add_u32_e32 v24, 0x9e3779b9, v24\n\t"
        "s_barrier\n\t"
        "s_cmp_lg_u32 %[ub], 0\n\t"
        "s_cbranch_scc1 .Lzf_%=\n\t"
        "s_mov_b32 exec_hi, 0\n\t"
        "global_store_dword v22, v24, %[fb] sc1\n\t"
        "s_mov_b32 exec_hi, -1\n\t"
        ".Lzf_%=:\n\t"
        "s_add_u32 m0, %[ub], 98304\n\t"
        "s_add_u32 s98, %[xlo], 384\n\t"
        "s_addc_u32 s99, %[xhi], 0\n\t"
        "global_load_lds_dwordx4 %[xo0], s[98:99]\n\t"
        "global_load_lds_dwordx4 %[xo1], s[98:99] offset:1024\n\t"
        "global_load_lds_dwordx4 %[xo2], s[98:99] offset:2048\n\t"
        "global_load_lds_dwordx4 %[xo3], s[98:99] offset:3072\n\t"
        "s_cmp_eq_u32 %[ub], 0\n\t"
        "s_waitcnt vmcnt(9) lgkmcnt(0)\n\t"
        "s_cbranch_scc1 .Lz8_%=\n\t"
        "s_waitcnt vmcnt(8)\n\t"
        ".Lz8_%=:\n\t"
        "s_barrier\n\t"
        "s_add_u32 m0, %[ub], 114688\n\t"
        "s_add_u32 s98, %[wlo], 737280\n\t"
        "s_addc_u32 s99, %[whi], 0\n\t"
        "global_load_lds_dwordx4 %[wo0], s[98:99]\n\t"
        "global_load_lds_dwordx4 %[wo1], s[98:99] offset:1024\n\t"
        "global_load_lds_dwordx4 %[wo2], s[98:99] offset:2048\n\t"
        "global_load_lds_dwordx4 %[wo3], s[98:99] offset:3072\n\t"
        "s_add_u32 m0, %[ub], 131072\n\t"
        "s_add_u32 s98, %[xlo], 512\n\t"
        "s_addc_u32 s99, %[xhi], 0\n\t"
        "global_load_lds_dwordx4 %[xo0], s[98:99]\n\t"
        "global_load_lds_dwordx4 %[xo1], s[98:99] offset:1024\n\t"
        "global_load_lds_dwordx4 %[xo2], s[98:99] offset:2048\n\t"
        "global_load_lds_dwordx4 %[xo3], s[98:99] offset:3072\n\t"
        "s_add_u32 m0, %[ub], 147456\n\t"
        "s_add_u32 s98, %[wlo], 983040\n\t"
        "s_addc_u32 s99, %[whi], 0\n\t"
        "global_load_lds_dwordx4 %[wo0], s[98:99]\n\t"
        "global_load_lds_dwordx4 %[wo1], s[98:99] offset:1024\n\t"
        "global_load_lds_dwordx4 %[wo2], s[98:99] offset:2048\n\t"
        "global_load_lds_dwordx4 %[wo3], s[98:99] offset:3072\n\t"
            : GEMM_OUT, "+{v62}"(wb), "+{v[16:19]}"(cs[4]), "+{v[20:23]}"(cs[5]), "=&{v24}"(epoch)
            : [ct] "s"(ct), [ub] "s"(u * 4096), [wlo] "s"((u32)(size_t)wsA), [whi] "s"((u32)((size_t)wsA >> 32)), [wo0] "v"(wo[0]), [wo1] "v"(wo[1]), [wo2] "v"(wo[2]), [wo3] "v"(wo[3]),
              [xlo] "s"((u32)(size_t)xs), [xhi] "s"((u32)((size_t)xs >> 32)), [xo0] "v"(xo[0]), [xo1] "v"(xo[1]), [xo2] "v"(xo[2]), [xo3] "v"(xo[3]),
              [so0] "v"(so0[0]), [so1] "v"(so0[1]), [so2] "v"(so0[2]), [so3] "v"(so0[3]), [d0] "s"(dst), [d1] "s"(dst + ts), [tch] "v"(tch), [tb] "s"(wA + ct * 256 + ((tb >> 1) * 4 + u) * 10 * 3840),
              [ws] "s"(ws), [fb] "s"(ws + W_FB + (ct * 16 + tb) * 128)
            : "v64","v65","v66","v67","v68","v69","v70","v71","v72","v73","v74","v75","v76","v77","v78","v79","v80","v81","v82","v83","v84","v85","v86","v87","v88","v89","v90","v91","v92","v93","v94","v95","v96","v97","v98","v99","v100","v101","v102","v103","v104","v105","v106","v107","v108","v109","v110","v111","v112","v113","v114","v115","v116","v117","v118","v119","v120","v121","v122","v123","v124","v125","v126","v127","v128","v129","v130","v131","v132","v133","v134","v135","v136","v137","v138","v139","v140","v141","v142","v143","v144","v145","v146","v147","v148","v149","v150","v151","v152","v153","v154","v155","v156","v157","v158","v159", "v192","v193","v194","v195","v196","v197","v198","v199","v200","v201","v202","v203","v204","v205","v206","v207","v208","v209","v210","v211","v212","v213","v214","v215","v216","v217","v218","v219","v220","v221","v222","v223","v224","v225","v226","v227","v228","v229","v230","v231","v232","v233","v234","v235","v236","v237","v238","v239", "s98", "s99", "scc", "memory");
        }
    } else {
        if (grp == 0) mkxo();
        const u32x4 gv0 = *(const GAS u32x4*)((const GAS char*)gw + l31 * 32 + hi * 16), gv1 = *(const GAS u32x4*)((const GAS char*)gw + 1024 + (l31 & 7) * 32 + hi * 16);
        __builtin_amdgcn_s_sleep(60);
        epoch = __hip_atomic_load((const GAS u32*)(ws + W_EPOCH), RLX_AGENT) + 0x9e3779b9u;
        consts0();
        if (grp == 1) { consts1(); consts2(); }
        BAR_VM(0);
#pragma unroll 1
        for (int s = grp; s < 10; s += 2) dma4<0, 0>(xs + s * 128, s * 16384 + u * 4096, xo);
        BAR_VM(0);
        if (grp == 0) {
            f32x16 z = {};
#pragma unroll 1
            for (int s = 0; s < 40; s += 8) {
                u32x4 xf[8]; const u32x4 gv = s < 32 ? gv0 : gv1;
#pragma unroll
                for (int k = 0; k < 8; ++k) xf[k] = lds128((s >> 2) * 16384 + (k >> 2) * 16384 + (32 * u + l31) * 128 + (((2 * (k & 3) + hi) ^ ((l31 >> 1) & 7)) << 4));
                SB();
#pragma unroll
                for (int k = 0; k < 8; ++k) z = mfma32(l31 == ((s + k) & 31) ? gv : u32x4{0, 0, 0, 0}, xf[k], z);
            }
            float zs = 0.f;
#pragma unroll
            for (int r = 0; r < 16; ++r) zs += z[r];
            { auto r = __builtin_amdgcn_permlane32_swap(__float_as_uint(zs), __float_as_uint(zs), false, false); zs = __uint_as_float(r[0]) + __uint_as_float(r[1]); }
            const int p = 32 * u + (l31 & 3) + ((l31 & 12) << 1) + ((l31 & 16) >> 2);
            st32(ws, W_G + hi * 8192 + (128 * tb + p) * 4, __float_as_uint(__builtin_amdgcn_rcpf(1.f + __expf(hi ? zs : -zs))));
        }
        BAR_L();
        if (grp == 1) BAR_L();
    }
    u32 l16 = lane * 16;
    asm volatile(
        "s_mov_b32 s96, 0x3ea52b5b\n\t"
        "s_mov_b32 s97, 0x3ea52b5b\n\t"
        "s_cmp_lg_u32 s59, 0\n\t"
        "s_cbranch_scc1 .Lh1\n\t"
        "buffer_load_dword v25, v20, s[40:43], 0 offen sc1\n\t"
        "s_bitcmp0_b32 s84, 0\n\t"
        "s_cbranch_scc1 .Lh8\n\t"
        "s_cmp_eq_u32 s78, 0\n\t"
        "s_waitcnt vmcnt(18)\n\t"
        "s_cbranch_scc1 .Lh9\n\t"
        "s_waitcnt vmcnt(17)\n\t"
        ".Lh9:\n\t"
        "s_barrier\n\t"
        ".Lh8:\n\t"
        "v_add_u32_e32 v45, s59, v16\n\t"
        "v_add_u32_e32 v46, s59, v18\n\t"
        "v_add_u32_e32 v28, s85, v150\n\t"
        "v_mov_b32_e32 v44, 0xdddddddd\n\t"
        "v_and_b32_e32 v19, 0xf0, v150\n\t"
        "v_lshrrev_b32_e32 v151, 5, v150\n\t"
        "v_mul_u32_u24_e32 v19, 0xa0, v19\n\t"
        "v_and_b32_e32 v151, 24, v151\n\t"
        "v_add_u32_e32 v19, v19, v151\n\t"
        "v_mov_b64_e32 v[152:153], 0\n\t"
        "v_lshrrev_b32_e32 v47, 2, v23\n\t"
        "v_add_u32_e32 v47, 1280, v47\n\t"
        "v_mfma_f32_32x32x8_bf16 v[48:63], v[152:153], v[152:153], 0\n\t"
        "v_mfma_f32_32x32x8_bf16 v[64:79], v[152:153], v[152:153], 0\n\t"
        "s_mov_b32 exec_lo, 0xf0000\n\t"
        "s_mov_b32 exec_hi, 0xffff\n\t"
        "v_add_u32_e32 v47, 16, v47\n\t"
        "s_mov_b64 exec, -1\n\t"
        "v_add_u32_e32 v31, 640, v47\n\t"
        "s_mov_b32 exec_lo, 0xffe00000\n\t"
        "s_mov_b32 exec_hi, 0xffe00000\n\t"
        "v_mov_b32_e32 v47, 0x40000\n\t"
        "v_mov_b32_e32 v31, 0x40000\n\t"
        "s_mov_b32 exec_lo, 0x100000\n\t"
        "s_mov_b32 exec_hi, 0\n\t"
        "v_mov_b32_e32 v47, 2576\n\t"
        "v_mov_b32_e32 v31, 2576\n\t"
        "s_mov_b32 exec_lo, 0\n\t"
        "s_mov_b32 exec_hi, 0x100000\n\t"
        "v_mov_b32_e32 v47, 2560\n\t"
        "v_mov_b32_e32 v31, 2560\n\t"
        "s_mov_b64 exec, -1\n\t"
        "v_add_u32_e32 v47, s59, v47\n\t"
        "v_add_u32_e32 v31, s59, v31\n\t"
        "s_waitcnt vmcnt(1)\n\t"
        "s_barrier\n\t"
        "s_waitcnt vmcnt(0)\n\t"
        "v_cmp_ne_u32_e32 vcc, v25, v24\n\t"
        "s_cbranch_vccz .Lq1\n\t"
        "s_mov_b32 s94, 0x80000\n\t"
        ".Lp1:\n\t"
        "s_sleep 1\n\t"
        "buffer_load_dword v25, v20, s[40:43], 0 offen sc1\n\t"
        "s_waitcnt vmcnt(0)\n\t"
        "v_cmp_ne_u32_e32 vcc, v25, v24\n\t"
        "s_sub_u32 s94, s94, 1\n\t"
        "s_cbranch_scc1 .Lq1\n\t"
        "s_cbranch_vccnz .Lp1\n\t"
        ".Lq1:\n\t"
        "buffer_load_dwordx2 v[144:145], v16, s[40:43], s77 offen sc1\n\t"
        "buffer_load_dwordx2 v[146:147], v16, s[40:43], s77 offen offset:256 sc1\n\t"
        "buffer_load_dwordx2 v[148:149], v17, s[40:43], s77 offen sc1\n\t"
        "buffer_load_dwordx2 v[36:37], v16, s[40:43], s86 offen sc1\n\t"
        "buffer_load_dwordx2 v[38:39], v16, s[40:43], s86 offen offset:256 sc1\n\t"
        "buffer_load_dwordx2 v[42:43], v17, s[40:43], s86 offen sc1\n\t"
        "s_add_u32 s92, s86, 1280\n\t"
        "buffer_load_dwordx2 v[32:33], v16, s[40:43], s92 offen sc1\n\t"
        "buffer_load_dwordx2 v[34:35], v16, s[40:43], s92 offen offset:256 sc1\n\t"
        "buffer_load_dwordx2 v[40:41], v17, s[40:43], s92 offen sc1\n\t"
        "s_add_u32 m0, s90, 0\n\t"
        "s_add_u32 s92, s87, 0\n\t"
        "buffer_load_dwordx4 v150, s[40:43], s92 offen sc1 lds\n\t"
        "buffer_load_dwordx4 v150, s[40:43], s92 offen offset:1024 sc1 lds\n\t"
        "s_mov_b32 exec_hi, 0\n\t"
        "buffer_load_dwordx4 v150, s[40:43], s92 offen offset:2048 sc1 lds\n\t"
        "s_mov_b32 exec_hi, -1\n\t"
        "s_add_u32 m0, s90, 2592\n\t"
        "s_add_u32 s92, s87, 10240\n\t"
        "buffer_load_dwordx4 v150, s[40:43], s92 offen sc1 lds\n\t"
        "buffer_load_dwordx4 v150, s[40:43], s92 offen offset:1024 sc1 lds\n\t"
        "s_mov_b32 exec_hi, 0\n\t"
        "buffer_load_dwordx4 v150, s[40:43], s92 offen offset:2048 sc1 lds\n\t"
        "s_mov_b32 exec_hi, -1\n\t"
        "s_waitcnt vmcnt(6)\n\t"
        "s_mov_b32 exec_lo, 0\n\t"
        "v_mov_b64_e32 v[148:149], 0\n\t"
        "s_mov_b32 exec_lo, -1\n\t"
        "s_nop 0\n\t"
        "v_mfma_f32_32x32x16_bf16 v[80:95], v[144:147], v[32:35], 0\n\t"
        "v_mfma_f32_32x32x16_bf16 v[96:111], v[144:147], v[36:39], 0\n\t"
        "v_mfma_f32_32x32x8_bf16 v[80:95], v[148:149], v[40:41], v[80:95]\n\t"
        "v_mfma_f32_32x32x8_bf16 v[96:111], v[148:149], v[42:43], v[96:111]\n\t"
        "v_lshlrev_b32_e32 v112, 16, v32\n\t"
        "v_and_b32_e32 v113, 0xffff0000, v32\n\t"
        "v_pk_mul_f32 v[112:113], v[112:113], s[96:97]\n\t"
        "v_cvt_pk_bf16_f32 v32, v112, v113\n\t"
        "v_lshlrev_b32_e32 v114, 16, v33\n\t"
        "v_and_b32_e32 v115, 0xffff0000, v33\n\t"
        "v_pk_mul_f32 v[114:115], v[114:115], s[96:97]\n\t"
        "v_cvt_pk_bf16_f32 v33, v114, v115\n\t"
        "v_lshlrev_b32_e32 v112, 16, v34\n\t"
        "v_and_b32_e32 v113, 0xffff0000, v34\n\t"
        "v_pk_mul_f32 v[112:113], v[112:113], s[96:97]\n\t"
        "v_cvt_pk_bf16_f32 v34, v112, v113\n\t"
        "v_lshlrev_b32_e32 v114, 16, v35\n\t"
        "v_and_b32_e32 v115, 0xffff0000, v35\n\t"
        "v_pk_mul_f32 v[114:115], v[114:115], s[96:97]\n\t"
        "v_cvt_pk_bf16_f32 v35, v114, v115\n\t"
        "v_lshlrev_b32_e32 v112, 16, v36\n\t"
        "v_and_b32_e32 v113, 0xffff0000, v36\n\t"
        "v_pk_mul_f32 v[112:113], v[112:113], s[96:97]\n\t"
        "v_cvt_pk_bf16_f32 v36, v112, v113\n\t"
        "v_lshlrev_b32_e32 v114, 16, v37\n\t"
        "v_and_b32_e32 v115, 0xffff0000, v37\n\t"
        "v_pk_mul_f32 v[114:115], v[114:115], s[96:97]\n\t"
        "v_cvt_pk_bf16_f32 v37, v114, v115\n\t"
        "v_lshlrev_b32_e32 v112, 16, v38\n\t"
        "v_and_b32_e32 v113, 0xffff0000, v38\n\t"
        "v_pk_mul_f32 v[112:113], v[112:113], s[96:97]\n\t"
        "v_cvt_pk_bf16_f32 v38, v112, v113\n\t"
        "v_lshlrev_b32_e32 v114, 16, v39\n\t"
        "v_and_b32_e32 v115, 0xffff0000, v39\n\t"
        "v_pk_mul_f32 v[114:115], v[114:115], s[96:97]\n\t"
        "v_cvt_pk_bf16_f32 v39, v114, v115\n\t"
        "ds_write_b128 v28, v[32:35] offset:1024\n\t"
        "ds_write_b128 v28, v[36:39]\n\t"
        "v_max3_f32 v116, v80, v81, v82\n\t"
        "v_max3_f32 v117, v83, v84, v85\n\t"
        "v_max3_f32 v116, v116, v86, v87\n\t"
        "v_max3_f32 v117, v117, v88, v89\n\t"
        "v_max3_f32 v116, v116, v90, v91\n\t"
        "v_max3_f32 v117, v117, v92, v93\n\t"
        "v_max3_f32 v116, v116, v94, v95\n\t"
        "v_max_f32_e32 v116, v116, v117\n\t"
        "v_max3_f32 v118, v96, v97, v98\n\t"
        "v_max3_f32 v119, v99, v100, v101\n\t"
        "v_max3_f32 v118, v118, v102, v103\n\t"
        "v_max3_f32 v119, v119, v104, v105\n\t"
        "v_max3_f32 v118, v118, v106, v107\n\t"
        "v_max3_f32 v119, v119, v108, v109\n\t"
        "v_max3_f32 v118, v118, v110, v111\n\t"
        "v_max_f32_e32 v118, v118, v119\n\t"
        "v_mov_b32_e32 v117, v116\n\t"
        "v_mov_b32_e32 v119, v118\n\t"
        "v_lshlrev_b32_e32 v112, 16, v40\n\t"
        "v_and_b32_e32 v113, 0xffff0000, v40\n\t"
        "v_pk_mul_f32 v[112:113], v[112:113], s[96:97]\n\t"
        "v_cvt_pk_bf16_f32 v40, v112, v113\n\t"
        "v_lshlrev_b32_e32 v114, 16, v41\n\t"
        "v_and_b32_e32 v115, 0xffff0000, v41\n\t"
        "v_pk_mul_f32 v[114:115], v[114:115], s[96:97]\n\t"
        "v_cvt_pk_bf16_f32 v41, v114, v115\n\t"
        "v_lshlrev_b32_e32 v112, 16, v42\n\t"
        "v_and_b32_e32 v113, 0xffff0000, v42\n\t"
        "v_pk_mul_f32 v[112:113], v[112:113], s[96:97]\n\t"
        "v_cvt_pk_bf16_f32 v42, v112, v113\n\t"
        "v_lshlrev_b32_e32 v114, 16, v43\n\t"
        "v_and_b32_e32 v115, 0xffff0000, v43\n\t"
        "v_pk_mul_f32 v[114:115], v[114:115], s[96:97]\n\t"
        "v_cvt_pk_bf16_f32 v43, v114, v115\n\t"
        "v_permlane32_swap_b32_e32 v116, v117\n\t"
        "v_permlane32_swap_b32_e32 v118, v119\n\t"
        "v_max_f32_e32 v116, v116, v117\n\t"
        "v_max_f32_e32 v118, v118, v119\n\t"
        "v_mul_f32_e64 v116, -v116, s96\n\t"
        "v_mul_f32_e64 v118, -v118, s96\n\t"
        "s_mov_b32 exec_lo, 0\n\t"
        "v_cvt_pk_bf16_f32 v40, v116, 0\n\t"
        "v_cvt_pk_bf16_f32 v42, v118, 0\n\t"
        "v_mov_b32_e32 v41, 0\n\t"
        "v_mov_b32_e32 v43, 0\n\t"
        "v_mov_b32_e32 v148, 0x3f803f80\n\t"
        "s_mov_b32 exec_lo, -1\n\t"
        "ds_write_b64 v28, v[40:41] offset:2056\n\t"
        "ds_write_b64 v28, v[42:43] offset:2048\n\t"
        "ds_write_b32 v28, v24 offset:12288\n\t"
        "v_mfma_f32_32x32x8_bf16 v[80:95], v[148:149], v[40:41], 0\n\t"
        "v_mfma_f32_32x32x16_bf16 v[80:95], v[144:147], v[32:35], v[80:95]\n\t"
        "s_mov_b64 vcc, 0\n\t"
        "s_nop 5\n\t"
        "s_waitcnt vmcnt(0) lgkmcnt(0)\n\t"
        "s_barrier\n\t"
        "s_branch .Lh2\n\t"
        ".Lh1:\n\t"
        "s_cmp_lg_u32 s78, 4\n\t"
        "s_cbranch_scc1 .Lrca\n\t"
        "s_lshr_b32 s92, s80, 15\n\t"
        "s_lshl_b32 s92, s92, 10\n\t"
        "v_add_u32_e32 v28, s92, v23\n\t"
        "s_getpc_b64 s[98:99]\n\t"
        ".Lrpa:\n\t"
        "v_min_u32_e32 v28, .Lend-.Lrpa, v28\n\t"
        "s_mov_b64 exec, 0xff\n\t"
        "global_load_dword v26, v28, s[98:99]\n\t"
        "s_mov_b64 exec, -1\n\t"
        ".Lrca:\n\t"
        "s_lshl_b32 s92, s84, 8\n\t"
        "s_and_b32 s92, s92, 0xc00\n\t"
        "v_add_u32_e32 v151, s92, v150\n\t"
        "ds_write_b128 v151, v[200:203] offset:57344\n\t"
        "s_cmp_ge_u32 s92, 0x800\n\t"
        "s_cbranch_scc1 .Lh4\n\t"
        "ds_write_b128 v151, v[204:207] offset:49152\n\t"
        "s_branch .Lh6\n\t"
        ".Lh4:\n\t"
        "s_cmp_eq_u32 s92, 0x800\n\t"
        "s_cbranch_scc1 .Lh5\n\t"
        "ds_write_b128 v150, v[0:3] offset:51200\n\t"
        "ds_write_b128 v150, v[4:7] offset:52224\n\t"
        "ds_write_b128 v150, v[8:11] offset:53248\n\t"
        "ds_write_b128 v150, v[12:15] offset:54272\n\t"
        "s_branch .Lh6\n\t"
        ".Lh5:\n\t"
        "v_lshrrev_b32_e32 v151, 5, v150\n\t"
        "v_mul_u32_u24_e32 v151, 2560, v151\n\t"
        "v_add_u32_e32 v151, v151, v150\n\t"
        "s_mov_b32 exec_hi, 15\n\t"
        "ds_write_b128 v151, v[204:207] offset:2560\n\t"
        "s_mov_b32 exec_hi, -1\n\t"
        ".Lh6:\n\t"
        "v_add_u32_e32 v45, s59, v16\n\t"
        "v_add_u32_e32 v46, s59, v18\n\t"
        "v_add_u32_e32 v28, s85, v150\n\t"
        "v_mov_b32_e32 v44, 0xdddddddd\n\t"
        "v_and_b32_e32 v19, 0xf0, v150\n\t"
        "v_lshrrev_b32_e32 v151, 5, v150\n\t"
        "v_mul_u32_u24_e32 v19, 0xa0, v19\n\t"
        "v_and_b32_e32 v151, 24, v151\n\t"
        "v_add_u32_e32 v19, v19, v151\n\t"
        "v_mov_b64_e32 v[152:153], 0\n\t"
        "v_lshrrev_b32_e32 v47, 2, v23\n\t"
        "v_add_u32_e32 v47, 1280, v47\n\t"
        "v_mfma_f32_32x32x8_bf16 v[48:63], v[152:153], v[152:153], 0\n\t"
        "v_mfma_f32_32x32x8_bf16 v[64:79], v[152:153], v[152:153], 0\n\t"
        "s_mov_b32 exec_lo, 0xf0000\n\t"
        "s_mov_b32 exec_hi, 0xffff\n\t"
        "v_add_u32_e32 v47, 16, v47\n\t"
        "s_mov_b64 exec, -1\n\t"
        "v_add_u32_e32 v31, 640, v47\n\t"
        "s_mov_b32 exec_lo, 0xffe00000\n\t"
        "s_mov_b32 exec_hi, 0xffe00000\n\t"
        "v_mov_b32_e32 v47, 0x40000\n\t"
        "v_mov_b32_e32 v31, 0x40000\n\t"
        "s_mov_b32 exec_lo, 0x100000\n\t"
        "s_mov_b32 exec_hi, 0\n\t"
        "v_mov_b32_e32 v47, 2576\n\t"
        "v_mov_b32_e32 v31, 2576\n\t"
        "s_mov_b32 exec_lo, 0\n\t"
        "s_mov_b32 exec_hi, 0x100000\n\t"
        "v_mov_b32_e32 v47, 2560\n\t"
        "v_mov_b32_e32 v31, 2560\n\t"
        "s_mov_b64 exec, -1\n\t"
        "v_add_u32_e32 v47, s59, v47\n\t"
        "v_add_u32_e32 v31, s59, v31\n\t"
        "v_add_u32_e32 v14, s79, v14\n\t"
        "v_add_u32_e32 v15, s79, v15\n\t"
        "s_bitcmp1_b32 s84, 0\n\t"
        "s_cselect_b64 vcc, -1, 0\n\t"
        "s_waitcnt lgkmcnt(0)\n\t"
        "s_barrier\n\t"
        "ds_read_b128 v[32:35], v28\n\t"
        "ds_read_b128 v[36:39], v28 offset:1024\n\t"
        "ds_read_b128 v[40:43], v28 offset:2048\n\t"
        "ds_read_b32 v24, v28 offset:12288\n\t"
        "ds_read_b64 v[144:145], v45 offset:0\n\t"
        "ds_read_b64 v[146:147], v45 offset:256\n\t"
        "ds_read_b64 v[148:149], v46 offset:0\n\t"
        "s_waitcnt lgkmcnt(0)\n\t"
        "v_mfma_f32_32x32x8_bf16 v[80:95], v[148:149], v[40:41], 0\n\t"
        "v_mfma_f32_32x32x16_bf16 v[80:95], v[144:147], v[32:35], v[80:95]\n\t"
        "s_nop 9\n\t"
        ".Lh2:\n\t"
        "v_mfma_f32_32x32x8_bf16 v[96:111], v[148:149], v[42:43], 0\n\t"
        "ds_read_b64 v[148:149], v46 offset:5184\n\t"
        "s_cmp_lg_u32 s59, 0\n\t"
        "s_cbranch_scc1 .Lo10\n\t"
        "ds_read_b128 v[0:3], v150 offset:51200\n\t"
        "ds_read_b128 v[4:7], v150 offset:52224\n\t"
        "ds_read_b128 v[8:11], v150 offset:53248\n\t"
        "ds_read_b128 v[12:15], v150 offset:54272\n\t"
        "s_add_u32 m0, s90, 20736\n\t"
        "s_add_u32 s92, s87, 20480\n\t"
        "buffer_load_dwordx4 v150, s[40:43], s92 offen sc1 lds\n\t"
        ".Lo10:\n\t"
        "v_exp_f32_e32 v80, v80\n\t"
        "v_exp_f32_e32 v81, v81\n\t"
        "v_exp_f32_e32 v82, v82\n\t"
        "v_exp_f32_e32 v83, v83\n\t"
        "v_mfma_f32_32x32x16_bf16 v[96:111], v[144:147], v[36:39], v[96:111]\n\t"
        "ds_read_b64 v[144:145], v45 offset:5184\n\t"
        "ds_read_b64 v[146:147], v45 offset:5440\n\t"
        "s_cmp_lg_u32 s59, 0\n\t"
        "s_cbranch_scc1 .Lo11\n\t"
        "buffer_load_dwordx4 v150, s[40:43], s92 offen offset:1024 sc1 lds\n\t"
        "s_mov_b32 exec_hi, 0\n\t"
        "buffer_load_dwordx4 v150, s[40:43], s92 offen offset:2048 sc1 lds\n\t"
        "s_mov_b32 exec_hi, -1\n\t"
        ".Lo11:\n\t"
        "v_exp_f32_e32 v84, v84\n\t"
        "v_exp_f32_e32 v85, v85\n\t"
        "v_exp_f32_e32 v86, v86\n\t"
        "v_exp_f32_e32 v87, v87\n\t"
        "ds_read_b128 v[152:155], v47 offset:0\n\t"
        "s_cmp_lg_u32 s59, 0\n\t"
        "s_cbranch_scc1 .Lo12\n\t"
        "s_add_u32 m0, s90, 23328\n\t"
        "s_add_u32 s92, s87, 30720\n\t"
        "buffer_load_dwordx4 v150, s[40:43], s92 offen sc1 lds\n\t"
        "buffer_load_dwordx4 v150, s[40:43], s92 offen offset:1024 sc1 lds\n\t"
        ".Lo12:\n\t"
        "v_exp_f32_e32 v88, v88\n\t"
        "v_exp_f32_e32 v89, v89\n\t"
        "v_exp_f32_e32 v90, v90\n\t"
        "v_exp_f32_e32 v91, v91\n\t"
        "ds_read_b128 v[156:159], v31 offset:0\n\t"
        "s_cmp_lg_u32 s59, 0\n\t"
        "s_cbranch_scc1 .Lo13\n\t"
        "s_mov_b32 exec_hi, 0\n\t"
        "buffer_load_dwordx4 v150, s[40:43], s92 offen offset:2048 sc1 lds\n\t"
        "s_mov_b32 exec_hi, -1\n\t"
        "buffer_load_dword v25, v21, s[40:43], 0 offen sc1\n\t"
        ".Lo13:\n\t"
        "v_exp_f32_e32 v92, v92\n\t"
        "v_exp_f32_e32 v93, v93\n\t"
        "v_exp_f32_e32 v94, v94\n\t"
        "v_exp_f32_e32 v95, v95\n\t"
        "s_setprio 2\n\t"
        "s_waitcnt lgkmcnt(2)\n\t"
        "v_mfma_f32_32x32x8_bf16 v[112:127], v[148:149], v[40:41], 0\n\t"
        "s_cbranch_vccz .Ls2\n\t"
        "buffer_store_dwordx4 v[160:163], v192, s[40:43], s83 offen sc1\n\t"
        ".Ls2:\n\t"
        "v_exp_f32_e32 v96, v96\n\t"
        "v_exp_f32_e32 v97, v97\n\t"
        "v_exp_f32_e32 v98, v98\n\t"
        "v_exp_f32_e32 v99, v99\n\t"
        "v_mfma_f32_32x32x16_bf16 v[112:127], v[144:147], v[32:35], v[112:127]\n\t"
        "s_cbranch_vccz .Ls3\n\t"
        "buffer_store_dwordx4 v[164:167], v196, s[40:43], s83 offen sc1\n\t"
        ".Ls3:\n\t"
        "v_exp_f32_e32 v100, v100\n\t"
        "v_exp_f32_e32 v101, v101\n\t"
        "v_exp_f32_e32 v102, v102\n\t"
        "v_exp_f32_e32 v103, v103\n\t"
        "s_waitcnt lgkmcnt(0)\n\t"
        "v_smfmac_f32_32x32x32_bf16 v[48:63], v[152:155], v[80:87], v44\n\t"
        "s_cbranch_vccz .Ls4\n\t"
        "buffer_store_dwordx4 v[168:171], v193, s[40:43], s83 offen sc1\n\t"
        ".Ls4:\n\t"
        "v_exp_f32_e32 v104, v104\n\t"
        "v_exp_f32_e32 v105, v105\n\t"
        "v_exp_f32_e32 v106, v106\n\t"
        "v_exp_f32_e32 v107, v107\n\t"
        "v_smfmac_f32_32x32x32_bf16 v[48:63], v[156:159], v[88:95], v44\n\t"
        "s_cbranch_vccz .Ls5\n\t"
        "buffer_store_dwordx4 v[172:175], v197, s[40:43], s83 offen sc1\n\t"
        ".Ls5:\n\t"
        "v_exp_f32_e32 v108, v108\n\t"
        "v_exp_f32_e32 v109, v109\n\t"
        "v_exp_f32_e32 v110, v110\n\t"
        "v_exp_f32_e32 v111, v111\n\t"
        "v_mfma_f32_32x32x8_bf16 v[128:143], v[148:149], v[42:43], 0\n\t"
        "ds_read_b64 v[148:149], v46 offset:10368\n\t"
        "v_exp_f32_e32 v112, v112\n\t"
        "v_exp_f32_e32 v113, v113\n\t"
        "v_exp_f32_e32 v114, v114\n\t"
        "v_exp_f32_e32 v115, v115\n\t"
        "v_mfma_f32_32x32x16_bf16 v[128:143], v[144:147], v[36:39], v[128:143]\n\t"
        "ds_read_b64 v[144:145], v45 offset:10368\n\t"
        "ds_read_b64 v[146:147], v45 offset:10624\n\t"
        "v_exp_f32_e32 v116, v116\n\t"
        "v_exp_f32_e32 v117, v117\n\t"
        "v_exp_f32_e32 v118, v118\n\t"
        "v_exp_f32_e32 v119, v119\n\t"
        "v_smfmac_f32_32x32x32_bf16 v[64:79], v[152:155], v[96:103], v44\n\t"
        "ds_read_b128 v[152:155], v47 offset:5184\n\t"
        "v_exp_f32_e32 v120, v120\n\t"
        "v_exp_f32_e32 v121, v121\n\t"
        "v_exp_f32_e32 v122, v122\n\t"
        "v_exp_f32_e32 v123, v123\n\t"
        "v_smfmac_f32_32x32x32_bf16 v[64:79], v[156:159], v[104:111], v44\n\t"
        "ds_read_b128 v[156:159], v31 offset:5184\n\t"
        "v_exp_f32_e32 v124, v124\n\t"
        "v_exp_f32_e32 v125, v125\n\t"
        "v_exp_f32_e32 v126, v126\n\t"
        "v_exp_f32_e32 v127, v127\n\t"
        "s_waitcnt lgkmcnt(2)\n\t"
        "v_mfma_f32_32x32x8_bf16 v[80:95], v[148:149], v[40:41], 0\n\t"
        "s_cbranch_vccz .Ls6\n\t"
        "buffer_store_dwordx4 v[176:179], v194, s[40:43], s83 offen sc1\n\t"
        ".Ls6:\n\t"
        "v_exp_f32_e32 v128, v128\n\t"
        "v_exp_f32_e32 v129, v129\n\t"
        "v_exp_f32_e32 v130, v130\n\t"
        "v_exp_f32_e32 v131, v131\n\t"
        "v_mfma_f32_32x32x16_bf16 v[80:95], v[144:147], v[32:35], v[80:95]\n\t"
        "s_cbranch_vccz .Ls7\n\t"
        "buffer_store_dwordx4 v[180:183], v198, s[40:43], s83 offen sc1\n\t"
        ".Ls7:\n\t"
        "v_exp_f32_e32 v132, v132\n\t"
        "v_exp_f32_e32 v133, v133\n\t"
        "v_exp_f32_e32 v134, v134\n\t"
        "v_exp_f32_e32 v135, v135\n\t"
        "s_waitcnt lgkmcnt(0)\n\t"
        "v_smfmac_f32_32x32x32_bf16 v[48:63], v[152:155], v[112:119], v44\n\t"
        "s_cbranch_vccz .Ls8\n\t"
        "buffer_store_dwordx4 v[184:187], v195, s[40:43], s83 offen sc1\n\t"
        ".Ls8:\n\t"
        "v_exp_f32_e32 v136, v136\n\t"
        "v_exp_f32_e32 v137, v137\n\t"
        "v_exp_f32_e32 v138, v138\n\t"
        "v_exp_f32_e32 v139, v139\n\t"
        "v_smfmac_f32_32x32x32_bf16 v[48:63], v[156:159], v[120:127], v44\n\t"
        "s_cbranch_vccz .Ls9\n\t"
        "buffer_store_dwordx4 v[188:191], v199, s[40:43], s83 offen sc1\n\t"
        ".Ls9:\n\t"
        "v_exp_f32_e32 v140, v140\n\t"
        "v_exp_f32_e32 v141, v141\n\t"
        "v_exp_f32_e32 v142, v142\n\t"
        "v_exp_f32_e32 v143, v143\n\t"
        "s_setprio 1\n\t"
        "v_mfma_f32_32x32x8_bf16 v[96:111], v[148:149], v[42:43], 0\n\t"
        "ds_read_b64 v[148:149], v46 offset:15552\n\t"
        "v_exp_f32_e32 v80, v80\n\t"
        "v_exp_f32_e32 v81, v81\n\t"
        "v_exp_f32_e32 v82, v82\n\t"
        "v_exp_f32_e32 v83, v83\n\t"
        "v_mfma_f32_32x32x16_bf16 v[96:111], v[144:147], v[36:39], v[96:111]\n\t"
        "ds_read_b64 v[144:145], v45 offset:15552\n\t"
        "ds_read_b64 v[146:147], v45 offset:15808\n\t"
        "v_exp_f32_e32 v84, v84\n\t"
        "v_exp_f32_e32 v85, v85\n\t"
        "v_exp_f32_e32 v86, v86\n\t"
        "v_exp_f32_e32 v87, v87\n\t"
        "v_smfmac_f32_32x32x32_bf16 v[64:79], v[152:155], v[128:135], v44\n\t"
        "ds_read_b128 v[152:155], v47 offset:10368\n\t"
        "v_exp_f32_e32 v88, v88\n\t"
        "v_exp_f32_e32 v89, v89\n\t"
        "v_exp_f32_e32 v90, v90\n\t"
        "v_exp_f32_e32 v91, v91\n\t"
        "v_smfmac_f32_32x32x32_bf16 v[64:79], v[156:159], v[136:143], v44\n\t"
        "ds_read_b128 v[156:159], v31 offset:10368\n\t"
        "v_exp_f32_e32 v92, v92\n\t"
        "v_exp_f32_e32 v93, v93\n\t"
        "v_exp_f32_e32 v94, v94\n\t"
        "v_exp_f32_e32 v95, v95\n\t"
        "s_setprio 0\n\t"
        "s_waitcnt lgkmcnt(2)\n\t"
        "v_mfma_f32_32x32x8_bf16 v[112:127], v[148:149], v[40:41], 0\n\t"
        "v_exp_f32_e32 v96, v96\n\t"
        "v_exp_f32_e32 v97, v97\n\t"
        "v_exp_f32_e32 v98, v98\n\t"
        "v_exp_f32_e32 v99, v99\n\t"
        "v_mfma_f32_32x32x16_bf16 v[112:127], v[144:147], v[32:35], v[112:127]\n\t"
        "v_exp_f32_e32 v100, v100\n\t"
        "v_exp_f32_e32 v101, v101\n\t"
        "v_exp_f32_e32 v102, v102\n\t"
        "v_exp_f32_e32 v103, v103\n\t"
        "s_waitcnt lgkmcnt(0)\n\t"
        "v_smfmac_f32_32x32x32_bf16 v[48:63], v[152:155], v[80:87], v44\n\t"
        "v_exp_f32_e32 v104, v104\n\t"
        "v_exp_f32_e32 v105, v105\n\t"
        "v_exp_f32_e32 v106, v106\n\t"
        "v_exp_f32_e32 v107, v107\n\t"
        "v_smfmac_f32_32x32x32_bf16 v[48:63], v[156:159], v[88:95], v44\n\t"
        "v_exp_f32_e32 v108, v108\n\t"
        "v_exp_f32_e32 v109, v109\n\t"
        "v_exp_f32_e32 v110, v110\n\t"
        "v_exp_f32_e32 v111, v111\n\t"
        "s_cmp_lg_u32 s59, 0\n\t"
        "s_cbranch_scc1 .Lb0\n\t"
        "s_waitcnt vmcnt(0)\n\t"
        "v_cmp_ne_u32_e32 vcc, v25, v24\n\t"
        "s_cbranch_vccz .Lq14\n\t"
        "s_mov_b32 s94, 0x80000\n\t"
        ".Lp14:\n\t"
        "s_sleep 1\n\t"
        "buffer_load_dword v25, v21, s[40:43], 0 offen sc1\n\t"
        "s_waitcnt vmcnt(0)\n\t"
        "v_cmp_ne_u32_e32 vcc, v25, v24\n\t"
        "s_sub_u32 s94, s94, 1\n\t"
        "s_cbranch_scc1 .Lq14\n\t"
        "s_cbranch_vccnz .Lp14\n\t"
        ".Lq14:\n\t"
        ".Lb0:\n\t"
        "s_barrier\n\t"
        "s_setprio 3\n\t"
        "v_mfma_f32_32x32x8_bf16 v[128:143], v[148:149], v[42:43], 0\n\t"
        "ds_read_b64 v[148:149], v46 offset:20736\n\t"
        "v_exp_f32_e32 v112, v112\n\t"
        "v_exp_f32_e32 v113, v113\n\t"
        "v_exp_f32_e32 v114, v114\n\t"
        "v_exp_f32_e32 v115, v115\n\t"
        "v_mfma_f32_32x32x16_bf16 v[128:143], v[144:147], v[36:39], v[128:143]\n\t"
        "ds_read_b64 v[144:145], v45 offset:20736\n\t"
        "ds_read_b64 v[146:147], v45 offset:20992\n\t"
        "v_exp_f32_e32 v116, v116\n\t"
        "v_exp_f32_e32 v117, v117\n\t"
        "v_exp_f32_e32 v118, v118\n\t"
        "v_exp_f32_e32 v119, v119\n\t"
        "v_smfmac_f32_32x32x32_bf16 v[64:79], v[152:155], v[96:103], v44\n\t"
        "ds_read_b128 v[152:155], v47 offset:15552\n\t"
        "v_exp_f32_e32 v120, v120\n\t"
        "v_exp_f32_e32 v121, v121\n\t"
        "v_exp_f32_e32 v122, v122\n\t"
        "v_exp_f32_e32 v123, v123\n\t"
        "v_smfmac_f32_32x32x32_bf16 v[64:79], v[156:159], v[104:111], v44\n\t"
        "ds_read_b128 v[156:159], v31 offset:15552\n\t"
        "v_exp_f32_e32 v124, v124\n\t"
        "v_exp_f32_e32 v125, v125\n\t"
        "v_exp_f32_e32 v126, v126\n\t"
        "v_exp_f32_e32 v127, v127\n\t"
        "s_waitcnt lgkmcnt(2)\n\t"
        "v_mfma_f32_32x32x8_bf16 v[80:95], v[148:149], v[40:41], 0\n\t"
        "s_mov_b32 m0, s57\n\t"
        "s_add_u32 s57, s57, 20736\n\t"
        "buffer_load_dwordx4 v150, s[40:43], s56 offen sc1 lds\n\t"
        "v_exp_f32_e32 v128, v128\n\t"
        "v_exp_f32_e32 v129, v129\n\t"
        "v_exp_f32_e32 v130, v130\n\t"
        "v_exp_f32_e32 v131, v131\n\t"
        "v_mfma_f32_32x32x16_bf16 v[80:95], v[144:147], v[32:35], v[80:95]\n\t"
        "buffer_load_dwordx4 v150, s[40:43], s56 offen offset:1024 sc1 lds\n\t"
        "s_cmp_ge_u32 s57, s58\n\t"
        "s_cselect_b32 s92, 46656, 0\n\t"
        "v_exp_f32_e32 v132, v132\n\t"
        "v_exp_f32_e32 v133, v133\n\t"
        "v_exp_f32_e32 v134, v134\n\t"
        "v_exp_f32_e32 v135, v135\n\t"
        "s_waitcnt lgkmcnt(0)\n\t"
        "v_smfmac_f32_32x32x32_bf16 v[48:63], v[152:155], v[112:119], v44\n\t"
        "s_mov_b32 exec_hi, 0\n\t"
        "buffer_load_dwordx4 v150, s[40:43], s56 offen offset:2048 sc1 lds\n\t"
        "s_mov_b32 exec_hi, -1\n\t"
        "s_sub_u32 s57, s57, s92\n\t"
        "s_add_u32 s56, s56, 20480\n\t"
        "v_exp_f32_e32 v136, v136\n\t"
        "v_exp_f32_e32 v137, v137\n\t"
        "v_exp_f32_e32 v138, v138\n\t"
        "v_exp_f32_e32 v139, v139\n\t"
        "v_smfmac_f32_32x32x32_bf16 v[48:63], v[156:159], v[120:127], v44\n\t"
        "s_mov_b64 exec, 15\n\t"
        "buffer_load_dword v26, v23, s[44:47], s80 offen\n\t"
        "buffer_load_dword v26, v23, s[48:51], s80 offen\n\t"
        "s_mov_b64 exec, -1\n\t"
        "v_exp_f32_e32 v140, v140\n\t"
        "v_exp_f32_e32 v141, v141\n\t"
        "v_exp_f32_e32 v142, v142\n\t"
        "v_exp_f32_e32 v143, v143\n\t"
        "v_mfma_f32_32x32x8_bf16 v[96:111], v[148:149], v[42:43], 0\n\t"
        "ds_read_b64 v[148:149], v46 offset:25920\n\t"
        "v_exp_f32_e32 v80, v80\n\t"
        "v_exp_f32_e32 v81, v81\n\t"
        "v_exp_f32_e32 v82, v82\n\t"
        "v_exp_f32_e32 v83, v83\n\t"
        "v_mfma_f32_32x32x16_bf16 v[96:111], v[144:147], v[36:39], v[96:111]\n\t"
        "ds_read_b64 v[144:145], v45 offset:25920\n\t"
        "ds_read_b64 v[146:147], v45 offset:26176\n\t"
        "v_exp_f32_e32 v84, v84\n\t"
        "v_exp_f32_e32 v85, v85\n\t"
        "v_exp_f32_e32 v86, v86\n\t"
        "v_exp_f32_e32 v87, v87\n\t"
        "v_smfmac_f32_32x32x32_bf16 v[64:79], v[152:155], v[128:135], v44\n\t"
        "ds_read_b128 v[152:155], v47 offset:20736\n\t"
        "v_exp_f32_e32 v88, v88\n\t"
        "v_exp_f32_e32 v89, v89\n\t"
        "v_exp_f32_e32 v90, v90\n\t"
        "v_exp_f32_e32 v91, v91\n\t"
        "v_smfmac_f32_32x32x32_bf16 v[64:79], v[156:159], v[136:143], v44\n\t"
        "ds_read_b128 v[156:159], v31 offset:20736\n\t"
        "v_exp_f32_e32 v92, v92\n\t"
        "v_exp_f32_e32 v93, v93\n\t"
        "v_exp_f32_e32 v94, v94\n\t"
        "v_exp_f32_e32 v95, v95\n\t"
        "s_setprio 2\n\t"
        "s_waitcnt lgkmcnt(2)\n\t"
        "v_mfma_f32_32x32x8_bf16 v[112:127], v[148:149], v[40:41], 0\n\t"
        "v_exp_f32_e32 v96, v96\n\t"
        "v_exp_f32_e32 v97, v97\n\t"
        "v_exp_f32_e32 v98, v98\n\t"
        "v_exp_f32_e32 v99, v99\n\t"
        "v_mfma_f32_32x32x16_bf16 v[112:127], v[144:147], v[32:35], v[112:127]\n\t"
        "v_exp_f32_e32 v100, v100\n\t"
        "v_exp_f32_e32 v101, v101\n\t"
        "v_exp_f32_e32 v102, v102\n\t"
        "v_exp_f32_e32 v103, v103\n\t"
        "s_waitcnt lgkmcnt(0)\n\t"
        "v_smfmac_f32_32x32x32_bf16 v[48:63], v[152:155], v[80:87], v44\n\t"
        "v_exp_f32_e32 v104, v104\n\t"
        "v_exp_f32_e32 v105, v105\n\t"
        "v_exp_f32_e32 v106, v106\n\t"
        "v_exp_f32_e32 v107, v107\n\t"
        "v_smfmac_f32_32x32x32_bf16 v[48:63], v[156:159], v[88:95], v44\n\t"
        "v_exp_f32_e32 v108, v108\n\t"
        "v_exp_f32_e32 v109, v109\n\t"
        "v_exp_f32_e32 v110, v110\n\t"
        "v_exp_f32_e32 v111, v111\n\t"
        "v_mfma_f32_32x32x8_bf16 v[128:143], v[148:149], v[42:43], 0\n\t"
        "ds_read_b64 v[148:149], v46 offset:31104\n\t"
        "v_exp_f32_e32 v112, v112\n\t"
        "v_exp_f32_e32 v113, v113\n\t"
        "v_exp_f32_e32 v114, v114\n\t"
        "v_exp_f32_e32 v115, v115\n\t"
        "v_mfma_f32_32x32x16_bf16 v[128:143], v[144:147], v[36:39], v[128:143]\n\t"
        "ds_read_b64 v[144:145], v45 offset:31104\n\t"
        "ds_read_b64 v[146:147], v45 offset:31360\n\t"
        "v_exp_f32_e32 v116, v116\n\t"
        "v_exp_f32_e32 v117, v117\n\t"
        "v_exp_f32_e32 v118, v118\n\t"
        "v_exp_f32_e32 v119, v119\n\t"
        "v_smfmac_f32_32x32x32_bf16 v[64:79], v[152:155], v[96:103], v44\n\t"
        "ds_read_b128 v[152:155], v47 offset:25920\n\t"
        "v_exp_f32_e32 v120, v120\n\t"
        "v_exp_f32_e32 v121, v121\n\t"
        "v_exp_f32_e32 v122, v122\n\t"
        "v_exp_f32_e32 v123, v123\n\t"
        "v_smfmac_f32_32x32x32_bf16 v[64:79], v[156:159], v[104:111], v44\n\t"
        "ds_read_b128 v[156:159], v31 offset:25920\n\t"
        "v_exp_f32_e32 v124, v124\n\t"
        "v_exp_f32_e32 v125, v125\n\t"
        "v_exp_f32_e32 v126, v126\n\t"
        "v_exp_f32_e32 v127, v127\n\t"
        "s_waitcnt lgkmcnt(2)\n\t"
        "v_mfma_f32_32x32x8_bf16 v[80:95], v[148:149], v[40:41], 0\n\t"
        "s_add_u32 m0, s75, 112640\n\t"
        "s_add_u32 s93, s74, 409600\n\t"
        "buffer_load_dwordx4 v0, s[44:47], s93 offen lds\n\t"
        "v_exp_f32_e32 v128, v128\n\t"
        "v_exp_f32_e32 v129, v129\n\t"
        "v_exp_f32_e32 v130, v130\n\t"
        "v_exp_f32_e32 v131, v131\n\t"
        "v_mfma_f32_32x32x16_bf16 v[80:95], v[144:147], v[32:35], v[80:95]\n\t"
        "buffer_load_dwordx4 v1, s[44:47], s93 offen offset:1024 lds\n\t"
        "v_exp_f32_e32 v132, v132\n\t"
        "v_exp_f32_e32 v133, v133\n\t"
        "v_exp_f32_e32 v134, v134\n\t"
        "v_exp_f32_e32 v135, v135\n\t"
        "s_waitcnt lgkmcnt(0)\n\t"
        "v_smfmac_f32_32x32x32_bf16 v[48:63], v[152:155], v[112:119], v44\n\t"
        "buffer_load_dwordx4 v2, s[44:47], s93 offen offset:2048 lds\n\t"
        "v_exp_f32_e32 v136, v136\n\t"
        "v_exp_f32_e32 v137, v137\n\t"
        "v_exp_f32_e32 v138, v138\n\t"
        "v_exp_f32_e32 v139, v139\n\t"
        "v_smfmac_f32_32x32x32_bf16 v[48:63], v[156:159], v[120:127], v44\n\t"
        "buffer_load_dwordx4 v3, s[44:47], s93 offen offset:3072 lds\n\t"
        "v_exp_f32_e32 v140, v140\n\t"
        "v_exp_f32_e32 v141, v141\n\t"
        "v_exp_f32_e32 v142, v142\n\t"
        "v_exp_f32_e32 v143, v143\n\t"
        "s_setprio 1\n\t"
        "v_mfma_f32_32x32x8_bf16 v[96:111], v[148:149], v[42:43], 0\n\t"
        "ds_read_b64 v[148:149], v46 offset:36288\n\t"
        "v_exp_f32_e32 v80, v80\n\t"
        "v_exp_f32_e32 v81, v81\n\t"
        "v_exp_f32_e32 v82, v82\n\t"
        "v_exp_f32_e32 v83, v83\n\t"
        "v_mfma_f32_32x32x16_bf16 v[96:111], v[144:147], v[36:39], v[96:111]\n\t"
        "ds_read_b64 v[144:145], v45 offset:36288\n\t"
        "ds_read_b64 v[146:147], v45 offset:36544\n\t"
        "v_exp_f32_e32 v84, v84\n\t"
        "v_exp_f32_e32 v85, v85\n\t"
        "v_exp_f32_e32 v86, v86\n\t"
        "v_exp_f32_e32 v87, v87\n\t"
        "v_smfmac_f32_32x32x32_bf16 v[64:79], v[152:155], v[128:135], v44\n\t"
        "ds_read_b128 v[152:155], v47 offset:31104\n\t"
        "v_exp_f32_e32 v88, v88\n\t"
        "v_exp_f32_e32 v89, v89\n\t"
        "v_exp_f32_e32 v90, v90\n\t"
        "v_exp_f32_e32 v91, v91\n\t"
        "v_smfmac_f32_32x32x32_bf16 v[64:79], v[156:159], v[136:143], v44\n\t"
        "ds_read_b128 v[156:159], v31 offset:31104\n\t"
        "v_exp_f32_e32 v92, v92\n\t"
        "v_exp_f32_e32 v93, v93\n\t"
        "v_exp_f32_e32 v94, v94\n\t"
        "v_exp_f32_e32 v95, v95\n\t"
        "s_setprio 0\n\t"
        "s_waitcnt lgkmcnt(2)\n\t"
        "v_mfma_f32_32x32x8_bf16 v[112:127], v[148:149], v[40:41], 0\n\t"
        "s_add_u32 m0, s75, 116736\n\t"
        "s_add_u32 s93, s74, 409600\n\t"
        "buffer_load_dwordx4 v4, s[44:47], s93 offen lds\n\t"
        "v_exp_f32_e32 v96, v96\n\t"
        "v_exp_f32_e32 v97, v97\n\t"
        "v_exp_f32_e32 v98, v98\n\t"
        "v_exp_f32_e32 v99, v99\n\t"
        "v_mfma_f32_32x32x16_bf16 v[112:127], v[144:147], v[32:35], v[112:127]\n\t"
        "buffer_load_dwordx4 v5, s[44:47], s93 offen offset:1024 lds\n\t"
        "v_exp_f32_e32 v100, v100\n\t"
        "v_exp_f32_e32 v101, v101\n\t"
        "v_exp_f32_e32 v102, v102\n\t"
        "v_exp_f32_e32 v103, v103\n\t"
        "s_waitcnt lgkmcnt(0)\n\t"
        "v_smfmac_f32_32x32x32_bf16 v[48:63], v[152:155], v[80:87], v44\n\t"
        "s_mov_b64 exec, 0xffff\n\t"
        "buffer_load_dwordx4 v6, s[44:47], s93 offen offset:2048 lds\n\t"
        "s_mov_b64 exec, -1\n\t"
        "v_exp_f32_e32 v104, v104\n\t"
        "v_exp_f32_e32 v105, v105\n\t"
        "v_exp_f32_e32 v106, v106\n\t"
        "v_exp_f32_e32 v107, v107\n\t"
        "v_smfmac_f32_32x32x32_bf16 v[48:63], v[156:159], v[88:95], v44\n\t"
        "v_exp_f32_e32 v108, v108\n\t"
        "v_exp_f32_e32 v109, v109\n\t"
        "v_exp_f32_e32 v110, v110\n\t"
        "v_exp_f32_e32 v111, v111\n\t"
        "s_waitcnt vmcnt(7)\n\t"
        "s_barrier\n\t"
        "s_setprio 3\n\t"
        "v_mfma_f32_32x32x8_bf16 v[128:143], v[148:149], v[42:43], 0\n\t"
        "ds_read_b64 v[148:149], v46 offset:41472\n\t"
        "s_cmp_lg_u32 s78, 0\n\t"
        "s_cbranch_scc1 .Lf15\n\t"
        "s_mov_b32 exec_hi, 0\n\t"
        "buffer_store_dword v24, v22, s[40:43], s60 offen sc1\n\t"
        "s_mov_b32 exec_hi, -1\n\t"
        ".Lf15:\n\t"
        "v_exp_f32_e32 v112, v112\n\t"
        "v_exp_f32_e32 v113, v113\n\t"
        "v_exp_f32_e32 v114, v114\n\t"
        "v_exp_f32_e32 v115, v115\n\t"
        "v_mfma_f32_32x32x16_bf16 v[128:143], v[144:147], v[36:39], v[128:143]\n\t"
        "ds_read_b64 v[144:145], v45 offset:41472\n\t"
        "ds_read_b64 v[146:147], v45 offset:41728\n\t"
        "v_exp_f32_e32 v116, v116\n\t"
        "v_exp_f32_e32 v117, v117\n\t"
        "v_exp_f32_e32 v118, v118\n\t"
        "v_exp_f32_e32 v119, v119\n\t"
        "v_smfmac_f32_32x32x32_bf16 v[64:79], v[152:155], v[96:103], v44\n\t"
        "ds_read_b128 v[152:155], v47 offset:36288\n\t"
        "v_exp_f32_e32 v120, v120\n\t"
        "v_exp_f32_e32 v121, v121\n\t"
        "v_exp_f32_e32 v122, v122\n\t"
        "v_exp_f32_e32 v123, v123\n\t"
        "v_smfmac_f32_32x32x32_bf16 v[64:79], v[156:159], v[104:111], v44\n\t"
        "ds_read_b128 v[156:159], v31 offset:36288\n\t"
        "v_exp_f32_e32 v124, v124\n\t"
        "v_exp_f32_e32 v125, v125\n\t"
        "v_exp_f32_e32 v126, v126\n\t"
        "v_exp_f32_e32 v127, v127\n\t"
        "s_waitcnt lgkmcnt(2)\n\t"
        "v_mfma_f32_32x32x8_bf16 v[80:95], v[148:149], v[40:41], 0\n\t"
        "s_mov_b32 m0, s57\n\t"
        "s_add_u32 s57, s57, 20736\n\t"
        "buffer_load_dwordx4 v150, s[40:43], s56 offen sc1 lds\n\t"
        "v_exp_f32_e32 v128, v128\n\t"
        "v_exp_f32_e32 v129, v129\n\t"
        "v_exp_f32_e32 v130, v130\n\t"
        "v_exp_f32_e32 v131, v131\n\t"
        "v_mfma_f32_32x32x16_bf16 v[80:95], v[144:147], v[32:35], v[80:95]\n\t"
        "buffer_load_dwordx4 v150, s[40:43], s56 offen offset:1024 sc1 lds\n\t"
        "s_cmp_ge_u32 s57, s58\n\t"
        "s_cselect_b32 s92, 46656, 0\n\t"
        "v_exp_f32_e32 v132, v132\n\t"
        "v_exp_f32_e32 v133, v133\n\t"
        "v_exp_f32_e32 v134, v134\n\t"
        "v_exp_f32_e32 v135, v135\n\t"
        "s_waitcnt lgkmcnt(0)\n\t"
        "v_smfmac_f32_32x32x32_bf16 v[48:63], v[152:155], v[112:119], v44\n\t"
        "s_mov_b32 exec_hi, 0\n\t"
        "buffer_load_dwordx4 v150, s[40:43], s56 offen offset:2048 sc1 lds\n\t"
        "s_mov_b32 exec_hi, -1\n\t"
        "s_sub_u32 s57, s57, s92\n\t"
        "s_add_u32 s56, s56, 20480\n\t"
        "v_exp_f32_e32 v136, v136\n\t"
        "v_exp_f32_e32 v137, v137\n\t"
        "v_exp_f32_e32 v138, v138\n\t"
        "v_exp_f32_e32 v139, v139\n\t"
        "v_smfmac_f32_32x32x32_bf16 v[48:63], v[156:159], v[120:127], v44\n\t"
        "v_exp_f32_e32 v140, v140\n\t"
        "v_exp_f32_e32 v141, v141\n\t"
        "v_exp_f32_e32 v142, v142\n\t"
        "v_exp_f32_e32 v143, v143\n\t"
        "v_mfma_f32_32x32x8_bf16 v[96:111], v[148:149], v[42:43], 0\n\t"
        "ds_read_b64 v[148:149], v46 offset:0\n\t"
        "v_exp_f32_e32 v80, v80\n\t"
        "v_exp_f32_e32 v81, v81\n\t"
        "v_exp_f32_e32 v82, v82\n\t"
        "v_exp_f32_e32 v83, v83\n\t"
        "v_mfma_f32_32x32x16_bf16 v[96:111], v[144:147], v[36:39], v[96:111]\n\t"
        "ds_read_b64 v[144:145], v45 offset:0\n\t"
        "ds_read_b64 v[146:147], v45 offset:256\n\t"
        "v_exp_f32_e32 v84, v84\n\t"
        "v_exp_f32_e32 v85, v85\n\t"
        "v_exp_f32_e32 v86, v86\n\t"
        "v_exp_f32_e32 v87, v87\n\t"
        "v_smfmac_f32_32x32x32_bf16 v[64:79], v[152:155], v[128:135], v44\n\t"
        "ds_read_b128 v[152:155], v47 offset:41472\n\t"
        "v_exp_f32_e32 v88, v88\n\t"
        "v_exp_f32_e32 v89, v89\n\t"
        "v_exp_f32_e32 v90, v90\n\t"
        "v_exp_f32_e32 v91, v91\n\t"
        "v_smfmac_f32_32x32x32_bf16 v[64:79], v[156:159], v[136:143], v44\n\t"
        "ds_read_b128 v[156:159], v31 offset:41472\n\t"
        "v_exp_f32_e32 v92, v92\n\t"
        "v_exp_f32_e32 v93, v93\n\t"
        "v_exp_f32_e32 v94, v94\n\t"
        "v_exp_f32_e32 v95, v95\n\t"
        "s_setprio 2\n\t"
        "s_waitcnt lgkmcnt(2)\n\t"
        "v_mfma_f32_32x32x8_bf16 v[112:127], v[148:149], v[40:41], 0\n\t"
        "v_exp_f32_e32 v96, v96\n\t"
        "v_exp_f32_e32 v97, v97\n\t"
        "v_exp_f32_e32 v98, v98\n\t"
        "v_exp_f32_e32 v99, v99\n\t"
        "v_mfma_f32_32x32x16_bf16 v[112:127], v[144:147], v[32:35], v[112:127]\n\t"
        "v_exp_f32_e32 v100, v100\n\t"
        "v_exp_f32_e32 v101, v101\n\t"
        "v_exp_f32_e32 v102, v102\n\t"
        "v_exp_f32_e32 v103, v103\n\t"
        "s_waitcnt lgkmcnt(0)\n\t"
        "v_smfmac_f32_32x32x32_bf16 v[48:63], v[152:155], v[80:87], v44\n\t"
        "v_exp_f32_e32 v104, v104\n\t"
        "v_exp_f32_e32 v105, v105\n\t"
        "v_exp_f32_e32 v106, v106\n\t"
        "v_exp_f32_e32 v107, v107\n\t"
        "v_smfmac_f32_32x32x32_bf16 v[48:63], v[156:159], v[88:95], v44\n\t"
        "v_exp_f32_e32 v108, v108\n\t"
        "v_exp_f32_e32 v109, v109\n\t"
        "v_exp_f32_e32 v110, v110\n\t"
        "v_exp_f32_e32 v111, v111\n\t"
        "v_mfma_f32_32x32x8_bf16 v[128:143], v[148:149], v[42:43], 0\n\t"
        "ds_read_b64 v[148:149], v46 offset:5184\n\t"
        "v_exp_f32_e32 v112, v112\n\t"
        "v_exp_f32_e32 v113, v113\n\t"
        "v_exp_f32_e32 v114, v114\n\t"
        "v_exp_f32_e32 v115, v115\n\t"
        "v_mfma_f32_32x32x16_bf16 v[128:143], v[144:147], v[36:39], v[128:143]\n\t"
        "ds_read_b64 v[144:145], v45 offset:5184\n\t"
        "ds_read_b64 v[146:147], v45 offset:5440\n\t"
        "v_exp_f32_e32 v116, v116\n\t"
        "v_exp_f32_e32 v117, v117\n\t"
        "v_exp_f32_e32 v118, v118\n\t"
        "v_exp_f32_e32 v119, v119\n\t"
        "v_smfmac_f32_32x32x32_bf16 v[64:79], v[152:155], v[96:103], v44\n\t"
        "ds_read_b128 v[152:155], v47 offset:0\n\t"
        "v_exp_f32_e32 v120, v120\n\t"
        "v_exp_f32_e32 v121, v121\n\t"
        "v_exp_f32_e32 v122, v122\n\t"
        "v_exp_f32_e32 v123, v123\n\t"
        "v_smfmac_f32_32x32x32_bf16 v[64:79], v[156:159], v[104:111], v44\n\t"
        "ds_read_b128 v[156:159], v31 offset:0\n\t"
        "v_exp_f32_e32 v124, v124\n\t"
        "v_exp_f32_e32 v125, v125\n\t"
        "v_exp_f32_e32 v126, v126\n\t"
        "v_exp_f32_e32 v127, v127\n\t"
        "s_waitcnt lgkmcnt(2)\n\t"
        "v_mfma_f32_32x32x8_bf16 v[80:95], v[148:149], v[40:41], 0\n\t"
        "buffer_load_dword v25, v7, s[40:43], 0 offen sc1\n\t"
        "v_exp_f32_e32 v128, v128\n\t"
        "v_exp_f32_e32 v129, v129\n\t"
        "v_exp_f32_e32 v130, v130\n\t"
        "v_exp_f32_e32 v131, v131\n\t"
        "v_mfma_f32_32x32x16_bf16 v[80:95], v[144:147], v[32:35], v[80:95]\n\t"
        "v_exp_f32_e32 v132, v132\n\t"
        "v_exp_f32_e32 v133, v133\n\t"
        "v_exp_f32_e32 v134, v134\n\t"
        "v_exp_f32_e32 v135, v135\n\t"
        "s_waitcnt lgkmcnt(0)\n\t"
        "v_smfmac_f32_32x32x32_bf16 v[48:63], v[152:155], v[112:119], v44\n\t"
        "v_exp_f32_e32 v136, v136\n\t"
        "v_exp_f32_e32 v137, v137\n\t"
        "v_exp_f32_e32 v138, v138\n\t"
        "v_exp_f32_e32 v139, v139\n\t"
        "v_smfmac_f32_32x32x32_bf16 v[48:63], v[156:159], v[120:127], v44\n\t"
        "v_exp_f32_e32 v140, v140\n\t"
        "v_exp_f32_e32 v141, v141\n\t"
        "v_exp_f32_e32 v142, v142\n\t"
        "v_exp_f32_e32 v143, v143\n\t"
        "s_setprio 1\n\t"
        "v_mfma_f32_32x32x8_bf16 v[96:111], v[148:149], v[42:43], 0\n\t"
        "ds_read_b64 v[148:149], v46 offset:10368\n\t"
        "v_exp_f32_e32 v80, v80\n\t"
        "v_exp_f32_e32 v81, v81\n\t"
        "v_exp_f32_e32 v82, v82\n\t"
        "v_exp_f32_e32 v83, v83\n\t"
        "v_mfma_f32_32x32x16_bf16 v[96:111], v[144:147], v[36:39], v[96:111]\n\t"
        "ds_read_b64 v[144:145], v45 offset:10368\n\t"
        "ds_read_b64 v[146:147], v45 offset:10624\n\t"
        "v_exp_f32_e32 v84, v84\n\t"
        "v_exp_f32_e32 v85, v85\n\t"
        "v_exp_f32_e32 v86, v86\n\t"
        "v_exp_f32_e32 v87, v87\n\t"
        "v_smfmac_f32_32x32x32_bf16 v[64:79], v[152:155], v[128:135], v44\n\t"
        "ds_read_b128 v[152:155], v47 offset:5184\n\t"
        "v_exp_f32_e32 v88, v88\n\t"
        "v_exp_f32_e32 v89, v89\n\t"
        "v_exp_f32_e32 v90, v90\n\t"
        "v_exp_f32_e32 v91, v91\n\t"
        "v_smfmac_f32_32x32x32_bf16 v[64:79], v[156:159], v[136:143], v44\n\t"
        "ds_read_b128 v[156:159], v31 offset:5184\n\t"
        "v_exp_f32_e32 v92, v92\n\t"
        "v_exp_f32_e32 v93, v93\n\t"
        "v_exp_f32_e32 v94, v94\n\t"
        "v_exp_f32_e32 v95, v95\n\t"
        "s_waitcnt vmcnt(0)\n\t"
        "v_cmp_ne_u32_e32 vcc, v25, v24\n\t"
        "s_cbranch_vccz .Lq16\n\t"
        "s_mov_b32 s94, 0x80000\n\t"
        ".Lp16:\n\t"
        "s_sleep 1\n\t"
        "buffer_load_dword v25, v7, s[40:43], 0 offen sc1\n\t"
        "s_waitcnt vmcnt(0)\n\t"
        "v_cmp_ne_u32_e32 vcc, v25, v24\n\t"
        "s_sub_u32 s94, s94, 1\n\t"
        "s_cbranch_scc1 .Lq16\n\t"
        "s_cbranch_vccnz .Lp16\n\t"
        ".Lq16:\n\t"
        "s_setprio 0\n\t"
        "s_waitcnt lgkmcnt(2)\n\t"
        "v_mfma_f32_32x32x8_bf16 v[112:127], v[148:149], v[40:41], 0\n\t"
        "buffer_load_dwordx2 v[160:161], v16, s[40:43], s63 offen sc1\n\t"
        "v_exp_f32_e32 v96, v96\n\t"
        "v_exp_f32_e32 v97, v97\n\t"
        "v_exp_f32_e32 v98, v98\n\t"
        "v_exp_f32_e32 v99, v99\n\t"
        "v_mfma_f32_32x32x16_bf16 v[112:127], v[144:147], v[32:35], v[112:127]\n\t"
        "buffer_load_dwordx2 v[162:163], v16, s[40:43], s63 offen offset:256 sc1\n\t"
        "v_exp_f32_e32 v100, v100\n\t"
        "v_exp_f32_e32 v101, v101\n\t"
        "v_exp_f32_e32 v102, v102\n\t"
        "v_exp_f32_e32 v103, v103\n\t"
        "s_waitcnt lgkmcnt(0)\n\t"
        "v_smfmac_f32_32x32x32_bf16 v[48:63], v[152:155], v[80:87], v44\n\t"
        "buffer_load_dwordx2 v[164:165], v17, s[40:43], s63 offen sc1\n\t"
        "v_exp_f32_e32 v104, v104\n\t"
        "v_exp_f32_e32 v105, v105\n\t"
        "v_exp_f32_e32 v106, v106\n\t"
        "v_exp_f32_e32 v107, v107\n\t"
        "v_smfmac_f32_32x32x32_bf16 v[48:63], v[156:159], v[88:95], v44\n\t"
        "buffer_load_dwordx2 v[168:169], v16, s[40:43], s64 offen sc1\n\t"
        "v_exp_f32_e32 v108, v108\n\t"
        "v_exp_f32_e32 v109, v109\n\t"
        "v_exp_f32_e32 v110, v110\n\t"
        "v_exp_f32_e32 v111, v111\n\t"
        "s_barrier\n\t"
        "s_setprio 3\n\t"
        "v_mfma_f32_32x32x8_bf16 v[128:143], v[148:149], v[42:43], 0\n\t"
        "ds_read_b64 v[148:149], v46 offset:15552\n\t"
        "v_exp_f32_e32 v112, v112\n\t"
        "v_exp_f32_e32 v113, v113\n\t"
        "v_exp_f32_e32 v114, v114\n\t"
        "v_exp_f32_e32 v115, v115\n\t"
        "v_mfma_f32_32x32x16_bf16 v[128:143], v[144:147], v[36:39], v[128:143]\n\t"
        "ds_read_b64 v[144:145], v45 offset:15552\n\t"
        "ds_read_b64 v[146:147], v45 offset:15808\n\t"
        "v_exp_f32_e32 v116, v116\n\t"
        "v_exp_f32_e32 v117, v117\n\t"
        "v_exp_f32_e32 v118, v118\n\t"
        "v_exp_f32_e32 v119, v119\n\t"
        "v_smfmac_f32_32x32x32_bf16 v[64:79], v[152:155], v[96:103], v44\n\t"
        "ds_read_b128 v[152:155], v47 offset:10368\n\t"
        "v_exp_f32_e32 v120, v120\n\t"
        "v_exp_f32_e32 v121, v121\n\t"
        "v_exp_f32_e32 v122, v122\n\t"
        "v_exp_f32_e32 v123, v123\n\t"
        "v_smfmac_f32_32x32x32_bf16 v[64:79], v[156:159], v[104:111], v44\n\t"
        "ds_read_b128 v[156:159], v31 offset:10368\n\t"
        "v_exp_f32_e32 v124, v124\n\t"
        "v_exp_f32_e32 v125, v125\n\t"
        "v_exp_f32_e32 v126, v126\n\t"
        "v_exp_f32_e32 v127, v127\n\t"
        "s_waitcnt lgkmcnt(2)\n\t"
        "v_mfma_f32_32x32x8_bf16 v[80:95], v[148:149], v[40:41], 0\n\t"
        "s_mov_b32 m0, s57\n\t"
        "s_add_u32 s57, s57, 20736\n\t"
        "buffer_load_dwordx4 v150, s[40:43], s56 offen sc1 lds\n\t"
        "v_exp_f32_e32 v128, v128\n\t"
        "v_exp_f32_e32 v129, v129\n\t"
        "v_exp_f32_e32 v130, v130\n\t"
        "v_exp_f32_e32 v131, v131\n\t"
        "v_mfma_f32_32x32x16_bf16 v[80:95], v[144:147], v[32:35], v[80:95]\n\t"
        "buffer_load_dwordx4 v150, s[40:43], s56 offen offset:1024 sc1 lds\n\t"
        "s_cmp_ge_u32 s57, s58\n\t"
        "s_cselect_b32 s92, 46656, 0\n\t"
        "v_exp_f32_e32 v132, v132\n\t"
        "v_exp_f32_e32 v133, v133\n\t"
        "v_exp_f32_e32 v134, v134\n\t"
        "v_exp_f32_e32 v135, v135\n\t"
        "s_waitcnt lgkmcnt(0)\n\t"
        "v_smfmac_f32_32x32x32_bf16 v[48:63], v[152:155], v[112:119], v44\n\t"
        "s_mov_b32 exec_hi, 0\n\t"
        "buffer_load_dwordx4 v150, s[40:43], s56 offen offset:2048 sc1 lds\n\t"
        "s_mov_b32 exec_hi, -1\n\t"
        "s_sub_u32 s57, s57, s92\n\t"
        "s_add_u32 s56, s56, 20480\n\t"
        "v_exp_f32_e32 v136, v136\n\t"
        "v_exp_f32_e32 v137, v137\n\t"
        "v_exp_f32_e32 v138, v138\n\t"
        "v_exp_f32_e32 v139, v139\n\t"
        "v_smfmac_f32_32x32x32_bf16 v[48:63], v[156:159], v[120:127], v44\n\t"
        "v_exp_f32_e32 v140, v140\n\t"
        "v_exp_f32_e32 v141, v141\n\t"
        "v_exp_f32_e32 v142, v142\n\t"
        "v_exp_f32_e32 v143, v143\n\t"
        "v_mfma_f32_32x32x8_bf16 v[96:111], v[148:149], v[42:43], 0\n\t"
        "ds_read_b64 v[148:149], v46 offset:20736\n\t"
        "v_exp_f32_e32 v80, v80\n\t"
        "v_exp_f32_e32 v81, v81\n\t"
        "v_exp_f32_e32 v82, v82\n\t"
        "v_exp_f32_e32 v83, v83\n\t"
        "v_mfma_f32_32x32x16_bf16 v[96:111], v[144:147], v[36:39], v[96:111]\n\t"
        "ds_read_b64 v[144:145], v45 offset:20736\n\t"
        "ds_read_b64 v[146:147], v45 offset:20992\n\t"
        "v_exp_f32_e32 v84, v84\n\t"
        "v_exp_f32_e32 v85, v85\n\t"
        "v_exp_f32_e32 v86, v86\n\t"
        "v_exp_f32_e32 v87, v87\n\t"
        "v_smfmac_f32_32x32x32_bf16 v[64:79], v[152:155], v[128:135], v44\n\t"
        "ds_read_b128 v[152:155], v47 offset:15552\n\t"
        "v_exp_f32_e32 v88, v88\n\t"
        "v_exp_f32_e32 v89, v89\n\t"
        "v_exp_f32_e32 v90, v90\n\t"
        "v_exp_f32_e32 v91, v91\n\t"
        "v_smfmac_f32_32x32x32_bf16 v[64:79], v[156:159], v[136:143], v44\n\t"
        "ds_read_b128 v[156:159], v31 offset:15552\n\t"
        "v_exp_f32_e32 v92, v92\n\t"
        "v_exp_f32_e32 v93, v93\n\t"
        "v_exp_f32_e32 v94, v94\n\t"
        "v_exp_f32_e32 v95, v95\n\t"
        "s_setprio 2\n\t"
        "s_waitcnt lgkmcnt(2)\n\t"
        "v_mfma_f32_32x32x8_bf16 v[112:127], v[148:149], v[40:41], 0\n\t"
        "buffer_load_dwordx2 v[170:171], v16, s[40:43], s64 offen offset:256 sc1\n\t"
        "v_exp_f32_e32 v96, v96\n\t"
        "v_exp_f32_e32 v97, v97\n\t"
        "v_exp_f32_e32 v98, v98\n\t"
        "v_exp_f32_e32 v99, v99\n\t"
        "v_mfma_f32_32x32x16_bf16 v[112:127], v[144:147], v[32:35], v[112:127]\n\t"
        "buffer_load_dwordx2 v[166:167], v17, s[40:43], s64 offen sc1\n\t"
        "v_exp_f32_e32 v100, v100\n\t"
        "v_exp_f32_e32 v101, v101\n\t"
        "v_exp_f32_e32 v102, v102\n\t"
        "v_exp_f32_e32 v103, v103\n\t"
        "s_waitcnt lgkmcnt(0)\n\t"
        "v_smfmac_f32_32x32x32_bf16 v[48:63], v[152:155], v[80:87], v44\n\t"
        "buffer_load_dwordx4 v[180:183], v10, s[40:43], s64 offen sc1\n\t"
        "v_exp_f32_e32 v104, v104\n\t"
        "v_exp_f32_e32 v105, v105\n\t"
        "v_exp_f32_e32 v106, v106\n\t"
        "v_exp_f32_e32 v107, v107\n\t"
        "v_smfmac_f32_32x32x32_bf16 v[48:63], v[156:159], v[88:95], v44\n\t"
        "buffer_load_dwordx4 v[184:187], v10, s[40:43], s64 offen offset:640 sc1\n\t"
        "v_exp_f32_e32 v108, v108\n\t"
        "v_exp_f32_e32 v109, v109\n\t"
        "v_exp_f32_e32 v110, v110\n\t"
        "v_exp_f32_e32 v111, v111\n\t"
        "v_mfma_f32_32x32x8_bf16 v[128:143], v[148:149], v[42:43], 0\n\t"
        "ds_read_b64 v[148:149], v46 offset:25920\n\t"
        "v_exp_f32_e32 v112, v112\n\t"
        "v_exp_f32_e32 v113, v113\n\t"
        "v_exp_f32_e32 v114, v114\n\t"
        "v_exp_f32_e32 v115, v115\n\t"
        "v_mfma_f32_32x32x16_bf16 v[128:143], v[144:147], v[36:39], v[128:143]\n\t"
        "ds_read_b64 v[144:145], v45 offset:25920\n\t"
        "ds_read_b64 v[146:147], v45 offset:26176\n\t"
        "v_exp_f32_e32 v116, v116\n\t"
        "v_exp_f32_e32 v117, v117\n\t"
        "v_exp_f32_e32 v118, v118\n\t"
        "v_exp_f32_e32 v119, v119\n\t"
        "v_smfmac_f32_32x32x32_bf16 v[64:79], v[152:155], v[96:103], v44\n\t"
        "ds_read_b128 v[152:155], v47 offset:20736\n\t"
        "v_exp_f32_e32 v120, v120\n\t"
        "v_exp_f32_e32 v121, v121\n\t"
        "v_exp_f32_e32 v122, v122\n\t"
        "v_exp_f32_e32 v123, v123\n\t"
        "v_smfmac_f32_32x32x32_bf16 v[64:79], v[156:159], v[104:111], v44\n\t"
        "ds_read_b128 v[156:159], v31 offset:20736\n\t"
        "v_exp_f32_e32 v124, v124\n\t"
        "v_exp_f32_e32 v125, v125\n\t"
        "v_exp_f32_e32 v126, v126\n\t"
        "v_exp_f32_e32 v127, v127\n\t"
        "s_waitcnt lgkmcnt(2)\n\t"
        "v_mfma_f32_32x32x8_bf16 v[80:95], v[148:149], v[40:41], 0\n\t"
        "buffer_load_dwordx2 v[172:173], v16, s[40:43], s65 offen sc1\n\t"
        "v_exp_f32_e32 v128, v128\n\t"
        "v_exp_f32_e32 v129, v129\n\t"
        "v_exp_f32_e32 v130, v130\n\t"
        "v_exp_f32_e32 v131, v131\n\t"
        "v_mfma_f32_32x32x16_bf16 v[80:95], v[144:147], v[32:35], v[80:95]\n\t"
        "buffer_load_dwordx2 v[174:175], v16, s[40:43], s65 offen offset:256 sc1\n\t"
        "v_exp_f32_e32 v132, v132\n\t"
        "v_exp_f32_e32 v133, v133\n\t"
        "v_exp_f32_e32 v134, v134\n\t"
        "v_exp_f32_e32 v135, v135\n\t"
        "s_waitcnt lgkmcnt(0)\n\t"
        "v_smfmac_f32_32x32x32_bf16 v[48:63], v[152:155], v[112:119], v44\n\t"
        "buffer_load_dwordx2 v[176:177], v17, s[40:43], s65 offen sc1\n\t"
        "v_exp_f32_e32 v136, v136\n\t"
        "v_exp_f32_e32 v137, v137\n\t"
        "v_exp_f32_e32 v138, v138\n\t"
        "v_exp_f32_e32 v139, v139\n\t"
        "v_smfmac_f32_32x32x32_bf16 v[48:63], v[156:159], v[120:127], v44\n\t"
        "buffer_load_dwordx4 v[188:191], v10, s[40:43], s65 offen offset:640 sc1\n\t"
        "buffer_load_dword v178, v22, s[40:43], s66 offen sc1\n\t"
        "buffer_load_dword v27, v22, s[40:43], s67 offen sc1\n\t"
        "v_exp_f32_e32 v140, v140\n\t"
        "v_exp_f32_e32 v141, v141\n\t"
        "v_exp_f32_e32 v142, v142\n\t"
        "v_exp_f32_e32 v143, v143\n\t"
        "s_setprio 1\n\t"
        "v_mfma_f32_32x32x8_bf16 v[96:111], v[148:149], v[42:43], 0\n\t"
        "ds_read_b64 v[148:149], v46 offset:31104\n\t"
        "v_exp_f32_e32 v80, v80\n\t"
        "v_exp_f32_e32 v81, v81\n\t"
        "v_exp_f32_e32 v82, v82\n\t"
        "v_exp_f32_e32 v83, v83\n\t"
        "v_mfma_f32_32x32x16_bf16 v[96:111], v[144:147], v[36:39], v[96:111]\n\t"
        "ds_read_b64 v[144:145], v45 offset:31104\n\t"
        "ds_read_b64 v[146:147], v45 offset:31360\n\t"
        "v_exp_f32_e32 v84, v84\n\t"
        "v_exp_f32_e32 v85, v85\n\t"
        "v_exp_f32_e32 v86, v86\n\t"
        "v_exp_f32_e32 v87, v87\n\t"
        "v_smfmac_f32_32x32x32_bf16 v[64:79], v[152:155], v[128:135], v44\n\t"
        "ds_read_b128 v[152:155], v47 offset:25920\n\t"
        "v_exp_f32_e32 v88, v88\n\t"
        "v_exp_f32_e32 v89, v89\n\t"
        "v_exp_f32_e32 v90, v90\n\t"
        "v_exp_f32_e32 v91, v91\n\t"
        "v_smfmac_f32_32x32x32_bf16 v[64:79], v[156:159], v[136:143], v44\n\t"
        "ds_read_b128 v[156:159], v31 offset:25920\n\t"
        "v_exp_f32_e32 v92, v92\n\t"
        "v_exp_f32_e32 v93, v93\n\t"
        "v_exp_f32_e32 v94, v94\n\t"
        "v_exp_f32_e32 v95, v95\n\t"
        "s_setprio 0\n\t"
        "s_waitcnt lgkmcnt(2)\n\t"
        "v_mfma_f32_32x32x8_bf16 v[112:127], v[148:149], v[40:41], 0\n\t"
        "v_mov_b64_e32 v[232:233], -1\n\t"
        "v_mov_b64_e32 v[234:235], -1\n\t"
        "v_exp_f32_e32 v96, v96\n\t"
        "v_exp_f32_e32 v97, v97\n\t"
        "v_exp_f32_e32 v98, v98\n\t"
        "v_exp_f32_e32 v99, v99\n\t"
        "v_mfma_f32_32x32x16_bf16 v[112:127], v[144:147], v[32:35], v[112:127]\n\t"
        "buffer_store_dwordx4 v[232:235], v11, s[40:43], s70 offen sc1\n\t"
        "v_exp_f32_e32 v100, v100\n\t"
        "v_exp_f32_e32 v101, v101\n\t"
        "v_exp_f32_e32 v102, v102\n\t"
        "v_exp_f32_e32 v103, v103\n\t"
        "s_waitcnt lgkmcnt(0)\n\t"
        "v_smfmac_f32_32x32x32_bf16 v[48:63], v[152:155], v[80:87], v44\n\t"
        "s_mov_b32 exec_hi, 0\n\t"
        "buffer_store_dwordx2 v[232:233], v17, s[40:43], s71 offen sc1\n\t"
        "s_mov_b32 exec_hi, -1\n\t"
        "v_exp_f32_e32 v104, v104\n\t"
        "v_exp_f32_e32 v105, v105\n\t"
        "v_exp_f32_e32 v106, v106\n\t"
        "v_exp_f32_e32 v107, v107\n\t"
        "v_smfmac_f32_32x32x32_bf16 v[48:63], v[156:159], v[88:95], v44\n\t"
        "v_exp_f32_e32 v108, v108\n\t"
        "v_exp_f32_e32 v109, v109\n\t"
        "v_exp_f32_e32 v110, v110\n\t"
        "v_exp_f32_e32 v111, v111\n\t"
        "s_waitcnt vmcnt(12)\n\t"
        "s_barrier\n\t"
        "s_setprio 3\n\t"
        "v_mfma_f32_32x32x8_bf16 v[128:143], v[148:149], v[42:43], 0\n\t"
        "ds_read_b64 v[148:149], v46 offset:36288\n\t"
        "v_exp_f32_e32 v112, v112\n\t"
        "v_exp_f32_e32 v113, v113\n\t"
        "v_exp_f32_e32 v114, v114\n\t"
        "v_exp_f32_e32 v115, v115\n\t"
        "v_mfma_f32_32x32x16_bf16 v[128:143], v[144:147], v[36:39], v[128:143]\n\t"
        "ds_read_b64 v[144:145], v45 offset:36288\n\t"
        "ds_read_b64 v[146:147], v45 offset:36544\n\t"
        "v_exp_f32_e32 v116, v116\n\t"
        "v_exp_f32_e32 v117, v117\n\t"
        "v_exp_f32_e32 v118, v118\n\t"
        "v_exp_f32_e32 v119, v119\n\t"
        "v_smfmac_f32_32x32x32_bf16 v[64:79], v[152:155], v[96:103], v44\n\t"
        "ds_read_b128 v[152:155], v47 offset:31104\n\t"
        "v_exp_f32_e32 v120, v120\n\t"
        "v_exp_f32_e32 v121, v121\n\t"
        "v_exp_f32_e32 v122, v122\n\t"
        "v_exp_f32_e32 v123, v123\n\t"
        "v_smfmac_f32_32x32x32_bf16 v[64:79], v[156:159], v[104:111], v44\n\t"
        "ds_read_b128 v[156:159], v31 offset:31104\n\t"
        "v_exp_f32_e32 v124, v124\n\t"
        "v_exp_f32_e32 v125, v125\n\t"
        "v_exp_f32_e32 v126, v126\n\t"
        "v_exp_f32_e32 v127, v127\n\t"
        "s_waitcnt lgkmcnt(2)\n\t"
        "v_mfma_f32_32x32x8_bf16 v[80:95], v[148:149], v[40:41], 0\n\t"
        "s_mov_b32 m0, s57\n\t"
        "s_add_u32 s57, s57, 20736\n\t"
        "buffer_load_dwordx4 v150, s[40:43], s56 offen sc1 lds\n\t"
        "v_exp_f32_e32 v128, v128\n\t"
        "v_exp_f32_e32 v129, v129\n\t"
        "v_exp_f32_e32 v130, v130\n\t"
        "v_exp_f32_e32 v131, v131\n\t"
        "v_mfma_f32_32x32x16_bf16 v[80:95], v[144:147], v[32:35], v[80:95]\n\t"
        "buffer_load_dwordx4 v150, s[40:43], s56 offen offset:1024 sc1 lds\n\t"
        "s_cmp_ge_u32 s57, s58\n\t"
        "s_cselect_b32 s92, 46656, 0\n\t"
        "v_exp_f32_e32 v132, v132\n\t"
        "v_exp_f32_e32 v133, v133\n\t"
        "v_exp_f32_e32 v134, v134\n\t"
        "v_exp_f32_e32 v135, v135\n\t"
        "s_waitcnt lgkmcnt(0)\n\t"
        "v_smfmac_f32_32x32x32_bf16 v[48:63], v[152:155], v[112:119], v44\n\t"
        "ds_read_b128 v[192:195], v150 offset:57344\n\t"
        "ds_read_b128 v[196:199], v150 offset:58368\n\t"
        "ds_read_b128 v[200:203], v150 offset:59392\n\t"
        "s_mov_b32 exec_hi, 0\n\t"
        "buffer_load_dwordx4 v150, s[40:43], s56 offen offset:2048 sc1 lds\n\t"
        "s_mov_b32 exec_hi, -1\n\t"
        "s_sub_u32 s57, s57, s92\n\t"
        "s_add_u32 s56, s56, 20480\n\t"
        "v_exp_f32_e32 v136, v136\n\t"
        "v_exp_f32_e32 v137, v137\n\t"
        "v_exp_f32_e32 v138, v138\n\t"
        "v_exp_f32_e32 v139, v139\n\t"
        "v_smfmac_f32_32x32x32_bf16 v[48:63], v[156:159], v[120:127], v44\n\t"
        "ds_read_b128 v[204:207], v150 offset:60416\n\t"
        "v_add_u32_e32 v224, s76, v150\n\t"
        "ds_read_b128 v[220:223], v224\n\t"
        "v_exp_f32_e32 v140, v140\n\t"
        "v_exp_f32_e32 v141, v141\n\t"
        "v_exp_f32_e32 v142, v142\n\t"
        "v_exp_f32_e32 v143, v143\n\t"
        "v_mfma_f32_32x32x8_bf16 v[96:111], v[148:149], v[42:43], 0\n\t"
        "ds_read_b64 v[148:149], v46 offset:41472\n\t"
        "v_exp_f32_e32 v80, v80\n\t"
        "v_exp_f32_e32 v81, v81\n\t"
        "v_exp_f32_e32 v82, v82\n\t"
        "v_exp_f32_e32 v83, v83\n\t"
        "v_mfma_f32_32x32x16_bf16 v[96:111], v[144:147], v[36:39], v[96:111]\n\t"
        "ds_read_b64 v[144:145], v45 offset:41472\n\t"
        "ds_read_b64 v[146:147], v45 offset:41728\n\t"
        "v_exp_f32_e32 v84, v84\n\t"
        "v_exp_f32_e32 v85, v85\n\t"
        "v_exp_f32_e32 v86, v86\n\t"
        "v_exp_f32_e32 v87, v87\n\t"
        "v_smfmac_f32_32x32x32_bf16 v[64:79], v[152:155], v[128:135], v44\n\t"
        "ds_read_b128 v[152:155], v47 offset:36288\n\t"
        "v_exp_f32_e32 v88, v88\n\t"
        "v_exp_f32_e32 v89, v89\n\t"
        "v_exp_f32_e32 v90, v90\n\t"
        "v_exp_f32_e32 v91, v91\n\t"
        "v_smfmac_f32_32x32x32_bf16 v[64:79], v[156:159], v[136:143], v44\n\t"
        "ds_read_b128 v[156:159], v31 offset:36288\n\t"
        "v_exp_f32_e32 v92, v92\n\t"
        "v_exp_f32_e32 v93, v93\n\t"
        "v_exp_f32_e32 v94, v94\n\t"
        "v_exp_f32_e32 v95, v95\n\t"
        "s_waitcnt vmcnt(3)\n\t"
        "s_setprio 2\n\t"
        "s_waitcnt lgkmcnt(2)\n\t"
        "v_mfma_f32_32x32x8_bf16 v[112:127], v[148:149], v[40:41], 0\n\t"
        "v_mov_b64_e32 v[228:229], 0\n\t"
        "s_mov_b32 exec_lo, 0x100000\n\t"
        "s_mov_b32 exec_hi, 0x100000\n\t"
        "v_mov_b32_e32 v228, 0x3f803f80\n\t"
        "v_mov_b32_e32 v229, 0x3f803f80\n\t"
        "s_mov_b32 exec_lo, 0xfff00000\n\t"
        "s_mov_b32 exec_hi, 0xfff00000\n\t"
        "v_mov_b64_e32 v[180:181], v[228:229]\n\t"
        "v_mov_b64_e32 v[182:183], v[228:229]\n\t"
        "v_mov_b64_e32 v[184:185], v[228:229]\n\t"
        "v_mov_b64_e32 v[186:187], v[228:229]\n\t"
        "v_mov_b64_e32 v[188:189], v[228:229]\n\t"
        "v_mov_b64_e32 v[190:191], v[228:229]\n\t"
        "s_mov_b64 exec, -1\n\t"
        "v_exp_f32_e32 v96, v96\n\t"
        "v_exp_f32_e32 v97, v97\n\t"
        "v_exp_f32_e32 v98, v98\n\t"
        "v_exp_f32_e32 v99, v99\n\t"
        "v_mfma_f32_32x32x16_bf16 v[112:127], v[144:147], v[32:35], v[112:127]\n\t"
        "s_mov_b32 exec_hi, 0\n\t"
        "v_mov_b64_e32 v[188:189], 0\n\t"
        "v_mov_b64_e32 v[190:191], 0\n\t"
        "s_mov_b32 exec_lo, 0\n\t"
        "s_mov_b32 exec_hi, -1\n\t"
        "v_mov_b64_e32 v[164:165], 0\n\t"
        "s_mov_b64 exec, -1\n\t"
        "v_mov_b64_e32 v[216:217], 0\n\t"
        "v_mov_b64_e32 v[218:219], 0\n\t"
        "v_mov_b64_e32 v[230:231], 0\n\t"
        "v_exp_f32_e32 v100, v100\n\t"
        "v_exp_f32_e32 v101, v101\n\t"
        "v_exp_f32_e32 v102, v102\n\t"
        "v_exp_f32_e32 v103, v103\n\t"
        "s_waitcnt lgkmcnt(0)\n\t"
        "v_smfmac_f32_32x32x32_bf16 v[48:63], v[152:155], v[80:87], v44\n\t"
        "v_exp_f32_e32 v104, v104\n\t"
        "v_exp_f32_e32 v105, v105\n\t"
        "v_exp_f32_e32 v106, v106\n\t"
        "v_exp_f32_e32 v107, v107\n\t"
        "v_smfmac_f32_32x32x32_bf16 v[48:63], v[156:159], v[88:95], v44\n\t"
        "v_exp_f32_e32 v108, v108\n\t"
        "v_exp_f32_e32 v109, v109\n\t"
        "v_exp_f32_e32 v110, v110\n\t"
        "v_exp_f32_e32 v111, v111\n\t"
        "v_mfma_f32_32x32x8_bf16 v[128:143], v[148:149], v[42:43], 0\n\t"
        "ds_read_b64 v[148:149], v46 offset:0\n\t"
        "v_mfma_f32_32x32x8_bf16 v[192:207], v[166:167], v[164:165], v[192:207]\n\t"
        "v_exp_f32_e32 v112, v112\n\t"
        "v_exp_f32_e32 v113, v113\n\t"
        "v_exp_f32_e32 v114, v114\n\t"
        "v_exp_f32_e32 v115, v115\n\t"
        "v_mfma_f32_32x32x16_bf16 v[128:143], v[144:147], v[36:39], v[128:143]\n\t"
        "ds_read_b64 v[144:145], v45 offset:0\n\t"
        "ds_read_b64 v[146:147], v45 offset:256\n\t"
        "v_mfma_f32_32x32x16_bf16 v[192:207], v[168:171], v[160:163], v[192:207]\n\t"
        "v_exp_f32_e32 v116, v116\n\t"
        "v_exp_f32_e32 v117, v117\n\t"
        "v_exp_f32_e32 v118, v118\n\t"
        "v_exp_f32_e32 v119, v119\n\t"
        "v_smfmac_f32_32x32x32_bf16 v[64:79], v[152:155], v[96:103], v44\n\t"
        "ds_read_b128 v[152:155], v47 offset:41472\n\t"
        "v_mfma_f32_32x32x8_bf16 v[208:223], v[176:177], v[164:165], v[208:223]\n\t"
        "v_exp_f32_e32 v120, v120\n\t"
        "v_exp_f32_e32 v121, v121\n\t"
        "v_exp_f32_e32 v122, v122\n\t"
        "v_exp_f32_e32 v123, v123\n\t"
        "v_smfmac_f32_32x32x32_bf16 v[64:79], v[156:159], v[104:111], v44\n\t"
        "ds_read_b128 v[156:159], v31 offset:41472\n\t"
        "v_mfma_f32_32x32x16_bf16 v[208:223], v[172:175], v[160:163], v[208:223]\n\t"
        "v_exp_f32_e32 v124, v124\n\t"
        "v_exp_f32_e32 v125, v125\n\t"
        "v_exp_f32_e32 v126, v126\n\t"
        "v_exp_f32_e32 v127, v127\n\t"
        "s_waitcnt lgkmcnt(2)\n\t"
        "v_mfma_f32_32x32x8_bf16 v[80:95], v[148:149], v[40:41], 0\n\t"
        "s_add_u32 m0, s75, 61440\n\t"
        "s_add_u32 s93, s74, 0\n\t"
        "buffer_load_dwordx4 v0, s[44:47], s93 offen lds\n\t"
        "v_mfma_f32_32x32x8_bf16 v[160:175], v[230:231], v[230:231], 0\n\t"
        "v_max3_f32 v224, v192, v193, v194\n\t"
        "v_max3_f32 v225, v195, v196, v197\n\t"
        "v_max3_f32 v224, v224, v198, v199\n\t"
        "v_max3_f32 v225, v225, v200, v201\n\t"
        "v_exp_f32_e32 v128, v128\n\t"
        "v_exp_f32_e32 v129, v129\n\t"
        "v_exp_f32_e32 v130, v130\n\t"
        "v_exp_f32_e32 v131, v131\n\t"
        "v_mfma_f32_32x32x16_bf16 v[80:95], v[144:147], v[32:35], v[80:95]\n\t"
        "buffer_load_dwordx4 v1, s[44:47], s93 offen offset:1024 lds\n\t"
        "v_max3_f32 v224, v224, v202, v203\n\t"
        "v_max3_f32 v225, v225, v204, v205\n\t"
        "v_max3_f32 v224, v224, v206, v207\n\t"
        "v_exp_f32_e32 v132, v132\n\t"
        "v_exp_f32_e32 v133, v133\n\t"
        "v_exp_f32_e32 v134, v134\n\t"
        "v_exp_f32_e32 v135, v135\n\t"
        "s_waitcnt lgkmcnt(0)\n\t"
        "v_smfmac_f32_32x32x32_bf16 v[48:63], v[152:155], v[112:119], v44\n\t"
        "buffer_load_dwordx4 v2, s[44:47], s93 offen offset:2048 lds\n\t"
        "v_max3_f32 v225, v225, v220, v221\n\t"
        "v_max3_f32 v224, v224, v222, v223\n\t"
        "v_max_f32_e32 v224, v224, v225\n\t"
        "v_mov_b32_e32 v225, v224\n\t"
        "v_exp_f32_e32 v136, v136\n\t"
        "v_exp_f32_e32 v137, v137\n\t"
        "v_exp_f32_e32 v138, v138\n\t"
        "v_exp_f32_e32 v139, v139\n\t"
        "v_smfmac_f32_32x32x32_bf16 v[48:63], v[156:159], v[120:127], v44\n\t"
        "buffer_load_dwordx4 v3, s[44:47], s93 offen offset:3072 lds\n\t"
        "v_permlane32_swap_b32_e32 v224, v225\n\t"
        "v_max_f32_e32 v224, v224, v225\n\t"
        "v_mul_f32_e64 v224, -v224, s96\n\t"
        "v_fma_f32 v192, v192, s96, v224\n\t"
        "v_fma_f32 v193, v193, s96, v224\n\t"
        "v_fma_f32 v194, v194, s96, v224\n\t"
        "v_fma_f32 v195, v195, s96, v224\n\t"
        "v_fma_f32 v196, v196, s96, v224\n\t"
        "v_fma_f32 v197, v197, s96, v224\n\t"
        "v_fma_f32 v198, v198, s96, v224\n\t"
        "v_fma_f32 v199, v199, s96, v224\n\t"
        "v_exp_f32_e32 v140, v140\n\t"
        "v_exp_f32_e32 v141, v141\n\t"
        "v_exp_f32_e32 v142, v142\n\t"
        "v_exp_f32_e32 v143, v143\n\t"
        "s_setprio 1\n\t"
        "v_mfma_f32_32x32x8_bf16 v[96:111], v[148:149], v[42:43], 0\n\t"
        "ds_read_b64 v[148:149], v46 offset:5184\n\t"
        "v_exp_f32_e32 v192, v192\n\t"
        "v_exp_f32_e32 v193, v193\n\t"
        "v_exp_f32_e32 v194, v194\n\t"
        "v_exp_f32_e32 v195, v195\n\t"
        "v_exp_f32_e32 v196, v196\n\t"
        "v_exp_f32_e32 v197, v197\n\t"
        "v_exp_f32_e32 v198, v198\n\t"
        "v_exp_f32_e32 v199, v199\n\t"
        "v_fma_f32 v200, v200, s96, v224\n\t"
        "v_fma_f32 v201, v201, s96, v224\n\t"
        "v_fma_f32 v202, v202, s96, v224\n\t"
        "v_fma_f32 v203, v203, s96, v224\n\t"
        "v_fma_f32 v204, v204, s96, v224\n\t"
        "v_fma_f32 v205, v205, s96, v224\n\t"
        "v_fma_f32 v206, v206, s96, v224\n\t"
        "v_fma_f32 v207, v207, s96, v224\n\t"
        "v_exp_f32_e32 v80, v80\n\t"
        "v_exp_f32_e32 v81, v81\n\t"
        "v_exp_f32_e32 v82, v82\n\t"
        "v_exp_f32_e32 v83, v83\n\t"
        "v_mfma_f32_32x32x16_bf16 v[96:111], v[144:147], v[36:39], v[96:111]\n\t"
        "ds_read_b64 v[144:145], v45 offset:5184\n\t"
        "ds_read_b64 v[146:147], v45 offset:5440\n\t"
        "v_exp_f32_e32 v200, v200\n\t"
        "v_exp_f32_e32 v201, v201\n\t"
        "v_exp_f32_e32 v202, v202\n\t"
        "v_exp_f32_e32 v203, v203\n\t"
        "v_smfmac_f32_32x32x32_bf16 v[160:175], v[180:183], v[192:199], v44\n\t"
        "v_exp_f32_e32 v204, v204\n\t"
        "v_exp_f32_e32 v205, v205\n\t"
        "v_exp_f32_e32 v206, v206\n\t"
        "v_exp_f32_e32 v207, v207\n\t"
        "v_fma_f32 v220, v220, s96, v224\n\t"
        "v_fma_f32 v221, v221, s96, v224\n\t"
        "v_fma_f32 v222, v222, s96, v224\n\t"
        "v_fma_f32 v223, v223, s96, v224\n\t"
        "v_exp_f32_e32 v84, v84\n\t"
        "v_exp_f32_e32 v85, v85\n\t"
        "v_exp_f32_e32 v86, v86\n\t"
        "v_exp_f32_e32 v87, v87\n\t"
        "v_smfmac_f32_32x32x32_bf16 v[64:79], v[152:155], v[128:135], v44\n\t"
        "ds_read_b128 v[152:155], v47 offset:0\n\t"
        "v_exp_f32_e32 v220, v220\n\t"
        "v_exp_f32_e32 v221, v221\n\t"
        "v_exp_f32_e32 v222, v222\n\t"
        "v_exp_f32_e32 v223, v223\n\t"
        "v_smfmac_f32_32x32x32_bf16 v[160:175], v[184:187], v[200:207], v44\n\t"
        "v_exp_f32_e32 v88, v88\n\t"
        "v_exp_f32_e32 v89, v89\n\t"
        "v_exp_f32_e32 v90, v90\n\t"
        "v_exp_f32_e32 v91, v91\n\t"
        "v_smfmac_f32_32x32x32_bf16 v[64:79], v[156:159], v[136:143], v44\n\t"
        "ds_read_b128 v[156:159], v31 offset:0\n\t"
        "v_smfmac_f32_32x32x32_bf16 v[160:175], v[188:191], v[216:223], v44\n\t"
        "v_exp_f32_e32 v92, v92\n\t"
        "v_exp_f32_e32 v93, v93\n\t"
        "v_exp_f32_e32 v94, v94\n\t"
        "v_exp_f32_e32 v95, v95\n\t"
        "s_setprio 0\n\t"
        "s_waitcnt lgkmcnt(2)\n\t"
        "v_mfma_f32_32x32x8_bf16 v[112:127], v[148:149], v[40:41], 0\n\t"
        "s_add_u32 m0, s75, 65536\n\t"
        "s_add_u32 s93, s74, 0\n\t"
        "buffer_load_dwordx4 v4, s[44:47], s93 offen lds\n\t"
        "v_exp_f32_e32 v96, v96\n\t"
        "v_exp_f32_e32 v97, v97\n\t"
        "v_exp_f32_e32 v98, v98\n\t"
        "v_exp_f32_e32 v99, v99\n\t"
        "v_mfma_f32_32x32x16_bf16 v[112:127], v[144:147], v[32:35], v[112:127]\n\t"
        "buffer_load_dwordx4 v5, s[44:47], s93 offen offset:1024 lds\n\t"
        "v_exp_f32_e32 v100, v100\n\t"
        "v_exp_f32_e32 v101, v101\n\t"
        "v_exp_f32_e32 v102, v102\n\t"
        "v_exp_f32_e32 v103, v103\n\t"
        "s_waitcnt lgkmcnt(0)\n\t"
        "v_smfmac_f32_32x32x32_bf16 v[48:63], v[152:155], v[80:87], v44\n\t"
        "s_mov_b64 exec, 0xffff\n\t"
        "buffer_load_dwordx4 v6, s[44:47], s93 offen offset:2048 lds\n\t"
        "s_mov_b64 exec, -1\n\t"
        "v_mov_b32_e32 v224, v168\n\t"
        "v_mov_b32_e32 v225, v168\n\t"
        "v_exp_f32_e32 v104, v104\n\t"
        "v_exp_f32_e32 v105, v105\n\t"
        "v_exp_f32_e32 v106, v106\n\t"
        "v_exp_f32_e32 v107, v107\n\t"
        "v_smfmac_f32_32x32x32_bf16 v[48:63], v[156:159], v[88:95], v44\n\t"
        "v_permlane32_swap_b32_e32 v224, v225\n\t"
        "v_rcp_f32_e32 v224, v225\n\t"
        "s_nop 0\n\t"
        "v_mul_f32_e32 v224, v178, v224\n\t"
        "v_pk_mul_f32 v[160:161], v[160:161], v[224:225] op_sel_hi:[1,0]\n\t"
        "v_pk_mul_f32 v[162:163], v[162:163], v[224:225] op_sel_hi:[1,0]\n\t"
        "v_pk_mul_f32 v[164:165], v[164:165], v[224:225] op_sel_hi:[1,0]\n\t"
        "v_pk_mul_f32 v[166:167], v[166:167], v[224:225] op_sel_hi:[1,0]\n\t"
        "v_pk_mul_f32 v[168:169], v[168:169], v[224:225] op_sel_hi:[1,0]\n\t"
        "v_pk_mul_f32 v[170:171], v[170:171], v[224:225] op_sel_hi:[1,0]\n\t"
        "v_exp_f32_e32 v108, v108\n\t"
        "v_exp_f32_e32 v109, v109\n\t"
        "v_exp_f32_e32 v110, v110\n\t"
        "v_exp_f32_e32 v111, v111\n\t"
        "s_waitcnt vmcnt(7)\n\t"
        "s_barrier\n\t"
        "s_setprio 3\n\t"
        "v_mfma_f32_32x32x8_bf16 v[128:143], v[148:149], v[42:43], 0\n\t"
        "ds_read_b64 v[148:149], v46 offset:10368\n\t"
        "v_cvt_pk_bf16_f32 v192, v160, v161\n\t"
        "v_cvt_pk_bf16_f32 v193, v162, v163\n\t"
        "v_cvt_pk_bf16_f32 v194, v164, v165\n\t"
        "v_cvt_pk_bf16_f32 v195, v166, v167\n\t"
        "v_cvt_pk_bf16_f32 v196, v168, v169\n\t"
        "v_cvt_pk_bf16_f32 v197, v170, v171\n\t"
        "buffer_store_dwordx4 v[192:195], v11, s[40:43], s68 offen sc1\n\t"
        "s_mov_b32 exec_hi, 0\n\t"
        "buffer_store_dwordx2 v[196:197], v17, s[40:43], s69 offen sc1\n\t"
        "s_mov_b32 exec_hi, -1\n\t"
        "v_exp_f32_e32 v112, v112\n\t"
        "v_exp_f32_e32 v113, v113\n\t"
        "v_exp_f32_e32 v114, v114\n\t"
        "v_exp_f32_e32 v115, v115\n\t"
        "v_mfma_f32_32x32x16_bf16 v[128:143], v[144:147], v[36:39], v[128:143]\n\t"
        "ds_read_b64 v[144:145], v45 offset:10368\n\t"
        "ds_read_b64 v[146:147], v45 offset:10624\n\t"
        "v_exp_f32_e32 v116, v116\n\t"
        "v_exp_f32_e32 v117, v117\n\t"
        "v_exp_f32_e32 v118, v118\n\t"
        "v_exp_f32_e32 v119, v119\n\t"
        "v_smfmac_f32_32x32x32_bf16 v[64:79], v[152:155], v[96:103], v44\n\t"
        "ds_read_b128 v[152:155], v47 offset:5184\n\t"
        "v_exp_f32_e32 v120, v120\n\t"
        "v_exp_f32_e32 v121, v121\n\t"
        "v_exp_f32_e32 v122, v122\n\t"
        "v_exp_f32_e32 v123, v123\n\t"
        "v_smfmac_f32_32x32x32_bf16 v[64:79], v[156:159], v[104:111], v44\n\t"
        "ds_read_b128 v[156:159], v31 offset:5184\n\t"
        "v_exp_f32_e32 v124, v124\n\t"
        "v_exp_f32_e32 v125, v125\n\t"
        "v_exp_f32_e32 v126, v126\n\t"
        "v_exp_f32_e32 v127, v127\n\t"
        "s_waitcnt lgkmcnt(2)\n\t"
        "v_mfma_f32_32x32x8_bf16 v[80:95], v[148:149], v[40:41], 0\n\t"
        "s_mov_b32 m0, s57\n\t"
        "s_add_u32 s57, s57, 20736\n\t"
        "buffer_load_dwordx4 v150, s[40:43], s56 offen sc1 lds\n\t"
        "v_exp_f32_e32 v128, v128\n\t"
        "v_exp_f32_e32 v129, v129\n\t"
        "v_exp_f32_e32 v130, v130\n\t"
        "v_exp_f32_e32 v131, v131\n\t"
        "v_mfma_f32_32x32x16_bf16 v[80:95], v[144:147], v[32:35], v[80:95]\n\t"
        "buffer_load_dwordx4 v150, s[40:43], s56 offen offset:1024 sc1 lds\n\t"
        "s_cmp_ge_u32 s57, s58\n\t"
        "s_cselect_b32 s92, 46656, 0\n\t"
        "v_exp_f32_e32 v132, v132\n\t"
        "v_exp_f32_e32 v133, v133\n\t"
        "v_exp_f32_e32 v134, v134\n\t"
        "v_exp_f32_e32 v135, v135\n\t"
        "s_waitcnt lgkmcnt(0)\n\t"
        "v_smfmac_f32_32x32x32_bf16 v[48:63], v[152:155], v[112:119], v44\n\t"
        "s_mov_b32 exec_hi, 0\n\t"
        "buffer_load_dwordx4 v150, s[40:43], s56 offen offset:2048 sc1 lds\n\t"
        "s_mov_b32 exec_hi, -1\n\t"
        "s_sub_u32 s57, s57, s92\n\t"
        "s_add_u32 s56, s56, 20480\n\t"
        "v_exp_f32_e32 v136, v136\n\t"
        "v_exp_f32_e32 v137, v137\n\t"
        "v_exp_f32_e32 v138, v138\n\t"
        "v_exp_f32_e32 v139, v139\n\t"
        "v_smfmac_f32_32x32x32_bf16 v[48:63], v[156:159], v[120:127], v44\n\t"
        "v_exp_f32_e32 v140, v140\n\t"
        "v_exp_f32_e32 v141, v141\n\t"
        "v_exp_f32_e32 v142, v142\n\t"
        "v_exp_f32_e32 v143, v143\n\t"
        "v_mfma_f32_32x32x8_bf16 v[96:111], v[148:149], v[42:43], 0\n\t"
        "ds_read_b64 v[148:149], v46 offset:15552\n\t"
        "v_exp_f32_e32 v80, v80\n\t"
        "v_exp_f32_e32 v81, v81\n\t"
        "v_exp_f32_e32 v82, v82\n\t"
        "v_exp_f32_e32 v83, v83\n\t"
        "v_mfma_f32_32x32x16_bf16 v[96:111], v[144:147], v[36:39], v[96:111]\n\t"
        "ds_read_b64 v[144:145], v45 offset:15552\n\t"
        "ds_read_b64 v[146:147], v45 offset:15808\n\t"
        "v_exp_f32_e32 v84, v84\n\t"
        "v_exp_f32_e32 v85, v85\n\t"
        "v_exp_f32_e32 v86, v86\n\t"
        "v_exp_f32_e32 v87, v87\n\t"
        "v_smfmac_f32_32x32x32_bf16 v[64:79], v[152:155], v[128:135], v44\n\t"
        "ds_read_b128 v[152:155], v47 offset:10368\n\t"
        "v_exp_f32_e32 v88, v88\n\t"
        "v_exp_f32_e32 v89, v89\n\t"
        "v_exp_f32_e32 v90, v90\n\t"
        "v_exp_f32_e32 v91, v91\n\t"
        "v_smfmac_f32_32x32x32_bf16 v[64:79], v[156:159], v[136:143], v44\n\t"
        "ds_read_b128 v[156:159], v31 offset:10368\n\t"
        "v_exp_f32_e32 v92, v92\n\t"
        "v_exp_f32_e32 v93, v93\n\t"
        "v_exp_f32_e32 v94, v94\n\t"
        "v_exp_f32_e32 v95, v95\n\t"
        "s_setprio 2\n\t"
        "s_waitcnt lgkmcnt(2)\n\t"
        "v_mfma_f32_32x32x8_bf16 v[112:127], v[148:149], v[40:41], 0\n\t"
        "s_mov_b64 exec, 0x1ffffff\n\t"
        "buffer_load_dword v26, v23, s[48:51], s81 offen\n\t"
        "s_mov_b64 exec, -1\n\t"
        "v_exp_f32_e32 v96, v96\n\t"
        "v_exp_f32_e32 v97, v97\n\t"
        "v_exp_f32_e32 v98, v98\n\t"
        "v_exp_f32_e32 v99, v99\n\t"
        "v_mfma_f32_32x32x16_bf16 v[112:127], v[144:147], v[32:35], v[112:127]\n\t"
        "v_exp_f32_e32 v100, v100\n\t"
        "v_exp_f32_e32 v101, v101\n\t"
        "v_exp_f32_e32 v102, v102\n\t"
        "v_exp_f32_e32 v103, v103\n\t"
        "s_waitcnt lgkmcnt(0)\n\t"
        "v_smfmac_f32_32x32x32_bf16 v[48:63], v[152:155], v[80:87], v44\n\t"
        "v_exp_f32_e32 v104, v104\n\t"
        "v_exp_f32_e32 v105, v105\n\t"
        "v_exp_f32_e32 v106, v106\n\t"
        "v_exp_f32_e32 v107, v107\n\t"
        "v_smfmac_f32_32x32x32_bf16 v[48:63], v[156:159], v[88:95], v44\n\t"
        "v_exp_f32_e32 v108, v108\n\t"
        "v_exp_f32_e32 v109, v109\n\t"
        "v_exp_f32_e32 v110, v110\n\t"
        "v_exp_f32_e32 v111, v111\n\t"
        "v_mfma_f32_32x32x8_bf16 v[128:143], v[148:149], v[42:43], 0\n\t"
        "ds_read_b64 v[148:149], v46 offset:20736\n\t"
        "v_exp_f32_e32 v112, v112\n\t"
        "v_exp_f32_e32 v113, v113\n\t"
        "v_exp_f32_e32 v114, v114\n\t"
        "v_exp_f32_e32 v115, v115\n\t"
        "v_mfma_f32_32x32x16_bf16 v[128:143], v[144:147], v[36:39], v[128:143]\n\t"
        "ds_read_b64 v[144:145], v45 offset:20736\n\t"
        "ds_read_b64 v[146:147], v45 offset:20992\n\t"
        "v_exp_f32_e32 v116, v116\n\t"
        "v_exp_f32_e32 v117, v117\n\t"
        "v_exp_f32_e32 v118, v118\n\t"
        "v_exp_f32_e32 v119, v119\n\t"
        "v_smfmac_f32_32x32x32_bf16 v[64:79], v[152:155], v[96:103], v44\n\t"
        "ds_read_b128 v[152:155], v47 offset:15552\n\t"
        "v_exp_f32_e32 v120, v120\n\t"
        "v_exp_f32_e32 v121, v121\n\t"
        "v_exp_f32_e32 v122, v122\n\t"
        "v_exp_f32_e32 v123, v123\n\t"
        "v_smfmac_f32_32x32x32_bf16 v[64:79], v[156:159], v[104:111], v44\n\t"
        "ds_read_b128 v[156:159], v31 offset:15552\n\t"
        "v_exp_f32_e32 v124, v124\n\t"
        "v_exp_f32_e32 v125, v125\n\t"
        "v_exp_f32_e32 v126, v126\n\t"
        "v_exp_f32_e32 v127, v127\n\t"
        "s_setprio 0\n\t"
        "s_waitcnt lgkmcnt(2)\n\t"
        "v_mfma_f32_32x32x8_bf16 v[80:95], v[148:149], v[40:41], 0\n\t"
        "v_exp_f32_e32 v128, v128\n\t"
        "v_exp_f32_e32 v129, v129\n\t"
        "v_exp_f32_e32 v130, v130\n\t"
        "v_exp_f32_e32 v131, v131\n\t"
        "v_mfma_f32_32x32x16_bf16 v[80:95], v[144:147], v[32:35], v[80:95]\n\t"
        "v_exp_f32_e32 v132, v132\n\t"
        "v_exp_f32_e32 v133, v133\n\t"
        "v_exp_f32_e32 v134, v134\n\t"
        "v_exp_f32_e32 v135, v135\n\t"
        "s_waitcnt lgkmcnt(0)\n\t"
        "v_smfmac_f32_32x32x32_bf16 v[48:63], v[152:155], v[112:119], v44\n\t"
        "v_exp_f32_e32 v136, v136\n\t"
        "v_exp_f32_e32 v137, v137\n\t"
        "v_exp_f32_e32 v138, v138\n\t"
        "v_exp_f32_e32 v139, v139\n\t"
        "v_smfmac_f32_32x32x32_bf16 v[48:63], v[156:159], v[120:127], v44\n\t"
        "v_exp_f32_e32 v140, v140\n\t"
        "v_exp_f32_e32 v141, v141\n\t"
        "v_exp_f32_e32 v142, v142\n\t"
        "v_exp_f32_e32 v143, v143\n\t"
        "s_waitcnt vmcnt(0)\n\t"
        "s_barrier\n\t"
        "s_cmp_lg_u32 s78, 0\n\t"
        "s_cbranch_scc1 .Lf17\n\t"
        "s_mov_b32 exec_hi, 0\n\t"
        "buffer_store_dword v24, v22, s[40:43], s61 offen sc1\n\t"
        "s_mov_b32 exec_hi, -1\n\t"
        ".Lf17:\n\t"
        "s_setprio 1\n\t"
        "v_mfma_f32_32x32x8_bf16 v[96:111], v[148:149], v[42:43], 0\n\t"
        "ds_read_b64 v[148:149], v46 offset:25920\n\t"
        "v_exp_f32_e32 v80, v80\n\t"
        "v_exp_f32_e32 v81, v81\n\t"
        "v_exp_f32_e32 v82, v82\n\t"
        "v_exp_f32_e32 v83, v83\n\t"
        "v_mfma_f32_32x32x16_bf16 v[96:111], v[144:147], v[36:39], v[96:111]\n\t"
        "ds_read_b64 v[144:145], v45 offset:25920\n\t"
        "ds_read_b64 v[146:147], v45 offset:26176\n\t"
        "v_exp_f32_e32 v84, v84\n\t"
        "v_exp_f32_e32 v85, v85\n\t"
        "v_exp_f32_e32 v86, v86\n\t"
        "v_exp_f32_e32 v87, v87\n\t"
        "v_smfmac_f32_32x32x32_bf16 v[64:79], v[152:155], v[128:135], v44\n\t"
        "ds_read_b128 v[152:155], v47 offset:20736\n\t"
        "v_exp_f32_e32 v88, v88\n\t"
        "v_exp_f32_e32 v89, v89\n\t"
        "v_exp_f32_e32 v90, v90\n\t"
        "v_exp_f32_e32 v91, v91\n\t"
        "v_smfmac_f32_32x32x32_bf16 v[64:79], v[156:159], v[136:143], v44\n\t"
        "ds_read_b128 v[156:159], v31 offset:20736\n\t"
        "v_exp_f32_e32 v92, v92\n\t"
        "v_exp_f32_e32 v93, v93\n\t"
        "v_exp_f32_e32 v94, v94\n\t"
        "v_exp_f32_e32 v95, v95\n\t"
        "s_setprio 0\n\t"
        "s_waitcnt lgkmcnt(2)\n\t"
        "v_mfma_f32_32x32x8_bf16 v[112:127], v[148:149], v[40:41], 0\n\t"
        "v_exp_f32_e32 v96, v96\n\t"
        "v_exp_f32_e32 v97, v97\n\t"
        "v_exp_f32_e32 v98, v98\n\t"
        "v_exp_f32_e32 v99, v99\n\t"
        "v_mfma_f32_32x32x16_bf16 v[112:127], v[144:147], v[32:35], v[112:127]\n\t"
        "v_exp_f32_e32 v100, v100\n\t"
        "v_exp_f32_e32 v101, v101\n\t"
        "v_exp_f32_e32 v102, v102\n\t"
        "v_exp_f32_e32 v103, v103\n\t"
        "s_waitcnt lgkmcnt(0)\n\t"
        "v_smfmac_f32_32x32x32_bf16 v[48:63], v[152:155], v[80:87], v44\n\t"
        "v_exp_f32_e32 v104, v104\n\t"
        "v_exp_f32_e32 v105, v105\n\t"
        "v_exp_f32_e32 v106, v106\n\t"
        "v_exp_f32_e32 v107, v107\n\t"
        "v_smfmac_f32_32x32x32_bf16 v[48:63], v[156:159], v[88:95], v44\n\t"
        "v_exp_f32_e32 v108, v108\n\t"
        "v_exp_f32_e32 v109, v109\n\t"
        "v_exp_f32_e32 v110, v110\n\t"
        "v_exp_f32_e32 v111, v111\n\t"
        "s_barrier\n\t"
        "s_setprio 3\n\t"
        "v_mfma_f32_32x32x8_bf16 v[128:143], v[148:149], v[42:43], 0\n\t"
        "ds_read_b64 v[148:149], v46 offset:31104\n\t"
        "v_exp_f32_e32 v112, v112\n\t"
        "v_exp_f32_e32 v113, v113\n\t"
        "v_exp_f32_e32 v114, v114\n\t"
        "v_exp_f32_e32 v115, v115\n\t"
        "v_mfma_f32_32x32x16_bf16 v[128:143], v[144:147], v[36:39], v[128:143]\n\t"
        "ds_read_b64 v[144:145], v45 offset:31104\n\t"
        "ds_read_b64 v[146:147], v45 offset:31360\n\t"
        "v_exp_f32_e32 v116, v116\n\t"
        "v_exp_f32_e32 v117, v117\n\t"
        "v_exp_f32_e32 v118, v118\n\t"
        "v_exp_f32_e32 v119, v119\n\t"
        "v_smfmac_f32_32x32x32_bf16 v[64:79], v[152:155], v[96:103], v44\n\t"
        "ds_read_b128 v[152:155], v47 offset:25920\n\t"
        "v_exp_f32_e32 v120, v120\n\t"
        "v_exp_f32_e32 v121, v121\n\t"
        "v_exp_f32_e32 v122, v122\n\t"
        "v_exp_f32_e32 v123, v123\n\t"
        "v_smfmac_f32_32x32x32_bf16 v[64:79], v[156:159], v[104:111], v44\n\t"
        "ds_read_b128 v[156:159], v31 offset:25920\n\t"
        "v_exp_f32_e32 v124, v124\n\t"
        "v_exp_f32_e32 v125, v125\n\t"
        "v_exp_f32_e32 v126, v126\n\t"
        "v_exp_f32_e32 v127, v127\n\t"
        "s_waitcnt lgkmcnt(2)\n\t"
        "v_mfma_f32_32x32x8_bf16 v[80:95], v[148:149], v[40:41], 0\n\t"
        "s_mov_b32 m0, s57\n\t"
        "s_add_u32 s57, s57, 20736\n\t"
        "buffer_load_dwordx4 v150, s[40:43], s56 offen sc1 lds\n\t"
        "v_exp_f32_e32 v128, v128\n\t"
        "v_exp_f32_e32 v129, v129\n\t"
        "v_exp_f32_e32 v130, v130\n\t"
        "v_exp_f32_e32 v131, v131\n\t"
        "v_mfma_f32_32x32x16_bf16 v[80:95], v[144:147], v[32:35], v[80:95]\n\t"
        "buffer_load_dwordx4 v150, s[40:43], s56 offen offset:1024 sc1 lds\n\t"
        "s_cmp_ge_u32 s57, s58\n\t"
        "s_cselect_b32 s92, 46656, 0\n\t"
        "v_exp_f32_e32 v132, v132\n\t"
        "v_exp_f32_e32 v133, v133\n\t"
        "v_exp_f32_e32 v134, v134\n\t"
        "v_exp_f32_e32 v135, v135\n\t"
        "s_waitcnt lgkmcnt(0)\n\t"
        "v_smfmac_f32_32x32x32_bf16 v[48:63], v[152:155], v[112:119], v44\n\t"
        "s_mov_b32 exec_hi, 0\n\t"
        "buffer_load_dwordx4 v150, s[40:43], s56 offen offset:2048 sc1 lds\n\t"
        "s_mov_b32 exec_hi, -1\n\t"
        "s_sub_u32 s57, s57, s92\n\t"
        "s_add_u32 s56, s56, 20480\n\t"
        "v_exp_f32_e32 v136, v136\n\t"
        "v_exp_f32_e32 v137, v137\n\t"
        "v_exp_f32_e32 v138, v138\n\t"
        "v_exp_f32_e32 v139, v139\n\t"
        "v_smfmac_f32_32x32x32_bf16 v[48:63], v[156:159], v[120:127], v44\n\t"
        "buffer_load_dword v25, v23, s[40:43], s62 offen sc1\n\t"
        "v_exp_f32_e32 v140, v140\n\t"
        "v_exp_f32_e32 v141, v141\n\t"
        "v_exp_f32_e32 v142, v142\n\t"
        "v_exp_f32_e32 v143, v143\n\t"
        "v_mfma_f32_32x32x8_bf16 v[96:111], v[148:149], v[42:43], 0\n\t"
        "ds_read_b64 v[148:149], v46 offset:36288\n\t"
        "v_exp_f32_e32 v80, v80\n\t"
        "v_exp_f32_e32 v81, v81\n\t"
        "v_exp_f32_e32 v82, v82\n\t"
        "v_exp_f32_e32 v83, v83\n\t"
        "v_mfma_f32_32x32x16_bf16 v[96:111], v[144:147], v[36:39], v[96:111]\n\t"
        "ds_read_b64 v[144:145], v45 offset:36288\n\t"
        "ds_read_b64 v[146:147], v45 offset:36544\n\t"
        "v_exp_f32_e32 v84, v84\n\t"
        "v_exp_f32_e32 v85, v85\n\t"
        "v_exp_f32_e32 v86, v86\n\t"
        "v_exp_f32_e32 v87, v87\n\t"
        "v_smfmac_f32_32x32x32_bf16 v[64:79], v[152:155], v[128:135], v44\n\t"
        "ds_read_b128 v[152:155], v47 offset:31104\n\t"
        "v_exp_f32_e32 v88, v88\n\t"
        "v_exp_f32_e32 v89, v89\n\t"
        "v_exp_f32_e32 v90, v90\n\t"
        "v_exp_f32_e32 v91, v91\n\t"
        "v_smfmac_f32_32x32x32_bf16 v[64:79], v[156:159], v[136:143], v44\n\t"
        "ds_read_b128 v[156:159], v31 offset:31104\n\t"
        "v_exp_f32_e32 v92, v92\n\t"
        "v_exp_f32_e32 v93, v93\n\t"
        "v_exp_f32_e32 v94, v94\n\t"
        "v_exp_f32_e32 v95, v95\n\t"
        "s_setprio 2\n\t"
        "s_waitcnt lgkmcnt(2)\n\t"
        "v_mfma_f32_32x32x8_bf16 v[112:127], v[148:149], v[40:41], 0\n\t"
        "v_exp_f32_e32 v96, v96\n\t"
        "v_exp_f32_e32 v97, v97\n\t"
        "v_exp_f32_e32 v98, v98\n\t"
        "v_exp_f32_e32 v99, v99\n\t"
        "v_mfma_f32_32x32x16_bf16 v[112:127], v[144:147], v[32:35], v[112:127]\n\t"
        "v_exp_f32_e32 v100, v100\n\t"
        "v_exp_f32_e32 v101, v101\n\t"
        "v_exp_f32_e32 v102, v102\n\t"
        "v_exp_f32_e32 v103, v103\n\t"
        "s_waitcnt lgkmcnt(0)\n\t"
        "v_smfmac_f32_32x32x32_bf16 v[48:63], v[152:155], v[80:87], v44\n\t"
        "v_exp_f32_e32 v104, v104\n\t"
        "v_exp_f32_e32 v105, v105\n\t"
        "v_exp_f32_e32 v106, v106\n\t"
        "v_exp_f32_e32 v107, v107\n\t"
        "v_smfmac_f32_32x32x32_bf16 v[48:63], v[156:159], v[88:95], v44\n\t"
        "v_exp_f32_e32 v108, v108\n\t"
        "v_exp_f32_e32 v109, v109\n\t"
        "v_exp_f32_e32 v110, v110\n\t"
        "v_exp_f32_e32 v111, v111\n\t"
        "s_waitcnt vmcnt(0)\n\t"
        "v_cmp_ne_u32_e32 vcc, v25, v24\n\t"
        "s_cbranch_vccz .Lq18\n\t"
        "s_mov_b32 s94, 0x80000\n\t"
        ".Lp18:\n\t"
        "s_sleep 1\n\t"
        "buffer_load_dword v25, v23, s[40:43], s62 offen sc1\n\t"
        "s_waitcnt vmcnt(0)\n\t"
        "v_cmp_ne_u32_e32 vcc, v25, v24\n\t"
        "s_sub_u32 s94, s94, 1\n\t"
        "s_cbranch_scc1 .Lq18\n\t"
        "s_cbranch_vccnz .Lp18\n\t"
        ".Lq18:\n\t"
        "v_mfma_f32_32x32x8_bf16 v[128:143], v[148:149], v[42:43], 0\n\t"
        "ds_read_b64 v[148:149], v46 offset:41472\n\t"
        "v_exp_f32_e32 v112, v112\n\t"
        "v_exp_f32_e32 v113, v113\n\t"
        "v_exp_f32_e32 v114, v114\n\t"
        "v_exp_f32_e32 v115, v115\n\t"
        "v_mfma_f32_32x32x16_bf16 v[128:143], v[144:147], v[36:39], v[128:143]\n\t"
        "ds_read_b64 v[144:145], v45 offset:41472\n\t"
        "ds_read_b64 v[146:147], v45 offset:41728\n\t"
        "v_exp_f32_e32 v116, v116\n\t"
        "v_exp_f32_e32 v117, v117\n\t"
        "v_exp_f32_e32 v118, v118\n\t"
        "v_exp_f32_e32 v119, v119\n\t"
        "v_smfmac_f32_32x32x32_bf16 v[64:79], v[152:155], v[96:103], v44\n\t"
        "ds_read_b128 v[152:155], v47 offset:36288\n\t"
        "s_add_u32 s93, s72, 0\n\t"
        "buffer_load_dwordx4 v[160:163], v12, s[40:43], s93 offen sc1\n\t"
        "v_exp_f32_e32 v120, v120\n\t"
        "v_exp_f32_e32 v121, v121\n\t"
        "v_exp_f32_e32 v122, v122\n\t"
        "v_exp_f32_e32 v123, v123\n\t"
        "v_smfmac_f32_32x32x32_bf16 v[64:79], v[156:159], v[104:111], v44\n\t"
        "ds_read_b128 v[156:159], v31 offset:36288\n\t"
        "s_add_u32 s93, s72, 20480\n\t"
        "buffer_load_dwordx4 v[164:167], v12, s[40:43], s93 offen sc1\n\t"
        "v_exp_f32_e32 v124, v124\n\t"
        "v_exp_f32_e32 v125, v125\n\t"
        "v_exp_f32_e32 v126, v126\n\t"
        "v_exp_f32_e32 v127, v127\n\t"
        "s_waitcnt lgkmcnt(2)\n\t"
        "v_mfma_f32_32x32x8_bf16 v[80:95], v[148:149], v[40:41], 0\n\t"
        "s_add_u32 s93, s72, 40960\n\t"
        "buffer_load_dwordx4 v[168:171], v12, s[40:43], s93 offen sc1\n\t"
        "v_exp_f32_e32 v128, v128\n\t"
        "v_exp_f32_e32 v129, v129\n\t"
        "v_exp_f32_e32 v130, v130\n\t"
        "v_exp_f32_e32 v131, v131\n\t"
        "v_mfma_f32_32x32x16_bf16 v[80:95], v[144:147], v[32:35], v[80:95]\n\t"
        "s_add_u32 s93, s72, 61440\n\t"
        "buffer_load_dwordx4 v[172:175], v12, s[40:43], s93 offen sc1\n\t"
        "v_exp_f32_e32 v132, v132\n\t"
        "v_exp_f32_e32 v133, v133\n\t"
        "v_exp_f32_e32 v134, v134\n\t"
        "v_exp_f32_e32 v135, v135\n\t"
        "s_waitcnt lgkmcnt(0)\n\t"
        "v_smfmac_f32_32x32x32_bf16 v[48:63], v[152:155], v[112:119], v44\n\t"
        "s_add_u32 s93, s73, 0\n\t"
        "buffer_load_dwordx2 v[176:177], v13, s[40:43], s93 offen sc1\n\t"
        "v_exp_f32_e32 v136, v136\n\t"
        "v_exp_f32_e32 v137, v137\n\t"
        "v_exp_f32_e32 v138, v138\n\t"
        "v_exp_f32_e32 v139, v139\n\t"
        "v_smfmac_f32_32x32x32_bf16 v[48:63], v[156:159], v[120:127], v44\n\t"
        "s_add_u32 s93, s73, 40960\n\t"
        "buffer_load_dwordx2 v[178:179], v13, s[40:43], s93 offen sc1\n\t"
        "v_exp_f32_e32 v140, v140\n\t"
        "v_exp_f32_e32 v141, v141\n\t"
        "v_exp_f32_e32 v142, v142\n\t"
        "v_exp_f32_e32 v143, v143\n\t"
        "s_setprio 1\n\t"
        "v_mfma_f32_32x32x8_bf16 v[96:111], v[148:149], v[42:43], 0\n\t"
        "ds_read_b64 v[148:149], v46 offset:0\n\t"
        "v_exp_f32_e32 v80, v80\n\t"
        "v_exp_f32_e32 v81, v81\n\t"
        "v_exp_f32_e32 v82, v82\n\t"
        "v_exp_f32_e32 v83, v83\n\t"
        "v_mfma_f32_32x32x16_bf16 v[96:111], v[144:147], v[36:39], v[96:111]\n\t"
        "ds_read_b64 v[144:145], v45 offset:0\n\t"
        "ds_read_b64 v[146:147], v45 offset:256\n\t"
        "v_exp_f32_e32 v84, v84\n\t"
        "v_exp_f32_e32 v85, v85\n\t"
        "v_exp_f32_e32 v86, v86\n\t"
        "v_exp_f32_e32 v87, v87\n\t"
        "v_smfmac_f32_32x32x32_bf16 v[64:79], v[152:155], v[128:135], v44\n\t"
        "ds_read_b128 v[152:155], v47 offset:41472\n\t"
        "v_exp_f32_e32 v88, v88\n\t"
        "v_exp_f32_e32 v89, v89\n\t"
        "v_exp_f32_e32 v90, v90\n\t"
        "v_exp_f32_e32 v91, v91\n\t"
        "v_smfmac_f32_32x32x32_bf16 v[64:79], v[156:159], v[136:143], v44\n\t"
        "ds_read_b128 v[156:159], v31 offset:41472\n\t"
        "v_exp_f32_e32 v92, v92\n\t"
        "v_exp_f32_e32 v93, v93\n\t"
        "v_exp_f32_e32 v94, v94\n\t"
        "v_exp_f32_e32 v95, v95\n\t"
        "s_setprio 0\n\t"
        "s_waitcnt lgkmcnt(2)\n\t"
        "v_mfma_f32_32x32x8_bf16 v[112:127], v[148:149], v[40:41], 0\n\t"
        "s_add_u32 s93, s72, 163840\n\t"
        "buffer_load_dwordx4 v[180:183], v12, s[40:43], s93 offen sc1\n\t"
        "v_exp_f32_e32 v96, v96\n\t"
        "v_exp_f32_e32 v97, v97\n\t"
        "v_exp_f32_e32 v98, v98\n\t"
        "v_exp_f32_e32 v99, v99\n\t"
        "v_mfma_f32_32x32x16_bf16 v[112:127], v[144:147], v[32:35], v[112:127]\n\t"
        "s_add_u32 s93, s72, 184320\n\t"
        "buffer_load_dwordx4 v[184:187], v12, s[40:43], s93 offen sc1\n\t"
        "v_exp_f32_e32 v100, v100\n\t"
        "v_exp_f32_e32 v101, v101\n\t"
        "v_exp_f32_e32 v102, v102\n\t"
        "v_exp_f32_e32 v103, v103\n\t"
        "s_waitcnt lgkmcnt(0)\n\t"
        "v_smfmac_f32_32x32x32_bf16 v[48:63], v[152:155], v[80:87], v44\n\t"
        "s_add_u32 s93, s72, 204800\n\t"
        "buffer_load_dwordx4 v[188:191], v12, s[40:43], s93 offen sc1\n\t"
        "v_exp_f32_e32 v104, v104\n\t"
        "v_exp_f32_e32 v105, v105\n\t"
        "v_exp_f32_e32 v106, v106\n\t"
        "v_exp_f32_e32 v107, v107\n\t"
        "v_smfmac_f32_32x32x32_bf16 v[48:63], v[156:159], v[88:95], v44\n\t"
        "s_add_u32 s93, s72, 225280\n\t"
        "buffer_load_dwordx4 v[192:195], v12, s[40:43], s93 offen sc1\n\t"
        "v_exp_f32_e32 v108, v108\n\t"
        "v_exp_f32_e32 v109, v109\n\t"
        "v_exp_f32_e32 v110, v110\n\t"
        "v_exp_f32_e32 v111, v111\n\t"
        "s_barrier\n\t"
        "s_setprio 3\n\t"
        "v_mfma_f32_32x32x8_bf16 v[128:143], v[148:149], v[42:43], 0\n\t"
        "ds_read_b64 v[148:149], v46 offset:5184\n\t"
        "v_exp_f32_e32 v112, v112\n\t"
        "v_exp_f32_e32 v113, v113\n\t"
        "v_exp_f32_e32 v114, v114\n\t"
        "v_exp_f32_e32 v115, v115\n\t"
        "v_mfma_f32_32x32x16_bf16 v[128:143], v[144:147], v[36:39], v[128:143]\n\t"
        "ds_read_b64 v[144:145], v45 offset:5184\n\t"
        "ds_read_b64 v[146:147], v45 offset:5440\n\t"
        "v_exp_f32_e32 v116, v116\n\t"
        "v_exp_f32_e32 v117, v117\n\t"
        "v_exp_f32_e32 v118, v118\n\t"
        "v_exp_f32_e32 v119, v119\n\t"
        "v_smfmac_f32_32x32x32_bf16 v[64:79], v[152:155], v[96:103], v44\n\t"
        "ds_read_b128 v[152:155], v47 offset:0\n\t"
        "v_exp_f32_e32 v120, v120\n\t"
        "v_exp_f32_e32 v121, v121\n\t"
        "v_exp_f32_e32 v122, v122\n\t"
        "v_exp_f32_e32 v123, v123\n\t"
        "v_smfmac_f32_32x32x32_bf16 v[64:79], v[156:159], v[104:111], v44\n\t"
        "ds_read_b128 v[156:159], v31 offset:0\n\t"
        "v_exp_f32_e32 v124, v124\n\t"
        "v_exp_f32_e32 v125, v125\n\t"
        "v_exp_f32_e32 v126, v126\n\t"
        "v_exp_f32_e32 v127, v127\n\t"
        "s_waitcnt lgkmcnt(2)\n\t"
        "v_mfma_f32_32x32x8_bf16 v[80:95], v[148:149], v[40:41], 0\n\t"
        "s_add_u32 s93, s73, 163840\n\t"
        "buffer_load_dwordx2 v[196:197], v13, s[40:43], s93 offen sc1\n\t"
        "v_exp_f32_e32 v128, v128\n\t"
        "v_exp_f32_e32 v129, v129\n\t"
        "v_exp_f32_e32 v130, v130\n\t"
        "v_exp_f32_e32 v131, v131\n\t"
        "v_mfma_f32_32x32x16_bf16 v[80:95], v[144:147], v[32:35], v[80:95]\n\t"
        "s_add_u32 s93, s73, 204800\n\t"
        "buffer_load_dwordx2 v[198:199], v13, s[40:43], s93 offen sc1\n\t"
        "v_exp_f32_e32 v132, v132\n\t"
        "v_exp_f32_e32 v133, v133\n\t"
        "v_exp_f32_e32 v134, v134\n\t"
        "v_exp_f32_e32 v135, v135\n\t"
        "s_waitcnt lgkmcnt(0)\n\t"
        "v_smfmac_f32_32x32x32_bf16 v[48:63], v[152:155], v[112:119], v44\n\t"
        "v_exp_f32_e32 v136, v136\n\t"
        "v_exp_f32_e32 v137, v137\n\t"
        "v_exp_f32_e32 v138, v138\n\t"
        "v_exp_f32_e32 v139, v139\n\t"
        "v_smfmac_f32_32x32x32_bf16 v[48:63], v[156:159], v[120:127], v44\n\t"
        "v_exp_f32_e32 v140, v140\n\t"
        "v_exp_f32_e32 v141, v141\n\t"
        "v_exp_f32_e32 v142, v142\n\t"
        "v_exp_f32_e32 v143, v143\n\t"
        "s_waitcnt vmcnt(6)\n\t"
        "v_add_u32_e32 v28, 61440, v14\n\t"
        "v_add_u32_e32 v151, 61440, v15\n\t"
        "ds_read_b64_tr_b16 v[96:97], v28 offset:0\n\t"
        "ds_read_b64_tr_b16 v[98:99], v28 offset:1280\n\t"
        "ds_read_b64_tr_b16 v[100:101], v28 offset:32\n\t"
        "ds_read_b64_tr_b16 v[102:103], v28 offset:1312\n\t"
        "ds_read_b64_tr_b16 v[104:105], v28 offset:64\n\t"
        "ds_read_b64_tr_b16 v[106:107], v28 offset:1344\n\t"
        "ds_read_b64_tr_b16 v[108:109], v28 offset:96\n\t"
        "ds_read_b64_tr_b16 v[110:111], v28 offset:1376\n\t"
        "ds_read_b64_tr_b16 v[112:113], v28 offset:128\n\t"
        "ds_read_b64_tr_b16 v[114:115], v28 offset:1408\n\t"
        "ds_read_b64_tr_b16 v[116:117], v28 offset:6400\n\t"
        "ds_read_b64_tr_b16 v[118:119], v28 offset:7680\n\t"
        "ds_read_b64_tr_b16 v[120:121], v28 offset:6432\n\t"
        "ds_read_b64_tr_b16 v[122:123], v28 offset:7712\n\t"
        "s_setprio 2\n\t"
        "s_waitcnt lgkmcnt(12)\n\t"
        "v_mfma_f32_16x16x32_bf16 v[232:235], v[96:99], v[160:163], 0\n\t"
        "ds_read_b64_tr_b16 v[124:125], v28 offset:6464\n\t"
        "ds_read_b64_tr_b16 v[126:127], v28 offset:7744\n\t"
        "s_waitcnt lgkmcnt(12)\n\t"
        "v_mfma_f32_16x16x32_bf16 v[236:239], v[100:103], v[160:163], 0\n\t"
        "ds_read_b64_tr_b16 v[96:97], v28 offset:6496\n\t"
        "ds_read_b64_tr_b16 v[98:99], v28 offset:7776\n\t"
        "s_waitcnt lgkmcnt(12)\n\t"
        "v_mfma_f32_16x16x32_bf16 v[240:243], v[104:107], v[160:163], 0\n\t"
        "ds_read_b64_tr_b16 v[100:101], v28 offset:6528\n\t"
        "ds_read_b64_tr_b16 v[102:103], v28 offset:7808\n\t"
        "s_waitcnt lgkmcnt(12)\n\t"
        "v_mfma_f32_16x16x32_bf16 v[244:247], v[108:111], v[160:163], 0\n\t"
        "ds_read_b64_tr_b16 v[104:105], v28 offset:12800\n\t"
        "ds_read_b64_tr_b16 v[106:107], v28 offset:14080\n\t"
        "s_waitcnt lgkmcnt(12)\n\t"
        "v_mfma_f32_16x16x32_bf16 v[248:251], v[112:115], v[160:163], 0\n\t"
        "ds_read_b64_tr_b16 v[108:109], v28 offset:12832\n\t"
        "ds_read_b64_tr_b16 v[110:111], v28 offset:14112\n\t"
        "s_waitcnt lgkmcnt(12)\n\t"
        "v_mfma_f32_16x16x32_bf16 v[232:235], v[116:119], v[164:167], v[232:235]\n\t"
        "ds_read_b64_tr_b16 v[112:113], v28 offset:12864\n\t"
        "ds_read_b64_tr_b16 v[114:115], v28 offset:14144\n\t"
        "s_waitcnt lgkmcnt(12)\n\t"
        "v_mfma_f32_16x16x32_bf16 v[236:239], v[120:123], v[164:167], v[236:239]\n\t"
        "ds_read_b64_tr_b16 v[116:117], v28 offset:12896\n\t"
        "ds_read_b64_tr_b16 v[118:119], v28 offset:14176\n\t"
        "s_waitcnt lgkmcnt(12)\n\t"
        "v_mfma_f32_16x16x32_bf16 v[240:243], v[124:127], v[164:167], v[240:243]\n\t"
        "ds_read_b64_tr_b16 v[120:121], v28 offset:12928\n\t"
        "ds_read_b64_tr_b16 v[122:123], v28 offset:14208\n\t"
        "s_waitcnt lgkmcnt(12)\n\t"
        "v_mfma_f32_16x16x32_bf16 v[244:247], v[96:99], v[164:167], v[244:247]\n\t"
        "ds_read_b64_tr_b16 v[124:125], v28 offset:19200\n\t"
        "ds_read_b64_tr_b16 v[126:127], v28 offset:20480\n\t"
        "s_waitcnt lgkmcnt(12)\n\t"
        "v_mfma_f32_16x16x32_bf16 v[248:251], v[100:103], v[164:167], v[248:251]\n\t"
        "ds_read_b64_tr_b16 v[96:97], v28 offset:19232\n\t"
        "ds_read_b64_tr_b16 v[98:99], v28 offset:20512\n\t"
        "s_waitcnt lgkmcnt(12)\n\t"
        "v_mfma_f32_16x16x32_bf16 v[232:235], v[104:107], v[168:171], v[232:235]\n\t"
        "ds_read_b64_tr_b16 v[100:101], v28 offset:19264\n\t"
        "ds_read_b64_tr_b16 v[102:103], v28 offset:20544\n\t"
        "s_waitcnt lgkmcnt(12)\n\t"
        "v_mfma_f32_16x16x32_bf16 v[236:239], v[108:111], v[168:171], v[236:239]\n\t"
        "ds_read_b64_tr_b16 v[104:105], v28 offset:19296\n\t"
        "ds_read_b64_tr_b16 v[106:107], v28 offset:20576\n\t"
        "s_waitcnt lgkmcnt(12)\n\t"
        "v_mfma_f32_16x16x32_bf16 v[240:243], v[112:115], v[168:171], v[240:243]\n\t"
        "ds_read_b64_tr_b16 v[108:109], v28 offset:19328\n\t"
        "ds_read_b64_tr_b16 v[110:111], v28 offset:20608\n\t"
        "s_waitcnt lgkmcnt(12)\n\t"
        "v_mfma_f32_16x16x32_bf16 v[244:247], v[116:119], v[168:171], v[244:247]\n\t"
        "ds_read_b64_tr_b16 v[112:113], v151 offset:0\n\t"
        "ds_read_b64_tr_b16 v[114:115], v151 offset:12800\n\t"
        "s_waitcnt lgkmcnt(12)\n\t"
        "v_mfma_f32_16x16x32_bf16 v[248:251], v[120:123], v[168:171], v[248:251]\n\t"
        "ds_read_b64_tr_b16 v[116:117], v151 offset:32\n\t"
        "ds_read_b64_tr_b16 v[118:119], v151 offset:12832\n\t"
        "s_waitcnt lgkmcnt(12)\n\t"
        "v_mfma_f32_16x16x32_bf16 v[232:235], v[124:127], v[172:175], v[232:235]\n\t"
        "ds_read_b64_tr_b16 v[120:121], v151 offset:64\n\t"
        "ds_read_b64_tr_b16 v[122:123], v151 offset:12864\n\t"
        "s_waitcnt lgkmcnt(12)\n\t"
        "v_mfma_f32_16x16x32_bf16 v[236:239], v[96:99], v[172:175], v[236:239]\n\t"
        "ds_read_b64_tr_b16 v[124:125], v151 offset:96\n\t"
        "ds_read_b64_tr_b16 v[126:127], v151 offset:12896\n\t"
        "s_waitcnt lgkmcnt(12)\n\t"
        "v_mfma_f32_16x16x32_bf16 v[240:243], v[100:103], v[172:175], v[240:243]\n\t"
        "ds_read_b64_tr_b16 v[96:97], v151 offset:128\n\t"
        "ds_read_b64_tr_b16 v[98:99], v151 offset:12928\n\t"
        "s_waitcnt lgkmcnt(12)\n\t"
        "v_mfma_f32_16x16x32_bf16 v[244:247], v[104:107], v[172:175], v[244:247]\n\t"
        "s_waitcnt lgkmcnt(10)\n\t"
        "v_mfma_f32_16x16x32_bf16 v[248:251], v[108:111], v[172:175], v[248:251]\n\t"
        "s_waitcnt lgkmcnt(8)\n\t"
        "v_mfma_f32_16x16x32_bf16 v[232:235], v[112:115], v[176:179], v[232:235]\n\t"
        "s_waitcnt lgkmcnt(6)\n\t"
        "v_mfma_f32_16x16x32_bf16 v[236:239], v[116:119], v[176:179], v[236:239]\n\t"
        "s_waitcnt lgkmcnt(4)\n\t"
        "v_mfma_f32_16x16x32_bf16 v[240:243], v[120:123], v[176:179], v[240:243]\n\t"
        "s_waitcnt lgkmcnt(2)\n\t"
        "v_mfma_f32_16x16x32_bf16 v[244:247], v[124:127], v[176:179], v[244:247]\n\t"
        "s_waitcnt lgkmcnt(0)\n\t"
        "v_mfma_f32_16x16x32_bf16 v[248:251], v[96:99], v[176:179], v[248:251]\n\t"
        "s_setprio 1\n\t"
        "v_mfma_f32_32x32x8_bf16 v[96:111], v[148:149], v[42:43], 0\n\t"
        "ds_read_b64 v[148:149], v46 offset:10368\n\t"
        "v_exp_f32_e32 v80, v80\n\t"
        "v_exp_f32_e32 v81, v81\n\t"
        "v_exp_f32_e32 v82, v82\n\t"
        "v_exp_f32_e32 v83, v83\n\t"
        "v_mfma_f32_32x32x16_bf16 v[96:111], v[144:147], v[36:39], v[96:111]\n\t"
        "ds_read_b64 v[144:145], v45 offset:10368\n\t"
        "ds_read_b64 v[146:147], v45 offset:10624\n\t"
        "v_exp_f32_e32 v84, v84\n\t"
        "v_exp_f32_e32 v85, v85\n\t"
        "v_exp_f32_e32 v86, v86\n\t"
        "v_exp_f32_e32 v87, v87\n\t"
        "v_smfmac_f32_32x32x32_bf16 v[64:79], v[152:155], v[128:135], v44\n\t"
        "ds_read_b128 v[152:155], v47 offset:5184\n\t"
        "v_exp_f32_e32 v88, v88\n\t"
        "v_exp_f32_e32 v89, v89\n\t"
        "v_exp_f32_e32 v90, v90\n\t"
        "v_exp_f32_e32 v91, v91\n\t"
        "v_smfmac_f32_32x32x32_bf16 v[64:79], v[156:159], v[136:143], v44\n\t"
        "ds_read_b128 v[156:159], v31 offset:5184\n\t"
        "v_exp_f32_e32 v92, v92\n\t"
        "v_exp_f32_e32 v93, v93\n\t"
        "v_exp_f32_e32 v94, v94\n\t"
        "v_exp_f32_e32 v95, v95\n\t"
        "s_setprio 0\n\t"
        "s_waitcnt lgkmcnt(2)\n\t"
        "v_mfma_f32_32x32x8_bf16 v[112:127], v[148:149], v[40:41], 0\n\t"
        "v_exp_f32_e32 v96, v96\n\t"
        "v_exp_f32_e32 v97, v97\n\t"
        "v_exp_f32_e32 v98, v98\n\t"
        "v_exp_f32_e32 v99, v99\n\t"
        "v_mfma_f32_32x32x16_bf16 v[112:127], v[144:147], v[32:35], v[112:127]\n\t"
        "v_exp_f32_e32 v100, v100\n\t"
        "v_exp_f32_e32 v101, v101\n\t"
        "v_exp_f32_e32 v102, v102\n\t"
        "v_exp_f32_e32 v103, v103\n\t"
        "s_waitcnt lgkmcnt(0)\n\t"
        "v_smfmac_f32_32x32x32_bf16 v[48:63], v[152:155], v[80:87], v44\n\t"
        "v_exp_f32_e32 v104, v104\n\t"
        "v_exp_f32_e32 v105, v105\n\t"
        "v_exp_f32_e32 v106, v106\n\t"
        "v_exp_f32_e32 v107, v107\n\t"
        "v_smfmac_f32_32x32x32_bf16 v[48:63], v[156:159], v[88:95], v44\n\t"
        "v_exp_f32_e32 v108, v108\n\t"
        "v_exp_f32_e32 v109, v109\n\t"
        "v_exp_f32_e32 v110, v110\n\t"
        "v_exp_f32_e32 v111, v111\n\t"
        "s_barrier\n\t"
        "s_setprio 3\n\t"
        "v_mfma_f32_32x32x8_bf16 v[128:143], v[148:149], v[42:43], 0\n\t"
        "ds_read_b64 v[148:149], v46 offset:15552\n\t"
        "v_exp_f32_e32 v112, v112\n\t"
        "v_exp_f32_e32 v113, v113\n\t"
        "v_exp_f32_e32 v114, v114\n\t"
        "v_exp_f32_e32 v115, v115\n\t"
        "v_mfma_f32_32x32x16_bf16 v[128:143], v[144:147], v[36:39], v[128:143]\n\t"
        "ds_read_b64 v[144:145], v45 offset:15552\n\t"
        "ds_read_b64 v[146:147], v45 offset:15808\n\t"
        "v_exp_f32_e32 v116, v116\n\t"
        "v_exp_f32_e32 v117, v117\n\t"
        "v_exp_f32_e32 v118, v118\n\t"
        "v_exp_f32_e32 v119, v119\n\t"
        "v_smfmac_f32_32x32x32_bf16 v[64:79], v[152:155], v[96:103], v44\n\t"
        "ds_read_b128 v[152:155], v47 offset:10368\n\t"
        "v_exp_f32_e32 v120, v120\n\t"
        "v_exp_f32_e32 v121, v121\n\t"
        "v_exp_f32_e32 v122, v122\n\t"
        "v_exp_f32_e32 v123, v123\n\t"
        "v_smfmac_f32_32x32x32_bf16 v[64:79], v[156:159], v[104:111], v44\n\t"
        "ds_read_b128 v[156:159], v31 offset:10368\n\t"
        "v_exp_f32_e32 v124, v124\n\t"
        "v_exp_f32_e32 v125, v125\n\t"
        "v_exp_f32_e32 v126, v126\n\t"
        "v_exp_f32_e32 v127, v127\n\t"
        "s_waitcnt lgkmcnt(2)\n\t"
        "v_mfma_f32_32x32x8_bf16 v[80:95], v[148:149], v[40:41], 0\n\t"
        "s_add_u32 m0, s75, 61440\n\t"
        "s_add_u32 s93, s74, 0\n\t"
        "buffer_load_dwordx4 v0, s[48:51], s93 offen lds\n\t"
        "v_exp_f32_e32 v128, v128\n\t"
        "v_exp_f32_e32 v129, v129\n\t"
        "v_exp_f32_e32 v130, v130\n\t"
        "v_exp_f32_e32 v131, v131\n\t"
        "v_mfma_f32_32x32x16_bf16 v[80:95], v[144:147], v[32:35], v[80:95]\n\t"
        "buffer_load_dwordx4 v1, s[48:51], s93 offen offset:1024 lds\n\t"
        "v_exp_f32_e32 v132, v132\n\t"
        "v_exp_f32_e32 v133, v133\n\t"
        "v_exp_f32_e32 v134, v134\n\t"
        "v_exp_f32_e32 v135, v135\n\t"
        "s_waitcnt lgkmcnt(0)\n\t"
        "v_smfmac_f32_32x32x32_bf16 v[48:63], v[152:155], v[112:119], v44\n\t"
        "buffer_load_dwordx4 v2, s[48:51], s93 offen offset:2048 lds\n\t"
        "v_exp_f32_e32 v136, v136\n\t"
        "v_exp_f32_e32 v137, v137\n\t"
        "v_exp_f32_e32 v138, v138\n\t"
        "v_exp_f32_e32 v139, v139\n\t"
        "v_smfmac_f32_32x32x32_bf16 v[48:63], v[156:159], v[120:127], v44\n\t"
        "buffer_load_dwordx4 v3, s[48:51], s93 offen offset:3072 lds\n\t"
        "v_exp_f32_e32 v140, v140\n\t"
        "v_exp_f32_e32 v141, v141\n\t"
        "v_exp_f32_e32 v142, v142\n\t"
        "v_exp_f32_e32 v143, v143\n\t"
        "s_setprio 1\n\t"
        "v_mfma_f32_32x32x8_bf16 v[96:111], v[148:149], v[42:43], 0\n\t"
        "ds_read_b64 v[148:149], v46 offset:20736\n\t"
        "v_exp_f32_e32 v80, v80\n\t"
        "v_exp_f32_e32 v81, v81\n\t"
        "v_exp_f32_e32 v82, v82\n\t"
        "v_exp_f32_e32 v83, v83\n\t"
        "v_mfma_f32_32x32x16_bf16 v[96:111], v[144:147], v[36:39], v[96:111]\n\t"
        "ds_read_b64 v[144:145], v45 offset:20736\n\t"
        "ds_read_b64 v[146:147], v45 offset:20992\n\t"
        "v_exp_f32_e32 v84, v84\n\t"
        "v_exp_f32_e32 v85, v85\n\t"
        "v_exp_f32_e32 v86, v86\n\t"
        "v_exp_f32_e32 v87, v87\n\t"
        "v_smfmac_f32_32x32x32_bf16 v[64:79], v[152:155], v[128:135], v44\n\t"
        "ds_read_b128 v[152:155], v47 offset:15552\n\t"
        "v_exp_f32_e32 v88, v88\n\t"
        "v_exp_f32_e32 v89, v89\n\t"
        "v_exp_f32_e32 v90, v90\n\t"
        "v_exp_f32_e32 v91, v91\n\t"
        "v_smfmac_f32_32x32x32_bf16 v[64:79], v[156:159], v[136:143], v44\n\t"
        "ds_read_b128 v[156:159], v31 offset:15552\n\t"
        "v_exp_f32_e32 v92, v92\n\t"
        "v_exp_f32_e32 v93, v93\n\t"
        "v_exp_f32_e32 v94, v94\n\t"
        "v_exp_f32_e32 v95, v95\n\t"
        "s_setprio 0\n\t"
        "s_waitcnt lgkmcnt(2)\n\t"
        "v_mfma_f32_32x32x8_bf16 v[112:127], v[148:149], v[40:41], 0\n\t"
        "s_add_u32 m0, s75, 65536\n\t"
        "s_add_u32 s93, s74, 0\n\t"
        "buffer_load_dwordx4 v4, s[48:51], s93 offen lds\n\t"
        "v_exp_f32_e32 v96, v96\n\t"
        "v_exp_f32_e32 v97, v97\n\t"
        "v_exp_f32_e32 v98, v98\n\t"
        "v_exp_f32_e32 v99, v99\n\t"
        "v_mfma_f32_32x32x16_bf16 v[112:127], v[144:147], v[32:35], v[112:127]\n\t"
        "buffer_load_dwordx4 v5, s[48:51], s93 offen offset:1024 lds\n\t"
        "v_exp_f32_e32 v100, v100\n\t"
        "v_exp_f32_e32 v101, v101\n\t"
        "v_exp_f32_e32 v102, v102\n\t"
        "v_exp_f32_e32 v103, v103\n\t"
        "s_waitcnt lgkmcnt(0)\n\t"
        "v_smfmac_f32_32x32x32_bf16 v[48:63], v[152:155], v[80:87], v44\n\t"
        "s_mov_b64 exec, 0xffff\n\t"
        "buffer_load_dwordx4 v6, s[48:51], s93 offen offset:2048 lds\n\t"
        "s_mov_b64 exec, -1\n\t"
        "v_exp_f32_e32 v104, v104\n\t"
        "v_exp_f32_e32 v105, v105\n\t"
        "v_exp_f32_e32 v106, v106\n\t"
        "v_exp_f32_e32 v107, v107\n\t"
        "v_smfmac_f32_32x32x32_bf16 v[48:63], v[156:159], v[88:95], v44\n\t"
        "v_exp_f32_e32 v108, v108\n\t"
        "v_exp_f32_e32 v109, v109\n\t"
        "v_exp_f32_e32 v110, v110\n\t"
        "v_exp_f32_e32 v111, v111\n\t"
        "s_waitcnt vmcnt(7)\n\t"
        "s_barrier\n\t"
        "s_setprio 3\n\t"
        "v_mfma_f32_32x32x8_bf16 v[128:143], v[148:149], v[42:43], 0\n\t"
        "v_exp_f32_e32 v112, v112\n\t"
        "v_exp_f32_e32 v113, v113\n\t"
        "v_exp_f32_e32 v114, v114\n\t"
        "v_exp_f32_e32 v115, v115\n\t"
        "v_mfma_f32_32x32x16_bf16 v[128:143], v[144:147], v[36:39], v[128:143]\n\t"
        "v_exp_f32_e32 v116, v116\n\t"
        "v_exp_f32_e32 v117, v117\n\t"
        "v_exp_f32_e32 v118, v118\n\t"
        "v_exp_f32_e32 v119, v119\n\t"
        "v_smfmac_f32_32x32x32_bf16 v[64:79], v[152:155], v[96:103], v44\n\t"
        "ds_read_b128 v[152:155], v47 offset:20736\n\t"
        "v_exp_f32_e32 v120, v120\n\t"
        "v_exp_f32_e32 v121, v121\n\t"
        "v_exp_f32_e32 v122, v122\n\t"
        "v_exp_f32_e32 v123, v123\n\t"
        "v_smfmac_f32_32x32x32_bf16 v[64:79], v[156:159], v[104:111], v44\n\t"
        "ds_read_b128 v[156:159], v31 offset:20736\n\t"
        "v_exp_f32_e32 v124, v124\n\t"
        "v_exp_f32_e32 v125, v125\n\t"
        "v_exp_f32_e32 v126, v126\n\t"
        "v_exp_f32_e32 v127, v127\n\t"
        "s_setprio 0\n\t"
        "v_exp_f32_e32 v128, v128\n\t"
        "v_exp_f32_e32 v129, v129\n\t"
        "s_waitcnt lgkmcnt(0)\n\t"
        "v_smfmac_f32_32x32x32_bf16 v[48:63], v[152:155], v[112:119], v44\n\t"
        "v_smfmac_f32_32x32x32_bf16 v[48:63], v[156:159], v[120:127], v44\n\t"
        "v_add_u32_e32 v28, s88, v150\n\t"
        "v_add_u32_e32 v151, s89, v150\n\t"
        "v_exp_f32_e32 v130, v130\n\t"
        "v_exp_f32_e32 v131, v131\n\t"
        "v_exp_f32_e32 v132, v132\n\t"
        "v_exp_f32_e32 v133, v133\n\t"
        "v_exp_f32_e32 v134, v134\n\t"
        "v_exp_f32_e32 v135, v135\n\t"
        "v_exp_f32_e32 v136, v136\n\t"
        "v_exp_f32_e32 v137, v137\n\t"
        "v_smfmac_f32_32x32x32_bf16 v[64:79], v[152:155], v[128:135], v44\n\t"
        "v_exp_f32_e32 v138, v138\n\t"
        "v_exp_f32_e32 v139, v139\n\t"
        "v_exp_f32_e32 v140, v140\n\t"
        "v_exp_f32_e32 v141, v141\n\t"
        "v_exp_f32_e32 v142, v142\n\t"
        "v_exp_f32_e32 v143, v143\n\t"
        "ds_write_b128 v28, v[48:51]\n\t"
        "ds_write_b128 v28, v[52:55] offset:1024\n\t"
        "v_smfmac_f32_32x32x32_bf16 v[64:79], v[156:159], v[136:143], v44\n\t"
        "ds_write_b128 v28, v[56:59] offset:2048\n\t"
        "s_waitcnt vmcnt(0) lgkmcnt(0)\n\t"
        "s_barrier\n\t"
        "ds_read_b128 v[80:83], v151 offset:0\n\t"
        "ds_read_b128 v[84:87], v151 offset:1024\n\t"
        "ds_read_b128 v[88:91], v151 offset:2048\n\t"
        "s_nop 3\n\t"
        "s_waitcnt lgkmcnt(0)\n\t"
        "s_barrier\n\t"
        "v_pk_add_f32 v[72:73], v[72:73], v[88:89]\n\t"
        "v_pk_add_f32 v[64:65], v[64:65], v[80:81]\n\t"
        "v_pk_add_f32 v[66:67], v[66:67], v[82:83]\n\t"
        "v_pk_add_f32 v[68:69], v[68:69], v[84:85]\n\t"
        "v_pk_add_f32 v[70:71], v[70:71], v[86:87]\n\t"
        "v_pk_add_f32 v[74:75], v[74:75], v[90:91]\n\t"
        "v_mov_b32_e32 v96, v72\n\t"
        "v_mov_b32_e32 v97, v72\n\t"
        "v_pk_mul_f32 v[98:99], v[64:65], 0\n\t"
        "v_pk_fma_f32 v[98:99], v[66:67], 0, v[98:99]\n\t"
        "v_pk_fma_f32 v[98:99], v[68:69], 0, v[98:99]\n\t"
        "v_pk_fma_f32 v[98:99], v[70:71], 0, v[98:99]\n\t"
        "v_pk_fma_f32 v[98:99], v[72:73], 0, v[98:99]\n\t"
        "v_pk_fma_f32 v[98:99], v[74:75], 0, v[98:99]\n\t"
        "v_permlane32_swap_b32_e32 v96, v97\n\t"
        "v_add_f32_e32 v98, v98, v99\n\t"
        "v_add_f32_e32 v98, v98, v97\n\t"
        "v_cmp_nlt_f32_e32 vcc, 0x1e3ce508, v98\n\t"
        "s_mov_b64 s[98:99], vcc\n\t"
        "v_cmp_ngt_f32_e32 vcc, 0x7149f2ca, v98\n\t"
        "s_or_b64 vcc, vcc, s[98:99]\n\t"
        "s_cbranch_vccnz .Lslow\n\t"
        ".Lfast:\n\t"
        "v_rcp_f32_e32 v96, v97\n\t"
        "s_nop 0\n\t"
        "v_mul_f32_e32 v96, v27, v96\n\t"
        "v_pk_mul_f32 v[64:65], v[64:65], v[96:97] op_sel_hi:[1,0]\n\t"
        "v_pk_mul_f32 v[66:67], v[66:67], v[96:97] op_sel_hi:[1,0]\n\t"
        "v_pk_mul_f32 v[68:69], v[68:69], v[96:97] op_sel_hi:[1,0]\n\t"
        "v_pk_mul_f32 v[70:71], v[70:71], v[96:97] op_sel_hi:[1,0]\n\t"
        "v_pk_mul_f32 v[72:73], v[72:73], v[96:97] op_sel_hi:[1,0]\n\t"
        "v_pk_mul_f32 v[74:75], v[74:75], v[96:97] op_sel_hi:[1,0]\n\t"
        "v_cvt_pk_bf16_f32 v80, v64, v65\n\t"
        "v_cvt_pk_bf16_f32 v81, v66, v67\n\t"
        "v_cvt_pk_bf16_f32 v82, v68, v69\n\t"
        "v_cvt_pk_bf16_f32 v83, v70, v71\n\t"
        "v_cvt_pk_bf16_f32 v84, v72, v73\n\t"
        "v_cvt_pk_bf16_f32 v85, v74, v75\n\t"
        "v_min_u32_e32 v80, -2, v80\n\t"
        "v_min_u32_e32 v81, -2, v81\n\t"
        "v_min_u32_e32 v82, -2, v82\n\t"
        "v_min_u32_e32 v83, -2, v83\n\t"
        "v_min_u32_e32 v84, -2, v84\n\t"
        "v_min_u32_e32 v85, -2, v85\n\t"
        "buffer_store_dwordx4 v[80:83], v11, s[40:43], s70 offen sc1\n\t"
        "s_mov_b32 exec_hi, 0\n\t"
        "buffer_store_dwordx2 v[84:85], v17, s[40:43], s71 offen sc1\n\t"
        "s_mov_b32 exec_hi, -1\n\t"
        "v_add_u32_e32 v28, 112640, v14\n\t"
        "v_add_u32_e32 v151, 112640, v15\n\t"
        "ds_read_b64_tr_b16 v[96:97], v28 offset:0\n\t"
        "ds_read_b64_tr_b16 v[98:99], v28 offset:1280\n\t"
        "ds_read_b64_tr_b16 v[100:101], v28 offset:32\n\t"
        "ds_read_b64_tr_b16 v[102:103], v28 offset:1312\n\t"
        "ds_read_b64_tr_b16 v[104:105], v28 offset:64\n\t"
        "ds_read_b64_tr_b16 v[106:107], v28 offset:1344\n\t"
        "ds_read_b64_tr_b16 v[108:109], v28 offset:96\n\t"
        "ds_read_b64_tr_b16 v[110:111], v28 offset:1376\n\t"
        "ds_read_b64_tr_b16 v[112:113], v28 offset:128\n\t"
        "ds_read_b64_tr_b16 v[114:115], v28 offset:1408\n\t"
        "ds_read_b64_tr_b16 v[116:117], v28 offset:6400\n\t"
        "ds_read_b64_tr_b16 v[118:119], v28 offset:7680\n\t"
        "ds_read_b64_tr_b16 v[120:121], v28 offset:6432\n\t"
        "ds_read_b64_tr_b16 v[122:123], v28 offset:7712\n\t"
        "s_setprio 3\n\t"
        "s_waitcnt lgkmcnt(12)\n\t"
        "v_mfma_f32_16x16x32_bf16 v[232:235], v[96:99], v[180:183], v[232:235]\n\t"
        "ds_read_b64_tr_b16 v[124:125], v28 offset:6464\n\t"
        "ds_read_b64_tr_b16 v[126:127], v28 offset:7744\n\t"
        "s_add_u32 m0, s75, 0\n\t"
        "s_add_u32 s93, s74, 409600\n\t"
        "buffer_load_dwordx4 v0, s[48:51], s93 offen lds\n\t"
        "s_waitcnt lgkmcnt(12)\n\t"
        "v_mfma_f32_16x16x32_bf16 v[236:239], v[100:103], v[180:183], v[236:239]\n\t"
        "ds_read_b64_tr_b16 v[96:97], v28 offset:6496\n\t"
        "ds_read_b64_tr_b16 v[98:99], v28 offset:7776\n\t"
        "s_waitcnt lgkmcnt(12)\n\t"
        "v_mfma_f32_16x16x32_bf16 v[240:243], v[104:107], v[180:183], v[240:243]\n\t"
        "ds_read_b64_tr_b16 v[100:101], v28 offset:6528\n\t"
        "ds_read_b64_tr_b16 v[102:103], v28 offset:7808\n\t"
        "s_waitcnt lgkmcnt(12)\n\t"
        "v_mfma_f32_16x16x32_bf16 v[244:247], v[108:111], v[180:183], v[244:247]\n\t"
        "ds_read_b64_tr_b16 v[104:105], v28 offset:12800\n\t"
        "ds_read_b64_tr_b16 v[106:107], v28 offset:14080\n\t"
        "s_waitcnt lgkmcnt(12)\n\t"
        "v_mfma_f32_16x16x32_bf16 v[248:251], v[112:115], v[180:183], v[248:251]\n\t"
        "ds_read_b64_tr_b16 v[108:109], v28 offset:12832\n\t"
        "ds_read_b64_tr_b16 v[110:111], v28 offset:14112\n\t"
        "s_waitcnt lgkmcnt(12)\n\t"
        "v_mfma_f32_16x16x32_bf16 v[232:235], v[116:119], v[184:187], v[232:235]\n\t"
        "ds_read_b64_tr_b16 v[112:113], v28 offset:12864\n\t"
        "ds_read_b64_tr_b16 v[114:115], v28 offset:14144\n\t"
        "s_waitcnt lgkmcnt(12)\n\t"
        "v_mfma_f32_16x16x32_bf16 v[236:239], v[120:123], v[184:187], v[236:239]\n\t"
        "ds_read_b64_tr_b16 v[116:117], v28 offset:12896\n\t"
        "ds_read_b64_tr_b16 v[118:119], v28 offset:14176\n\t"
        "s_waitcnt lgkmcnt(12)\n\t"
        "v_mfma_f32_16x16x32_bf16 v[240:243], v[124:127], v[184:187], v[240:243]\n\t"
        "ds_read_b64_tr_b16 v[120:121], v28 offset:12928\n\t"
        "ds_read_b64_tr_b16 v[122:123], v28 offset:14208\n\t"
        "s_setprio 2\n\t"
        "s_waitcnt lgkmcnt(12)\n\t"
        "v_mfma_f32_16x16x32_bf16 v[244:247], v[96:99], v[184:187], v[244:247]\n\t"
        "ds_read_b64_tr_b16 v[124:125], v28 offset:19200\n\t"
        "ds_read_b64_tr_b16 v[126:127], v28 offset:20480\n\t"
        "buffer_load_dwordx4 v1, s[48:51], s93 offen offset:1024 lds\n\t"
        "s_waitcnt lgkmcnt(12)\n\t"
        "v_mfma_f32_16x16x32_bf16 v[248:251], v[100:103], v[184:187], v[248:251]\n\t"
        "ds_read_b64_tr_b16 v[96:97], v28 offset:19232\n\t"
        "ds_read_b64_tr_b16 v[98:99], v28 offset:20512\n\t"
        "s_waitcnt lgkmcnt(12)\n\t"
        "v_mfma_f32_16x16x32_bf16 v[232:235], v[104:107], v[188:191], v[232:235]\n\t"
        "ds_read_b64_tr_b16 v[100:101], v28 offset:19264\n\t"
        "ds_read_b64_tr_b16 v[102:103], v28 offset:20544\n\t"
        "s_waitcnt lgkmcnt(12)\n\t"
        "v_mfma_f32_16x16x32_bf16 v[236:239], v[108:111], v[188:191], v[236:239]\n\t"
        "ds_read_b64_tr_b16 v[104:105], v28 offset:19296\n\t"
        "ds_read_b64_tr_b16 v[106:107], v28 offset:20576\n\t"
        "s_waitcnt lgkmcnt(12)\n\t"
        "v_mfma_f32_16x16x32_bf16 v[240:243], v[112:115], v[188:191], v[240:243]\n\t"
        "ds_read_b64_tr_b16 v[108:109], v28 offset:19328\n\t"
        "ds_read_b64_tr_b16 v[110:111], v28 offset:20608\n\t"
        "s_waitcnt lgkmcnt(12)\n\t"
        "v_mfma_f32_16x16x32_bf16 v[244:247], v[116:119], v[188:191], v[244:247]\n\t"
        "ds_read_b64_tr_b16 v[112:113], v151 offset:0\n\t"
        "ds_read_b64_tr_b16 v[114:115], v151 offset:12800\n\t"
        "s_waitcnt lgkmcnt(12)\n\t"
        "v_mfma_f32_16x16x32_bf16 v[248:251], v[120:123], v[188:191], v[248:251]\n\t"
        "ds_read_b64_tr_b16 v[116:117], v151 offset:32\n\t"
        "ds_read_b64_tr_b16 v[118:119], v151 offset:12832\n\t"
        "s_waitcnt lgkmcnt(12)\n\t"
        "v_mfma_f32_16x16x32_bf16 v[232:235], v[124:127], v[192:195], v[232:235]\n\t"
        "ds_read_b64_tr_b16 v[120:121], v151 offset:64\n\t"
        "ds_read_b64_tr_b16 v[122:123], v151 offset:12864\n\t"
        "s_setprio 1\n\t"
        "s_waitcnt lgkmcnt(12)\n\t"
        "v_mfma_f32_16x16x32_bf16 v[236:239], v[96:99], v[192:195], v[236:239]\n\t"
        "ds_read_b64_tr_b16 v[124:125], v151 offset:96\n\t"
        "ds_read_b64_tr_b16 v[126:127], v151 offset:12896\n\t"
        "buffer_load_dwordx4 v2, s[48:51], s93 offen offset:2048 lds\n\t"
        "s_waitcnt lgkmcnt(12)\n\t"
        "v_mfma_f32_16x16x32_bf16 v[240:243], v[100:103], v[192:195], v[240:243]\n\t"
        "ds_read_b64_tr_b16 v[96:97], v151 offset:128\n\t"
        "ds_read_b64_tr_b16 v[98:99], v151 offset:12928\n\t"
        "s_waitcnt lgkmcnt(12)\n\t"
        "v_mfma_f32_16x16x32_bf16 v[244:247], v[104:107], v[192:195], v[244:247]\n\t"
        "s_waitcnt lgkmcnt(10)\n\t"
        "v_mfma_f32_16x16x32_bf16 v[248:251], v[108:111], v[192:195], v[248:251]\n\t"
        "s_waitcnt lgkmcnt(8)\n\t"
        "v_mfma_f32_16x16x32_bf16 v[232:235], v[112:115], v[196:199], v[232:235]\n\t"
        "s_waitcnt lgkmcnt(6)\n\t"
        "v_mfma_f32_16x16x32_bf16 v[236:239], v[116:119], v[196:199], v[236:239]\n\t"
        "s_setprio 0\n\t"
        "s_waitcnt lgkmcnt(4)\n\t"
        "v_mfma_f32_16x16x32_bf16 v[240:243], v[120:123], v[196:199], v[240:243]\n\t"
        "s_waitcnt lgkmcnt(2)\n\t"
        "v_mfma_f32_16x16x32_bf16 v[244:247], v[124:127], v[196:199], v[244:247]\n\t"
        "s_waitcnt lgkmcnt(0)\n\t"
        "v_mfma_f32_16x16x32_bf16 v[248:251], v[96:99], v[196:199], v[248:251]\n\t"
        "v_add_u32_e32 v28, 61440, v14\n\t"
        "v_add_u32_e32 v151, 61440, v15\n\t"
        "buffer_load_dwordx4 v3, s[48:51], s93 offen offset:3072 lds\n\t"
        "ds_read_b64_tr_b16 v[32:33], v28 offset:0\n\t"
        "ds_read_b64_tr_b16 v[34:35], v28 offset:1280\n\t"
        "ds_read_b64_tr_b16 v[36:37], v28 offset:32\n\t"
        "ds_read_b64_tr_b16 v[38:39], v28 offset:1312\n\t"
        "ds_read_b64_tr_b16 v[40:41], v28 offset:64\n\t"
        "ds_read_b64_tr_b16 v[42:43], v28 offset:1344\n\t"
        "ds_read_b64_tr_b16 v[44:45], v28 offset:96\n\t"
        "ds_read_b64_tr_b16 v[46:47], v28 offset:1376\n\t"
        "ds_read_b64_tr_b16 v[48:49], v28 offset:128\n\t"
        "ds_read_b64_tr_b16 v[50:51], v28 offset:1408\n\t"
        "s_add_u32 m0, s75, 4096\n\t"
        "s_add_u32 s93, s74, 409600\n\t"
        "buffer_load_dwordx4 v4, s[48:51], s93 offen lds\n\t"
        "ds_read_b64_tr_b16 v[52:53], v28 offset:6400\n\t"
        "ds_read_b64_tr_b16 v[54:55], v28 offset:7680\n\t"
        "ds_read_b64_tr_b16 v[56:57], v28 offset:6432\n\t"
        "ds_read_b64_tr_b16 v[58:59], v28 offset:7712\n\t"
        "ds_read_b64_tr_b16 v[60:61], v28 offset:6464\n\t"
        "ds_read_b64_tr_b16 v[62:63], v28 offset:7744\n\t"
        "ds_read_b64_tr_b16 v[64:65], v28 offset:6496\n\t"
        "ds_read_b64_tr_b16 v[66:67], v28 offset:7776\n\t"
        "ds_read_b64_tr_b16 v[68:69], v28 offset:6528\n\t"
        "ds_read_b64_tr_b16 v[70:71], v28 offset:7808\n\t"
        "buffer_load_dwordx4 v5, s[48:51], s93 offen offset:1024 lds\n\t"
        "ds_read_b64_tr_b16 v[72:73], v28 offset:12800\n\t"
        "ds_read_b64_tr_b16 v[74:75], v28 offset:14080\n\t"
        "ds_read_b64_tr_b16 v[76:77], v28 offset:12832\n\t"
        "ds_read_b64_tr_b16 v[78:79], v28 offset:14112\n\t"
        "ds_read_b64_tr_b16 v[80:81], v28 offset:12864\n\t"
        "ds_read_b64_tr_b16 v[82:83], v28 offset:14144\n\t"
        "ds_read_b64_tr_b16 v[84:85], v28 offset:12896\n\t"
        "ds_read_b64_tr_b16 v[86:87], v28 offset:14176\n\t"
        "ds_read_b64_tr_b16 v[88:89], v28 offset:12928\n\t"
        "ds_read_b64_tr_b16 v[90:91], v28 offset:14208\n\t"
        "s_mov_b64 exec, 0xffff\n\t"
        "buffer_load_dwordx4 v6, s[48:51], s93 offen offset:2048 lds\n\t"
        "s_mov_b64 exec, -1\n\t"
        "ds_read_b64_tr_b16 v[92:93], v28 offset:19200\n\t"
        "ds_read_b64_tr_b16 v[94:95], v28 offset:20480\n\t"
        "ds_read_b64_tr_b16 v[96:97], v28 offset:19232\n\t"
        "ds_read_b64_tr_b16 v[98:99], v28 offset:20512\n\t"
        "ds_read_b64_tr_b16 v[100:101], v28 offset:19264\n\t"
        "ds_read_b64_tr_b16 v[102:103], v28 offset:20544\n\t"
        "ds_read_b64_tr_b16 v[104:105], v28 offset:19296\n\t"
        "ds_read_b64_tr_b16 v[106:107], v28 offset:20576\n\t"
        "ds_read_b64_tr_b16 v[108:109], v28 offset:19328\n\t"
        "ds_read_b64_tr_b16 v[110:111], v28 offset:20608\n\t"
        "ds_read_b64_tr_b16 v[112:113], v151 offset:0\n\t"
        "ds_read_b64_tr_b16 v[114:115], v151 offset:12800\n\t"
        "ds_read_b64_tr_b16 v[116:117], v151 offset:32\n\t"
        "ds_read_b64_tr_b16 v[118:119], v151 offset:12832\n\t"
        "ds_read_b64_tr_b16 v[120:121], v151 offset:64\n\t"
        "ds_read_b64_tr_b16 v[122:123], v151 offset:12864\n\t"
        "ds_read_b64_tr_b16 v[124:125], v151 offset:96\n\t"
        "ds_read_b64_tr_b16 v[126:127], v151 offset:12896\n\t"
        "ds_read_b64_tr_b16 v[128:129], v151 offset:128\n\t"
        "ds_read_b64_tr_b16 v[130:131], v151 offset:12928\n\t"
        "s_mov_b32 s94, 0x80000\n\t"
        "s_waitcnt vmcnt(0) lgkmcnt(0)\n\t"
        "s_barrier\n\t"
        "s_add_u32 s93, s72, 2621440\n\t"
        "buffer_load_dwordx4 v[160:163], v12, s[40:43], s93 offen sc1\n\t"
        "s_add_u32 s93, s72, 2641920\n\t"
        "buffer_load_dwordx4 v[164:167], v12, s[40:43], s93 offen sc1\n\t"
        "s_add_u32 s93, s72, 2662400\n\t"
        "buffer_load_dwordx4 v[168:171], v12, s[40:43], s93 offen sc1\n\t"
        "s_add_u32 s93, s72, 2682880\n\t"
        "buffer_load_dwordx4 v[172:175], v12, s[40:43], s93 offen sc1\n\t"
        "s_add_u32 s93, s73, 2621440\n\t"
        "buffer_load_dwordx2 v[176:177], v13, s[40:43], s93 offen sc1\n\t"
        "s_add_u32 s93, s73, 2662400\n\t"
        "buffer_load_dwordx2 v[178:179], v13, s[40:43], s93 offen sc1\n\t"
        "s_add_u32 s93, s72, 2785280\n\t"
        "buffer_load_dwordx4 v[180:183], v12, s[40:43], s93 offen sc1\n\t"
        "s_add_u32 s93, s72, 2805760\n\t"
        "buffer_load_dwordx4 v[184:187], v12, s[40:43], s93 offen sc1\n\t"
        "s_add_u32 s93, s72, 2826240\n\t"
        "buffer_load_dwordx4 v[188:191], v12, s[40:43], s93 offen sc1\n\t"
        "s_add_u32 s93, s72, 2846720\n\t"
        "buffer_load_dwordx4 v[192:195], v12, s[40:43], s93 offen sc1\n\t"
        "s_add_u32 s93, s73, 2785280\n\t"
        "buffer_load_dwordx2 v[196:197], v13, s[40:43], s93 offen sc1\n\t"
        "s_add_u32 s93, s73, 2826240\n\t"
        "buffer_load_dwordx2 v[198:199], v13, s[40:43], s93 offen sc1\n\t"
        ".Lc0:\n\t"
        "s_waitcnt vmcnt(11)\n\t"
        "v_max3_u32 v25, v160, v161, v162\n\t"
        "v_max_u32_e32 v25, v25, v163\n\t"
        "v_cmp_eq_u32_e32 vcc, -1, v25\n\t"
        "s_cbranch_vccnz .Lr0\n\t"
        ".Lk0:\n\t"
        "s_setprio 3\n\t"
        "v_mfma_f32_16x16x32_bf16 v[232:235], v[32:35], v[160:163], v[232:235]\n\t"
        "ds_read_b64_tr_b16 v[32:33], v14 offset:0\n\t"
        "ds_read_b64_tr_b16 v[34:35], v14 offset:1280\n\t"
        "v_mfma_f32_16x16x32_bf16 v[236:239], v[36:39], v[160:163], v[236:239]\n\t"
        "ds_read_b64_tr_b16 v[36:37], v14 offset:32\n\t"
        "ds_read_b64_tr_b16 v[38:39], v14 offset:1312\n\t"
        "v_mfma_f32_16x16x32_bf16 v[240:243], v[40:43], v[160:163], v[240:243]\n\t"
        "ds_read_b64_tr_b16 v[40:41], v14 offset:64\n\t"
        "ds_read_b64_tr_b16 v[42:43], v14 offset:1344\n\t"
        "v_mfma_f32_16x16x32_bf16 v[244:247], v[44:47], v[160:163], v[244:247]\n\t"
        "ds_read_b64_tr_b16 v[44:45], v14 offset:96\n\t"
        "ds_read_b64_tr_b16 v[46:47], v14 offset:1376\n\t"
        "v_mfma_f32_16x16x32_bf16 v[248:251], v[48:51], v[160:163], v[248:251]\n\t"
        "ds_read_b64_tr_b16 v[48:49], v14 offset:128\n\t"
        "ds_read_b64_tr_b16 v[50:51], v14 offset:1408\n\t"
        ".Lc1:\n\t"
        "s_waitcnt vmcnt(10)\n\t"
        "v_max3_u32 v25, v164, v165, v166\n\t"
        "v_max_u32_e32 v25, v25, v167\n\t"
        "v_cmp_eq_u32_e32 vcc, -1, v25\n\t"
        "s_cbranch_vccnz .Lr1\n\t"
        ".Lk1:\n\t"
        "v_mfma_f32_16x16x32_bf16 v[232:235], v[52:55], v[164:167], v[232:235]\n\t"
        "ds_read_b64_tr_b16 v[52:53], v14 offset:6400\n\t"
        "ds_read_b64_tr_b16 v[54:55], v14 offset:7680\n\t"
        "v_mfma_f32_16x16x32_bf16 v[236:239], v[56:59], v[164:167], v[236:239]\n\t"
        "ds_read_b64_tr_b16 v[56:57], v14 offset:6432\n\t"
        "ds_read_b64_tr_b16 v[58:59], v14 offset:7712\n\t"
        "v_mfma_f32_16x16x32_bf16 v[240:243], v[60:63], v[164:167], v[240:243]\n\t"
        "ds_read_b64_tr_b16 v[60:61], v14 offset:6464\n\t"
        "ds_read_b64_tr_b16 v[62:63], v14 offset:7744\n\t"
        "v_mfma_f32_16x16x32_bf16 v[244:247], v[64:67], v[164:167], v[244:247]\n\t"
        "ds_read_b64_tr_b16 v[64:65], v14 offset:6496\n\t"
        "ds_read_b64_tr_b16 v[66:67], v14 offset:7776\n\t"
        "v_mfma_f32_16x16x32_bf16 v[248:251], v[68:71], v[164:167], v[248:251]\n\t"
        "ds_read_b64_tr_b16 v[68:69], v14 offset:6528\n\t"
        "ds_read_b64_tr_b16 v[70:71], v14 offset:7808\n\t"
        ".Lc2:\n\t"
        "s_waitcnt vmcnt(9)\n\t"
        "v_max3_u32 v25, v168, v169, v170\n\t"
        "v_max_u32_e32 v25, v25, v171\n\t"
        "v_cmp_eq_u32_e32 vcc, -1, v25\n\t"
        "s_cbranch_vccnz .Lr2\n\t"
        ".Lk2:\n\t"
        "v_mfma_f32_16x16x32_bf16 v[232:235], v[72:75], v[168:171], v[232:235]\n\t"
        "ds_read_b64_tr_b16 v[72:73], v14 offset:12800\n\t"
        "ds_read_b64_tr_b16 v[74:75], v14 offset:14080\n\t"
        "v_mfma_f32_16x16x32_bf16 v[236:239], v[76:79], v[168:171], v[236:239]\n\t"
        "ds_read_b64_tr_b16 v[76:77], v14 offset:12832\n\t"
        "ds_read_b64_tr_b16 v[78:79], v14 offset:14112\n\t"
        "v_mfma_f32_16x16x32_bf16 v[240:243], v[80:83], v[168:171], v[240:243]\n\t"
        "ds_read_b64_tr_b16 v[80:81], v14 offset:12864\n\t"
        "ds_read_b64_tr_b16 v[82:83], v14 offset:14144\n\t"
        "v_mfma_f32_16x16x32_bf16 v[244:247], v[84:87], v[168:171], v[244:247]\n\t"
        "ds_read_b64_tr_b16 v[84:85], v14 offset:12896\n\t"
        "ds_read_b64_tr_b16 v[86:87], v14 offset:14176\n\t"
        "v_mfma_f32_16x16x32_bf16 v[248:251], v[88:91], v[168:171], v[248:251]\n\t"
        "ds_read_b64_tr_b16 v[88:89], v14 offset:12928\n\t"
        "ds_read_b64_tr_b16 v[90:91], v14 offset:14208\n\t"
        ".Lc3:\n\t"
        "s_waitcnt vmcnt(8)\n\t"
        "v_max3_u32 v25, v172, v173, v174\n\t"
        "v_max_u32_e32 v25, v25, v175\n\t"
        "v_cmp_eq_u32_e32 vcc, -1, v25\n\t"
        "s_cbranch_vccnz .Lr3\n\t"
        ".Lk3:\n\t"
        "s_setprio 2\n\t"
        "v_mfma_f32_16x16x32_bf16 v[232:235], v[92:95], v[172:175], v[232:235]\n\t"
        "ds_read_b64_tr_b16 v[92:93], v14 offset:19200\n\t"
        "ds_read_b64_tr_b16 v[94:95], v14 offset:20480\n\t"
        "v_mfma_f32_16x16x32_bf16 v[236:239], v[96:99], v[172:175], v[236:239]\n\t"
        "ds_read_b64_tr_b16 v[96:97], v14 offset:19232\n\t"
        "ds_read_b64_tr_b16 v[98:99], v14 offset:20512\n\t"
        "v_mfma_f32_16x16x32_bf16 v[240:243], v[100:103], v[172:175], v[240:243]\n\t"
        "ds_read_b64_tr_b16 v[100:101], v14 offset:19264\n\t"
        "ds_read_b64_tr_b16 v[102:103], v14 offset:20544\n\t"
        "v_mfma_f32_16x16x32_bf16 v[244:247], v[104:107], v[172:175], v[244:247]\n\t"
        "ds_read_b64_tr_b16 v[104:105], v14 offset:19296\n\t"
        "ds_read_b64_tr_b16 v[106:107], v14 offset:20576\n\t"
        "v_mfma_f32_16x16x32_bf16 v[248:251], v[108:111], v[172:175], v[248:251]\n\t"
        "ds_read_b64_tr_b16 v[108:109], v14 offset:19328\n\t"
        "ds_read_b64_tr_b16 v[110:111], v14 offset:20608\n\t"
        ".Lc4:\n\t"
        "s_waitcnt vmcnt(6)\n\t"
        "v_max3_u32 v25, v176, v177, v178\n\t"
        "v_max_u32_e32 v25, v25, v179\n\t"
        "v_cmp_eq_u32_e32 vcc, -1, v25\n\t"
        "s_cbranch_vccnz .Lr4\n\t"
        ".Lk4:\n\t"
        "v_mfma_f32_16x16x32_bf16 v[232:235], v[112:115], v[176:179], v[232:235]\n\t"
        "ds_read_b64_tr_b16 v[112:113], v15 offset:0\n\t"
        "ds_read_b64_tr_b16 v[114:115], v15 offset:12800\n\t"
        "v_mfma_f32_16x16x32_bf16 v[236:239], v[116:119], v[176:179], v[236:239]\n\t"
        "ds_read_b64_tr_b16 v[116:117], v15 offset:32\n\t"
        "ds_read_b64_tr_b16 v[118:119], v15 offset:12832\n\t"
        "v_mfma_f32_16x16x32_bf16 v[240:243], v[120:123], v[176:179], v[240:243]\n\t"
        "ds_read_b64_tr_b16 v[120:121], v15 offset:64\n\t"
        "ds_read_b64_tr_b16 v[122:123], v15 offset:12864\n\t"
        "v_mfma_f32_16x16x32_bf16 v[244:247], v[124:127], v[176:179], v[244:247]\n\t"
        "ds_read_b64_tr_b16 v[124:125], v15 offset:96\n\t"
        "ds_read_b64_tr_b16 v[126:127], v15 offset:12896\n\t"
        "v_mfma_f32_16x16x32_bf16 v[248:251], v[128:131], v[176:179], v[248:251]\n\t"
        "ds_read_b64_tr_b16 v[128:129], v15 offset:128\n\t"
        "ds_read_b64_tr_b16 v[130:131], v15 offset:12928\n\t"
        ".Lc5:\n\t"
        "s_waitcnt vmcnt(5) lgkmcnt(8)\n\t"
        "v_max3_u32 v25, v180, v181, v182\n\t"
        "v_max_u32_e32 v25, v25, v183\n\t"
        "v_cmp_eq_u32_e32 vcc, -1, v25\n\t"
        "s_cbranch_vccnz .Lr5\n\t"
        ".Lk5:\n\t"
        "v_mfma_f32_16x16x32_bf16 v[232:235], v[32:35], v[180:183], v[232:235]\n\t"
        "v_mfma_f32_16x16x32_bf16 v[236:239], v[36:39], v[180:183], v[236:239]\n\t"
        "v_mfma_f32_16x16x32_bf16 v[240:243], v[40:43], v[180:183], v[240:243]\n\t"
        "v_mfma_f32_16x16x32_bf16 v[244:247], v[44:47], v[180:183], v[244:247]\n\t"
        "v_mfma_f32_16x16x32_bf16 v[248:251], v[48:51], v[180:183], v[248:251]\n\t"
        ".Lc6:\n\t"
        "s_waitcnt vmcnt(4)\n\t"
        "v_max3_u32 v25, v184, v185, v186\n\t"
        "v_max_u32_e32 v25, v25, v187\n\t"
        "v_cmp_eq_u32_e32 vcc, -1, v25\n\t"
        "s_cbranch_vccnz .Lr6\n\t"
        ".Lk6:\n\t"
        "s_setprio 1\n\t"
        "v_mfma_f32_16x16x32_bf16 v[232:235], v[52:55], v[184:187], v[232:235]\n\t"
        "v_mfma_f32_16x16x32_bf16 v[236:239], v[56:59], v[184:187], v[236:239]\n\t"
        "v_mfma_f32_16x16x32_bf16 v[240:243], v[60:63], v[184:187], v[240:243]\n\t"
        "v_mfma_f32_16x16x32_bf16 v[244:247], v[64:67], v[184:187], v[244:247]\n\t"
        "v_mfma_f32_16x16x32_bf16 v[248:251], v[68:71], v[184:187], v[248:251]\n\t"
        ".Lc7:\n\t"
        "s_waitcnt vmcnt(3)\n\t"
        "v_max3_u32 v25, v188, v189, v190\n\t"
        "v_max_u32_e32 v25, v25, v191\n\t"
        "v_cmp_eq_u32_e32 vcc, -1, v25\n\t"
        "s_cbranch_vccnz .Lr7\n\t"
        ".Lk7:\n\t"
        "v_mfma_f32_16x16x32_bf16 v[232:235], v[72:75], v[188:191], v[232:235]\n\t"
        "v_mfma_f32_16x16x32_bf16 v[236:239], v[76:79], v[188:191], v[236:239]\n\t"
        "v_mfma_f32_16x16x32_bf16 v[240:243], v[80:83], v[188:191], v[240:243]\n\t"
        "v_mfma_f32_16x16x32_bf16 v[244:247], v[84:87], v[188:191], v[244:247]\n\t"
        "v_mfma_f32_16x16x32_bf16 v[248:251], v[88:91], v[188:191], v[248:251]\n\t"
        ".Lc8:\n\t"
        "s_waitcnt vmcnt(2) lgkmcnt(0)\n\t"
        "v_max3_u32 v25, v192, v193, v194\n\t"
        "v_max_u32_e32 v25, v25, v195\n\t"
        "v_cmp_eq_u32_e32 vcc, -1, v25\n\t"
        "s_cbranch_vccnz .Lr8\n\t"
        ".Lk8:\n\t"
        "v_mfma_f32_16x16x32_bf16 v[232:235], v[92:95], v[192:195], v[232:235]\n\t"
        "v_mfma_f32_16x16x32_bf16 v[236:239], v[96:99], v[192:195], v[236:239]\n\t"
        "v_mfma_f32_16x16x32_bf16 v[240:243], v[100:103], v[192:195], v[240:243]\n\t"
        "v_mfma_f32_16x16x32_bf16 v[244:247], v[104:107], v[192:195], v[244:247]\n\t"
        "v_mfma_f32_16x16x32_bf16 v[248:251], v[108:111], v[192:195], v[248:251]\n\t"
        ".Lc9:\n\t"
        "s_waitcnt vmcnt(0)\n\t"
        "v_max3_u32 v25, v196, v197, v198\n\t"
        "v_max_u32_e32 v25, v25, v199\n\t"
        "v_cmp_eq_u32_e32 vcc, -1, v25\n\t"
        "s_cbranch_vccnz .Lr9\n\t"
        ".Lk9:\n\t"
        "s_setprio 0\n\t"
        "v_mfma_f32_16x16x32_bf16 v[232:235], v[112:115], v[196:199], v[232:235]\n\t"
        "v_mfma_f32_16x16x32_bf16 v[236:239], v[116:119], v[196:199], v[236:239]\n\t"
        "v_mfma_f32_16x16x32_bf16 v[240:243], v[120:123], v[196:199], v[240:243]\n\t"
        "v_mfma_f32_16x16x32_bf16 v[244:247], v[124:127], v[196:199], v[244:247]\n\t"
        "v_mfma_f32_16x16x32_bf16 v[248:251], v[128:131], v[196:199], v[248:251]\n\t"
        "s_add_u32 s92, s90, 112640\n\t"
        "v_add_u32_e32 v28, s92, v150\n\t"
        "s_cmp_lg_u32 s59, 0\n\t"
        "s_nop 4\n\t"
        "s_cbranch_scc1 .Le1\n\t"
        "ds_write_b128 v28, v[244:247] offset:3072\n\t"
        "ds_write_b128 v28, v[248:251] offset:4096\n\t"
        "s_waitcnt lgkmcnt(0)\n\t"
        "s_barrier\n\t"
        "ds_read_b128 v[32:35], v28 offset:0\n\t"
        "ds_read_b128 v[36:39], v28 offset:1024\n\t"
        "ds_read_b128 v[40:43], v28 offset:2048\n\t"
        "s_waitcnt lgkmcnt(2)\n\t"
        "v_pk_add_f32 v[32:33], v[32:33], v[232:233]\n\t"
        "v_pk_add_f32 v[34:35], v[34:35], v[234:235]\n\t"
        "v_cvt_pk_bf16_f32 v32, v32, v33\n\t"
        "v_cvt_pk_bf16_f32 v33, v34, v35\n\t"
        "buffer_store_dwordx2 v[32:33], v19, s[52:55], s82 offen\n\t"
        "s_waitcnt lgkmcnt(1)\n\t"
        "v_pk_add_f32 v[36:37], v[36:37], v[236:237]\n\t"
        "v_pk_add_f32 v[38:39], v[38:39], v[238:239]\n\t"
        "v_cvt_pk_bf16_f32 v36, v36, v37\n\t"
        "v_cvt_pk_bf16_f32 v37, v38, v39\n\t"
        "buffer_store_dwordx2 v[36:37], v19, s[52:55], s82 offen offset:32\n\t"
        "s_waitcnt lgkmcnt(0)\n\t"
        "v_pk_add_f32 v[40:41], v[40:41], v[240:241]\n\t"
        "v_pk_add_f32 v[42:43], v[42:43], v[242:243]\n\t"
        "v_cvt_pk_bf16_f32 v40, v40, v41\n\t"
        "v_cvt_pk_bf16_f32 v41, v42, v43\n\t"
        "buffer_store_dwordx2 v[40:41], v19, s[52:55], s82 offen offset:64\n\t"
        "s_cmp_lg_u32 s91, 0\n\t"
        "s_cbranch_scc1 .Lf19\n\t"
        "s_mov_b32 exec_hi, 0\n\t"
        "buffer_store_dword v24, v22, s[40:43], 0 offen sc1\n\t"
        "s_mov_b32 exec_hi, -1\n\t"
        ".Lf19:\n\t"
        "s_branch .Lend\n\t"
        ".Le1:\n\t"
        "ds_write_b128 v28, v[232:235] offset:0\n\t"
        "ds_write_b128 v28, v[236:239] offset:1024\n\t"
        "ds_write_b128 v28, v[240:243] offset:2048\n\t"
        "s_waitcnt lgkmcnt(0)\n\t"
        "s_barrier\n\t"
        "ds_read_b128 v[44:47], v28 offset:3072\n\t"
        "ds_read_b128 v[48:51], v28 offset:4096\n\t"
        "s_waitcnt lgkmcnt(1)\n\t"
        "v_pk_add_f32 v[44:45], v[44:45], v[244:245]\n\t"
        "v_pk_add_f32 v[46:47], v[46:47], v[246:247]\n\t"
        "v_cvt_pk_bf16_f32 v44, v44, v45\n\t"
        "v_cvt_pk_bf16_f32 v45, v46, v47\n\t"
        "buffer_store_dwordx2 v[44:45], v19, s[52:55], s82 offen offset:96\n\t"
        "s_waitcnt lgkmcnt(0)\n\t"
        "v_pk_add_f32 v[48:49], v[48:49], v[248:249]\n\t"
        "v_pk_add_f32 v[50:51], v[50:51], v[250:251]\n\t"
        "v_cvt_pk_bf16_f32 v48, v48, v49\n\t"
        "v_cvt_pk_bf16_f32 v49, v50, v51\n\t"
        "buffer_store_dwordx2 v[48:49], v19, s[52:55], s82 offen offset:128\n\t"
        "s_branch .Lend\n\t"
        ".Lr0:\n\t"
        "s_sub_u32 s94, s94, 1\n\t"
        "s_cbranch_scc1 .Lx0\n\t"
        "s_call_b64 s[98:99], .Ld0\n\t"
        "s_branch .Lc0\n\t"
        ".Lx0:\n\t"
        "s_mov_b32 s94, 0\n\t"
        "s_branch .Lk0\n\t"
        ".Lr1:\n\t"
        "s_sub_u32 s94, s94, 1\n\t"
        "s_cbranch_scc1 .Lx1\n\t"
        "s_call_b64 s[98:99], .Ld1\n\t"
        "s_branch .Lc1\n\t"
        ".Lx1:\n\t"
        "s_mov_b32 s94, 0\n\t"
        "s_branch .Lk1\n\t"
        ".Lr2:\n\t"
        "s_sub_u32 s94, s94, 1\n\t"
        "s_cbranch_scc1 .Lx2\n\t"
        "s_call_b64 s[98:99], .Ld2\n\t"
        "s_branch .Lc2\n\t"
        ".Lx2:\n\t"
        "s_mov_b32 s94, 0\n\t"
        "s_branch .Lk2\n\t"
        ".Lr3:\n\t"
        "s_sub_u32 s94, s94, 1\n\t"
        "s_cbranch_scc1 .Lx3\n\t"
        "s_call_b64 s[98:99], .Ld3\n\t"
        "s_branch .Lc3\n\t"
        ".Lx3:\n\t"
        "s_mov_b32 s94, 0\n\t"
        "s_branch .Lk3\n\t"
        ".Lr4:\n\t"
        "s_sub_u32 s94, s94, 1\n\t"
        "s_cbranch_scc1 .Lx4\n\t"
        "s_call_b64 s[98:99], .Ld4\n\t"
        "s_branch .Lc4\n\t"
        ".Lx4:\n\t"
        "s_mov_b32 s94, 0\n\t"
        "s_branch .Lk4\n\t"
        ".Lr5:\n\t"
        "s_sub_u32 s94, s94, 1\n\t"
        "s_cbranch_scc1 .Lx5\n\t"
        "s_call_b64 s[98:99], .Ld6\n\t"
        "s_branch .Lc5\n\t"
        ".Lx5:\n\t"
        "s_mov_b32 s94, 0\n\t"
        "s_branch .Lk5\n\t"
        ".Lr6:\n\t"
        "s_sub_u32 s94, s94, 1\n\t"
        "s_cbranch_scc1 .Lx6\n\t"
        "s_call_b64 s[98:99], .Ld7\n\t"
        "s_branch .Lc6\n\t"
        ".Lx6:\n\t"
        "s_mov_b32 s94, 0\n\t"
        "s_branch .Lk6\n\t"
        ".Lr7:\n\t"
        "s_sub_u32 s94, s94, 1\n\t"
        "s_cbranch_scc1 .Lx7\n\t"
        "s_call_b64 s[98:99], .Ld8\n\t"
        "s_branch .Lc7\n\t"
        ".Lx7:\n\t"
        "s_mov_b32 s94, 0\n\t"
        "s_branch .Lk7\n\t"
        ".Lr8:\n\t"
        "s_sub_u32 s94, s94, 1\n\t"
        "s_cbranch_scc1 .Lx8\n\t"
        "s_call_b64 s[98:99], .Ld9\n\t"
        "s_branch .Lc8\n\t"
        ".Lx8:\n\t"
        "s_mov_b32 s94, 0\n\t"
        "s_branch .Lk8\n\t"
        ".Lr9:\n\t"
        "s_sub_u32 s94, s94, 1\n\t"
        "s_cbranch_scc1 .Lx9\n\t"
        "s_call_b64 s[98:99], .Ld10\n\t"
        "s_branch .Lc9\n\t"
        ".Lx9:\n\t"
        "s_mov_b32 s94, 0\n\t"
        "s_branch .Lk9\n\t"
        ".Ld0:\n\t"
        "s_add_u32 s93, s72, 2621440\n\t"
        "buffer_load_dwordx4 v[160:163], v12, s[40:43], s93 offen sc1\n\t"
        ".Ld1:\n\t"
        "s_add_u32 s93, s72, 2641920\n\t"
        "buffer_load_dwordx4 v[164:167], v12, s[40:43], s93 offen sc1\n\t"
        ".Ld2:\n\t"
        "s_add_u32 s93, s72, 2662400\n\t"
        "buffer_load_dwordx4 v[168:171], v12, s[40:43], s93 offen sc1\n\t"
        ".Ld3:\n\t"
        "s_add_u32 s93, s72, 2682880\n\t"
        "buffer_load_dwordx4 v[172:175], v12, s[40:43], s93 offen sc1\n\t"
        ".Ld4:\n\t"
        "s_add_u32 s93, s73, 2621440\n\t"
        "buffer_load_dwordx2 v[176:177], v13, s[40:43], s93 offen sc1\n\t"
        ".Ld5:\n\t"
        "s_add_u32 s93, s73, 2662400\n\t"
        "buffer_load_dwordx2 v[178:179], v13, s[40:43], s93 offen sc1\n\t"
        ".Ld6:\n\t"
        "s_add_u32 s93, s72, 2785280\n\t"
        "buffer_load_dwordx4 v[180:183], v12, s[40:43], s93 offen sc1\n\t"
        ".Ld7:\n\t"
        "s_add_u32 s93, s72, 2805760\n\t"
        "buffer_load_dwordx4 v[184:187], v12, s[40:43], s93 offen sc1\n\t"
        ".Ld8:\n\t"
        "s_add_u32 s93, s72, 2826240\n\t"
        "buffer_load_dwordx4 v[188:191], v12, s[40:43], s93 offen sc1\n\t"
        ".Ld9:\n\t"
        "s_add_u32 s93, s72, 2846720\n\t"
        "buffer_load_dwordx4 v[192:195], v12, s[40:43], s93 offen sc1\n\t"
        ".Ld10:\n\t"
        "s_add_u32 s93, s73, 2785280\n\t"
        "buffer_load_dwordx2 v[196:197], v13, s[40:43], s93 offen sc1\n\t"
        ".Ld11:\n\t"
        "s_add_u32 s93, s73, 2826240\n\t"
        "buffer_load_dwordx2 v[198:199], v13, s[40:43], s93 offen sc1\n\t"
        "s_setpc_b64 s[98:99]\n\t"
        ".Lslow:\n\t"
        "s_mov_b32 exec_lo, 0\n\t"
        "v_mov_b64_e32 v[42:43], 0\n\t"
        "s_mov_b32 exec_lo, -1\n\t"
        "v_mov_b32_e32 v114, 0xff800000\n\t"
        "v_mov_b64_e32 v[116:117], 0\n\t"
        "s_mov_b32 exec_lo, 0x100000\n\t"
        "s_mov_b32 exec_hi, 0x100000\n\t"
        "v_mov_b32_e32 v116, 0x3f803f80\n\t"
        "v_mov_b32_e32 v117, 0x3f803f80\n\t"
        "s_mov_b64 exec, -1\n\t"
        "s_mov_b32 s92, s77\n\t"
        "s_mov_b32 s94, 63\n\t"
        ".Lb1:\n\t"
        "buffer_load_dwordx2 v[144:145], v16, s[40:43], s92 offen sc1\n\t"
        "buffer_load_dwordx2 v[146:147], v16, s[40:43], s92 offen offset:256 sc1\n\t"
        "buffer_load_dwordx2 v[148:149], v17, s[40:43], s92 offen sc1\n\t"
        "s_waitcnt vmcnt(0)\n\t"
        "v_mfma_f32_32x32x8_bf16 v[96:111], v[148:149], v[42:43], 0\n\t"
        "v_mfma_f32_32x32x16_bf16 v[96:111], v[144:147], v[36:39], v[96:111]\n\t"
        "s_nop 15\n\t"
        "v_max3_f32 v112, v96, v97, v98\n\t"
        "v_max3_f32 v113, v99, v100, v101\n\t"
        "v_max3_f32 v112, v112, v102, v103\n\t"
        "v_max3_f32 v113, v113, v104, v105\n\t"
        "v_max3_f32 v112, v112, v106, v107\n\t"
        "v_max3_f32 v113, v113, v108, v109\n\t"
        "v_max3_f32 v112, v112, v110, v111\n\t"
        "v_max_f32_e32 v112, v112, v113\n\t"
        "v_max_f32_e32 v114, v114, v112\n\t"
        "s_add_u32 s92, s92, 2560\n\t"
        "s_sub_u32 s94, s94, 1\n\t"
        "s_cbranch_scc0 .Lb1\n\t"
        "v_mov_b32_e32 v115, v114\n\t"
        "s_nop 1\n\t"
        "v_permlane32_swap_b32_e32 v114, v115\n\t"
        "v_max_f32_e32 v114, v114, v115\n\t"
        "v_mov_b64_e32 v[64:65], 0\n\t"
        "v_mov_b64_e32 v[66:67], 0\n\t"
        "v_mov_b64_e32 v[68:69], 0\n\t"
        "v_mov_b64_e32 v[70:71], 0\n\t"
        "v_mov_b64_e32 v[72:73], 0\n\t"
        "v_mov_b64_e32 v[74:75], 0\n\t"
        "v_mov_b64_e32 v[76:77], 0\n\t"
        "v_mov_b64_e32 v[78:79], 0\n\t"
        "s_mov_b32 s92, s77\n\t"
        "s_mov_b32 s94, 63\n\t"
        ".Lb2:\n\t"
        "buffer_load_dwordx4 v[152:155], v10, s[40:43], s92 offen sc1\n\t"
        "buffer_load_dwordx4 v[156:159], v10, s[40:43], s92 offen offset:640 sc1\n\t"
        "buffer_load_dwordx2 v[144:145], v16, s[40:43], s92 offen sc1\n\t"
        "buffer_load_dwordx2 v[146:147], v16, s[40:43], s92 offen offset:256 sc1\n\t"
        "buffer_load_dwordx2 v[148:149], v17, s[40:43], s92 offen sc1\n\t"
        "s_waitcnt vmcnt(0)\n\t"
        "v_mfma_f32_32x32x8_bf16 v[96:111], v[148:149], v[42:43], 0\n\t"
        "v_mfma_f32_32x32x16_bf16 v[96:111], v[144:147], v[36:39], v[96:111]\n\t"
        "s_nop 15\n\t"
        "s_mov_b32 exec_lo, 0xfff00000\n\t"
        "s_mov_b32 exec_hi, 0xfff00000\n\t"
        "v_mov_b64_e32 v[152:153], v[116:117]\n\t"
        "v_mov_b64_e32 v[154:155], v[116:117]\n\t"
        "v_mov_b64_e32 v[156:157], v[116:117]\n\t"
        "v_mov_b64_e32 v[158:159], v[116:117]\n\t"
        "s_mov_b64 exec, -1\n\t"
        "v_sub_f32_e32 v96, v96, v114\n\t"
        "v_sub_f32_e32 v97, v97, v114\n\t"
        "v_sub_f32_e32 v98, v98, v114\n\t"
        "v_sub_f32_e32 v99, v99, v114\n\t"
        "v_sub_f32_e32 v100, v100, v114\n\t"
        "v_sub_f32_e32 v101, v101, v114\n\t"
        "v_sub_f32_e32 v102, v102, v114\n\t"
        "v_sub_f32_e32 v103, v103, v114\n\t"
        "v_sub_f32_e32 v104, v104, v114\n\t"
        "v_sub_f32_e32 v105, v105, v114\n\t"
        "v_sub_f32_e32 v106, v106, v114\n\t"
        "v_sub_f32_e32 v107, v107, v114\n\t"
        "v_sub_f32_e32 v108, v108, v114\n\t"
        "v_sub_f32_e32 v109, v109, v114\n\t"
        "v_sub_f32_e32 v110, v110, v114\n\t"
        "v_sub_f32_e32 v111, v111, v114\n\t"
        "v_exp_f32_e32 v96, v96\n\t"
        "v_exp_f32_e32 v97, v97\n\t"
        "v_exp_f32_e32 v98, v98\n\t"
        "v_exp_f32_e32 v99, v99\n\t"
        "v_exp_f32_e32 v100, v100\n\t"
        "v_exp_f32_e32 v101, v101\n\t"
        "v_exp_f32_e32 v102, v102\n\t"
        "v_exp_f32_e32 v103, v103\n\t"
        "v_exp_f32_e32 v104, v104\n\t"
        "v_exp_f32_e32 v105, v105\n\t"
        "v_exp_f32_e32 v106, v106\n\t"
        "v_exp_f32_e32 v107, v107\n\t"
        "v_exp_f32_e32 v108, v108\n\t"
        "v_exp_f32_e32 v109, v109\n\t"
        "v_exp_f32_e32 v110, v110\n\t"
        "v_exp_f32_e32 v111, v111\n\t"
        "s_nop 1\n\t"
        "v_smfmac_f32_32x32x32_bf16 v[64:79], v[152:155], v[96:103], v44\n\t"
        "v_smfmac_f32_32x32x32_bf16 v[64:79], v[156:159], v[104:111], v44\n\t"
        "s_nop 7\n\t"
        "s_add_u32 s92, s92, 2560\n\t"
        "s_sub_u32 s94, s94, 1\n\t"
        "s_cbranch_scc0 .Lb2\n\t"
        "s_nop 15\n\t"
        "v_mov_b32_e32 v96, v72\n\t"
        "v_mov_b32_e32 v97, v72\n\t"
        "s_nop 1\n\t"
        "v_permlane32_swap_b32_e32 v96, v97\n\t"
        "s_branch .Lfast\n\t"
        ".Lend:\n\t"
        : "+{v[0:3]}"(cs[0]), "+{v[4:7]}"(cs[1]), "+{v[8:11]}"(cs[2]), "+{v[12:15]}"(cs[3]), "+{v[16:19]}"(cs[4]), "+{v[20:23]}"(cs[5]), "+{v150}"(l16), "+{v24}"(epoch),
          PK_REGS("+"), "+{v[192:195]}"(sa0), "+{v[196:199]}"(sa1), "+{v[200:203]}"(mk), "+{v[204:207]}"(mp),
          "+{s[56:59]}"(sv[0])
        : "{s[40:43]}"(srd(ws, W_END)), "{s[44:47]}"(srd(woA, 640 * 1280)), "{s[48:51]}"(srd(woB, 640 * 1280)), "{s[52:55]}"(srd(out, 2048 * 1280)),
          "{s[60:63]}"(sv[1]), "{s[64:67]}"(sv[2]), "{s[68:71]}"(sv[3]), "{s[72:75]}"(sv[4]), "{s[76:79]}"(sv[5]), "{s[80:83]}"(sv[6]), "{s[84:87]}"(sv[7]), "{s[88:91]}"(sv[8]) : "v25","v26","v27","v28","v29","v30","v31","v32","v33","v34","v35","v36","v37","v38","v39","v40","v41","v42","v43","v44","v45","v46","v47","v48","v49","v50","v51","v52","v53","v54","v55","v56","v57","v58","v59","v60","v61","v62","v63","v64","v65","v66","v67","v68","v69","v70","v71","v72","v73","v74","v75","v76","v77","v78","v79","v80","v81","v82","v83","v84","v85","v86","v87","v88","v89","v90","v91","v92","v93","v94","v95","v96","v97","v98","v99","v100","v101","v102","v103","v104","v105","v106","v107","v108","v109","v110","v111","v112","v113","v114","v115","v116","v117","v118","v119","v120","v121","v122","v123","v124","v125","v126","v127","v128","v129","v130","v131","v132","v133","v134","v135","v136","v137","v138","v139","v140","v141","v142","v143","v144","v145","v146","v147","v148","v149","v151","v152","v153","v154","v155","v156","v157","v158","v159","v208","v209","v210","v211","v212","v213","v214","v215","v216","v217","v218","v219","v220","v221","v222","v223","v224","v225","v226","v227","v228","v229","v230","v231","v232","v233","v234","v235","v236","v237","v238","v239","v240","v241","v242","v243","v244","v245","v246","v247","v248","v249","v250","v251","s92","s93","s94","s95","s96","s97","s98","s99", "vcc", "scc", "memory");
    asm volatile(".Lpc_end:");
}

extern "C" void kernel_launch(void* const* d_in, const int* in_sizes, int n_in, void* d_out, int out_size, void* d_ws, size_t ws_size, hipStream_t stream) {
    (void)hipFuncSetAttribute((const void*)k_fused, hipFuncAttributeMaxDynamicSharedMemorySize, LDS_BYTES);
    k_fused<<<256, 512, LDS_BYTES, stream>>>((const char*)d_in[0], (const char*)d_in[1], (const char*)d_in[4], (const char*)d_in[6],
                                             (const char*)d_in[2], (const char*)d_in[5], (char*)d_ws, (char*)d_out);
}
```
